```python
import torch

def _softmax(x, axis=-1):
    return torch.softmax(x, dim=axis)
def _gelu(x):
    return torch.nn.functional.gelu(x, approximate="tanh")
def _silu(x):
    return torch.nn.functional.silu(x)
def _sigmoid(x):
    return torch.sigmoid(x)
def _layernorm(x, axis=-1, eps=1e-5):
    m = x.mean(dim=axis, keepdim=True)
    v = x.var(dim=axis, keepdim=True, unbiased=False)
    return (x - m) / torch.sqrt(v + eps)
def _scatter_add(tgt, idx, src, axis=0):
    return tgt.clone().index_add_(axis, idx, src)
def _segment_sum(src, seg, n):
    out = torch.zeros((n,) + src.shape[1:], dtype=src.dtype)
    return out.index_add_(0, seg, src)
def _pad(x, pw):
    flat = tuple(v for pair in reversed(pw) for v in pair)
    return torch.nn.functional.pad(x, flat)

kernel_name = 'gated_mix__ssm_conv_s5__glu_gated_swiglu__prompt'

def setup_inputs():

    _w = (lambda a: (lambda t: t.to(torch.bfloat16) if t.is_floating_point() else t)(torch.as_tensor(a)))
    def _A_setup():
        B, S, D = 1, 2048, 640
        P = D
        Lambda_re = -0.5 * (lambda _x: torch.exp(torch.as_tensor(_x)))((lambda *s: torch.randn(*s, dtype=torch.bfloat16) * 0.2)(P))
        Lambda_im = (lambda *s: torch.randn(*s, dtype=torch.bfloat16) * 0.4)(P) * 1.0
        B_re = (lambda *s: torch.randn(*s, dtype=torch.bfloat16) * 0.2)(P, D) * (D ** -0.5)
        B_im = (lambda *s: torch.randn(*s, dtype=torch.bfloat16) * 0.2)(P, D) * (D ** -0.5)
        C_re = (lambda *s: torch.randn(*s, dtype=torch.bfloat16) * 0.2)(D, P) * (P ** -0.5)
        C_im = (lambda *s: torch.randn(*s, dtype=torch.bfloat16) * 0.2)(D, P) * (P ** -0.5)
        log_dt = (lambda *s: torch.randn(*s, dtype=torch.bfloat16) * 0.2)(P) * 0.3 - 2.0
        Dskip = (lambda *s: torch.randn(*s, dtype=torch.bfloat16) * 0.05)(D)
        return {
            "x": _w((lambda *s: torch.randn(*s, dtype=torch.bfloat16) * 0.05)(B, S, D)),
            "Lambda_re": _w(Lambda_re),
            "Lambda_im": _w(Lambda_im),
            "B_re": _w(B_re),
            "B_im": _w(B_im),
            "C_re": _w(C_re),
            "C_im": _w(C_im),
            "log_dt": _w(log_dt),
            "Dskip": _w(Dskip),
        }
    _A_in = _A_setup()

    def _B_setup():
        B, S, D, F = 1, 2048, 640, 1728
        return {
            "x": _w((lambda *s: torch.randn(*s, dtype=torch.bfloat16) * 0.05)(B, S, D)),
            "w1": _w((lambda *s: torch.randn(*s, dtype=torch.bfloat16) * 0.8)(D, F) * (2.0 / D) ** 0.5),
            "w3": _w((lambda *s: torch.randn(*s, dtype=torch.bfloat16) * 0.8)(D, F) * (2.0 / D) ** 0.5),
            "w2": _w((lambda *s: torch.randn(*s, dtype=torch.bfloat16) * 0.8)(F, D) * (2.0 / F) ** 0.5),
        }
    _B_in = _B_setup()

    return {
        "A_x": _A_in["x"],
        "A_Lambda_re": _A_in["Lambda_re"],
        "A_Lambda_im": _A_in["Lambda_im"],
        "A_B_re": _A_in["B_re"],
        "A_B_im": _A_in["B_im"],
        "A_C_re": _A_in["C_re"],
        "A_C_im": _A_in["C_im"],
        "A_log_dt": _A_in["log_dt"],
        "A_Dskip": _A_in["Dskip"],
        "B_x": _B_in["x"],
        "B_w1": _B_in["w1"],
        "B_w3": _B_in["w3"],
        "B_w2": _B_in["w2"],
        "gate_w": _w((lambda *s: torch.randn(*s, dtype=torch.bfloat16) * 0.05)(640) * 16.0),
    }

def reference(A_x, A_Lambda_re, A_Lambda_im, A_B_re, A_B_im, A_C_re, A_C_im, A_log_dt, A_Dskip, B_x, B_w1, B_w3, B_w2, gate_w):
    def _A_ref(x, Lambda_re, Lambda_im, B_re, B_im, C_re, C_im, log_dt, Dskip):
        Bsz, L, H = x.shape
        Lam = Lambda_re + 1j * Lambda_im
        Bmat = B_re + 1j * B_im
        Cmat = C_re + 1j * C_im
        dt = torch.exp(log_dt)
        Lam_bar = torch.exp(dt * Lam)
        B_bar = ((Lam_bar - 1.0) / Lam)[:, None] * Bmat
        Bu = torch.einsum("bld,pd->blp", x + 0j, B_bar)
        h = torch.zeros_like(Bu[:, 0, :])
        hs = []
        for t in range(L):
            h = Lam_bar[None, :] * h + Bu[:, t, :]
            hs.append(h)
        hseq = torch.stack(hs, 1)
        y = 2.0 * torch.einsum("blp,dp->bld", hseq, Cmat).real + x * Dskip[None, None, :]
        return y

    def _B_ref(x, w1, w3, w2):
        gate = torch.einsum("bsd,df->bsf", x, w1)
        up = torch.einsum("bsd,df->bsf", x, w3)
        h = _silu(gate) * up
        out = torch.einsum("bsf,fd->bsd", h, w2)
        return out

    x = A_x
    g = _sigmoid((x * gate_w).sum(-1, keepdims=True))
    a = _A_ref(x, A_Lambda_re, A_Lambda_im, A_B_re, A_B_im, A_C_re, A_C_im, A_log_dt, A_Dskip)
    b = _B_ref(x, B_w1, B_w3, B_w2)
    return g * a + (1 - g) * b
```

```cpp
#include <hip/hip_runtime.h>
#include <hip/hip_bf16.h>
#include <stdint.h>

typedef unsigned short u16;
__device__ __forceinline__ float bf2f(u16 v) { return __uint_as_float(((unsigned)v) << 16); }
__device__ __forceinline__ u16 f2bf(float x) { unsigned u = __float_as_uint(x); u += 0x7fffu + ((u >> 16) & 1u); return (u16)(u >> 16); }
__device__ __forceinline__ float rbf(float x) { return bf2f(f2bf(x)); }

constexpr int L = 2048, D = 640, P = 640, F = 1728;

__global__ void k_bu(const u16* x, const u16* Bre, const u16* Bim, float* bur, float* bui) {
  int p = blockIdx.x * 64 + (threadIdx.x & 63);
  int t = blockIdx.y * 4 + (threadIdx.x >> 6);
  float sr = 0.f, si = 0.f;
  for (int d = 0; d < D; ++d) { float xv = bf2f(x[t * D + d]); sr += xv * bf2f(Bre[p * D + d]); si += xv * bf2f(Bim[p * D + d]); }
  bur[t * P + p] = sr; bui[t * P + p] = si;
}
__global__ void k_scan(const u16* Lre, const u16* Lim, const u16* logdt, float* bur, float* bui) {
  int p = blockIdx.x * 64 + threadIdx.x;
  float lr = bf2f(Lre[p]), li = bf2f(Lim[p]);
  float dt = rbf(expf(bf2f(logdt[p])));
  float ar = dt * lr, ai = dt * li;
  float e = expf(ar);
  float br = e * cosf(ai), bi = e * sinf(ai);
  float nr = br - 1.f, ni = bi;
  float den = lr * lr + li * li;
  float cr = (nr * lr + ni * li) / den, ci = (ni * lr - nr * li) / den;
  float hr = 0.f, hi = 0.f;
  for (int t = 0; t < L; ++t) {
    float rr = bur[t * P + p], ri = bui[t * P + p];
    float ur = cr * rr - ci * ri, ui = cr * ri + ci * rr;
    float nhr = br * hr - bi * hi + ur, nhi = br * hi + bi * hr + ui;
    hr = nhr; hi = nhi;
    bur[t * P + p] = hr; bui[t * P + p] = hi;
  }
}
__global__ void k_ffn_up(const u16* x, const u16* w1, const u16* w3, u16* h) {
  int f = blockIdx.x * 64 + (threadIdx.x & 63);
  int t = blockIdx.y * 4 + (threadIdx.x >> 6);
  float g = 0.f, u = 0.f;
  for (int d = 0; d < D; ++d) { float xv = bf2f(x[t * D + d]); g += xv * bf2f(w1[d * F + f]); u += xv * bf2f(w3[d * F + f]); }
  g = rbf(g); u = rbf(u);
  float s = rbf(g / (1.f + expf(-g)));
  h[t * F + f] = f2bf(s * u);
}
__global__ void k_gate(const u16* x, const u16* gw, float* g) {
  int t = blockIdx.x * 64 + threadIdx.x;
  float s = 0.f;
  for (int d = 0; d < D; ++d) s += rbf(bf2f(x[t * D + d]) * bf2f(gw[d]));
  s = rbf(s);
  g[t] = rbf(1.f / (1.f + expf(-s)));
}
__global__ void k_final(const u16* x, const u16* Cre, const u16* Cim, const u16* Dsk, const u16* w2, const u16* h,
                        const float* hr, const float* hi, const float* g, float* out) {
  int d = blockIdx.x * 64 + (threadIdx.x & 63);
  int t = blockIdx.y * 4 + (threadIdx.x >> 6);
  float y = 0.f;
  for (int p = 0; p < P; ++p) y += hr[t * P + p] * bf2f(Cre[d * P + p]) - hi[t * P + p] * bf2f(Cim[d * P + p]);
  float a = 2.f * y + rbf(bf2f(x[t * D + d]) * bf2f(Dsk[d]));
  float b = 0.f;
  for (int f = 0; f < F; ++f) b += bf2f(h[t * F + f]) * bf2f(w2[f * D + d]);
  b = rbf(b);
  float gg = g[t];
  out[t * D + d] = gg * a + rbf(rbf(1.f - gg) * b);
}

extern "C" void kernel_launch(void* const* d_in, const int* in_sizes, int n_in,
                              void* d_out, int out_size, void* d_ws, size_t ws_size,
                              hipStream_t stream) {
  const u16* x = (const u16*)d_in[0];
  const u16* Lre = (const u16*)d_in[1];
  const u16* Lim = (const u16*)d_in[2];
  const u16* Bre = (const u16*)d_in[3];
  const u16* Bim = (const u16*)d_in[4];
  const u16* Cre = (const u16*)d_in[5];
  const u16* Cim = (const u16*)d_in[6];
  const u16* logdt = (const u16*)d_in[7];
  const u16* Dsk = (const u16*)d_in[8];
  const u16* w1 = (const u16*)d_in[10];
  const u16* w3 = (const u16*)d_in[11];
  const u16* w2 = (const u16*)d_in[12];
  const u16* gw = (const u16*)d_in[13];
  float* out = (float*)d_out;
  char* ws = (char*)d_ws;
  float* bur = (float*)ws; float* bui = bur + L * P; float* g = bui + L * P; u16* h = (u16*)(g + L);
  k_bu<<<dim3(P / 64, L / 4), 256, 0, stream>>>(x, Bre, Bim, bur, bui);
  k_scan<<<P / 64, 64, 0, stream>>>(Lre, Lim, logdt, bur, bui);
  k_ffn_up<<<dim3(F / 64, L / 4), 256, 0, stream>>>(x, w1, w3, h);
  k_gate<<<L / 64, 64, 0, stream>>>(x, gw, g);
  k_final<<<dim3(D / 64, L / 4), 256, 0, stream>>>(x, Cre, Cim, Dsk, w2, h, bur, bui, g, out);
}
```

```cpp
#include <hip/hip_runtime.h>
#include <stdint.h>

typedef unsigned short u16;
typedef unsigned long long u64;
typedef unsigned v2u __attribute__((ext_vector_type(2)));
typedef unsigned v4u __attribute__((ext_vector_type(4)));
typedef unsigned v8u __attribute__((ext_vector_type(8)));
typedef unsigned v16u __attribute__((ext_vector_type(16)));
typedef unsigned v32u __attribute__((ext_vector_type(32)));
typedef float f2 __attribute__((ext_vector_type(2)));
typedef float f4 __attribute__((ext_vector_type(4)));
typedef float f32v __attribute__((ext_vector_type(32)));
typedef __bf16 bf2 __attribute__((ext_vector_type(2)));
#define DI __device__ __forceinline__
#define LAS __attribute__((address_space(3)))
#define GAS __attribute__((address_space(1)))

#define P1_SSM_START \
  "s_nop 4\n\t" \
  "s_load_dwordx4 s[56:59], %[ka], 8\n\t" \
  "s_load_dwordx2 s[60:61], %[ka], 56\n\t" \
  "s_load_dwordx2 s[36:37], %[ka], 0\n\t" \
  "s_load_dwordx4 s[24:27], %[ka], 24\n\t" \
  "s_load_dwordx2 s[40:41], %[ka], 112\n\t" \
  "s_getpc_b64 s[30:31]\n\t" \
  "MIXK_PC_%=:\n\t" \
  "s_sub_u32 s30, s30, MIXK_PC_%=-mixk_entry\n\t" \
  "s_subb_u32 s31, s31, 0\n\t" \
  "s_lshr_b32 s16, %[b], 6\n\t" \
  "s_mov_b32 s4, %[w]\n\t" \
  "s_and_b32 s6, %[b], 4\n\t" \
  "s_add_u32 s6, s6, s16\n\t" \
  "s_bfe_u32 s7, %[b], 0x30003\n\t" \
  "v_mbcnt_lo_u32_b32 v1, -1, 0\n\t" \
  "v_mbcnt_hi_u32_b32 v1, -1, v1\n\t" \
  "s_add_u32 s90, s7, s16\n\t" \
  "s_and_b32 s90, s90, 1\n\t" \
  "s_mul_i32 s90, s90, 4\n\t" \
  "s_mul_i32 s42, s6, 327680\n\t" \
  "s_mul_i32 s16, s4, 81920\n\t" \
  "s_add_u32 s42, s42, s16\n\t" \
  "s_lshl_b32 s88, s4, 13\n\t" \
  "s_add_u32 s88, s88, %[lb]\n\t" \
  "s_lshr_b32 s5, s4, 1\n\t" \
  "v_lshrrev_b32 v8, 3, v1\n\t" \
  "v_lshrrev_b32 v9, 4, v1\n\t" \
  "v_xor_b32 v9, v9, v1\n\t" \
  "v_and_b32 v9, 7, v9\n\t" \
  "v_lshlrev_b32 v9, 4, v9\n\t" \
  "v_xor_b32 v10, 64, v9\n\t" \
  "v_and_b32 v11, 4, v8\n\t" \
  "v_lshlrev_b32 v11, 2, v11\n\t" \
  "v_and_b32 v12, 1, v8\n\t" \
  "v_lshl_add_u32 v11, v12, 3, v11\n\t" \
  "v_bfe_u32 v12, v8, 1, 1\n\t" \
  "v_add_u32 v11, v11, v12\n\t" \
  "v_mul_u32_u24 v11, 1280, v11\n\t" \
  "v_add_u32 v240, v11, v9\n\t" \
  "v_add_u32 v12, v11, v10\n\t" \
  "v_add_u32 v241, 39936, v12\n\t" \
  "v_add_u32 v242, 512, v240\n\t" \
  "v_add_u32 v243, 40448, v12\n\t" \
  "v_add_u32 v244, 5120, v240\n\t" \
  "v_add_u32 v245, 45056, v12\n\t" \
  "v_add_u32 v246, 5632, v240\n\t" \
  "v_add_u32 v247, 45568, v12\n\t" \
  "s_bfe_u32 s16, %[b], 0x10001\n\t" \
  "s_mul_i32 s8, s16, 5\n\t" \
  "s_and_b32 s16, %[b], 1\n\t" \
  "s_mul_i32 s16, s16, 3\n\t" \
  "s_add_u32 s8, s8, s16\n\t" \
  "s_add_u32 s8, s8, s7\n\t" \
  "s_lshl_b32 s16, s8, 6\n\t" \
  "v_add_u32 v15, s16, v1\n\t" \
  "v_lshlrev_b32 v15, 1, v15\n\t" \
  "s_waitcnt lgkmcnt(0)\n\t" \
  "s_mov_b64 s[44:45], s[36:37]\n\t" \
  "s_mov_b32 s46, 2621440\n\t" \
  "s_mov_b32 s47, 0x20000\n\t" \
  "s_mov_b32 s54, 0\n\t" \
  "s_lshl_b32 s52, s54, 7\n\t" \
  "s_add_u32 s52, s52, s42\n\t" \
  "global_load_ushort v16, v15, s[56:57]\n\t" \
  "global_load_ushort v17, v15, s[58:59]\n\t" \
  "global_load_ushort v18, v15, s[60:61]\n\t" \
  "s_add_u32 m0, s88, 0\n\t" \
  "s_nop 0\n\t" \
  "buffer_load_dwordx4 v240, s[44:47], s52 offen offset:0 lds\n\t" \
  "buffer_load_dwordx4 v241, s[44:47], s52 offen offset:1024 lds\n\t" \
  "buffer_load_dwordx4 v242, s[44:47], s52 offen offset:2048 lds\n\t" \
  "buffer_load_dwordx4 v243, s[44:47], s52 offen offset:3072 lds\n\t" \
  "s_add_u32 m0, s88, 4096\n\t" \
  "s_nop 0\n\t" \
  "buffer_load_dwordx4 v244, s[44:47], s52 offen offset:0 lds\n\t" \
  "buffer_load_dwordx4 v245, s[44:47], s52 offen offset:1024 lds\n\t" \
  "buffer_load_dwordx4 v246, s[44:47], s52 offen offset:2048 lds\n\t" \
  "buffer_load_dwordx4 v247, s[44:47], s52 offen offset:3072 lds\n\t" \
  "s_cmp_lg_u32 s5, 0\n\t" \
  "s_cselect_b64 s[38:39], s[26:27], s[24:25]\n\t" \
  "s_and_b32 s16, s4, 1\n\t" \
  "s_lshl_b32 s43, s8, 5\n\t" \
  "s_add_u32 s43, s43, s16\n\t" \
  "s_mul_i32 s43, s43, 2560\n\t" \
  "s_lshl_b32 s89, s4, 12\n\t" \
  "v_mul_u32_u24 v11, 5120, v8\n\t" \
  "v_add_u32 v248, v11, v9\n\t" \
  "v_add_u32 v11, v11, v10\n\t" \
  "v_add_u32 v249, 40960, v11\n\t" \
  "v_add_u32 v250, 1280, v248\n\t" \
  "v_add_u32 v251, 1280, v249\n\t" \
  "s_add_u32 s89, s89, %[lb]\n\t" \
  "s_add_u32 s89, s89, 32768\n\t" \
  "s_and_b32 s16, %[b], 3\n\t" \
  "s_lshl_b32 s16, s16, 3\n\t" \
  "s_add_u32 s16, s16, s7\n\t" \
  "s_mul_i32 s16, s16, 2560\n\t" \
  "s_add_u32 s16, s16, s42\n\t" \
  "v_min_u32 v13, 19, v1\n\t" \
  "v_lshlrev_b32 v13, 7, v13\n\t" \
  "v_add_u32 v13, s16, v13\n\t" \
  "s_lshr_b32 s16, %[b], 3\n\t" \
  "s_lshl_b32 s16, s16, 2\n\t" \
  "s_add_u32 s16, s16, s4\n\t" \
  "s_lshl_b32 s16, s16, 9\n\t" \
  "v_min_u32 v14, 3, v1\n\t" \
  "v_lshlrev_b32 v14, 7, v14\n\t" \
  "v_add_u32 v14, s16, v14\n\t" \
  "v_min_u32 v14, %[cb], v14\n\t" \
  "s_mov_b64 s[48:49], s[38:39]\n\t" \
  "s_mov_b32 s50, 819200\n\t" \
  "s_mov_b32 s51, 0x20000\n\t" \
  "s_lshl_b32 s53, s54, 7\n\t" \
  "s_add_u32 s53, s53, s43\n\t" \
  "s_add_u32 m0, s89, 0\n\t" \
  "s_nop 0\n\t" \
  "buffer_load_dwordx4 v248, s[48:51], s53 offen offset:0 lds\n\t" \
  "s_add_u32 m0, s89, 1024\n\t" \
  "s_nop 0\n\t" \
  "buffer_load_dwordx4 v249, s[48:51], s53 offen offset:0 lds\n\t" \
  "s_add_u32 m0, s89, 2048\n\t" \
  "s_nop 0\n\t" \
  "buffer_load_dwordx4 v250, s[48:51], s53 offen offset:0 lds\n\t" \
  "s_add_u32 m0, s89, 3072\n\t" \
  "s_nop 0\n\t" \
  "buffer_load_dwordx4 v251, s[48:51], s53 offen offset:0 lds\n\t" \
  "global_load_dword a242, v13, s[36:37]\n\t" \
  "global_load_dword a243, v14, s[30:31]\n\t" \
  "s_load_dwordx4 s[24:27], %[ka], 40\n\t" \
  "s_load_dwordx2 s[60:61], %[ka], 88\n\t" \
  "s_load_dwordx2 s[92:93], %[ka], 96\n\t" \
  "s_load_dwordx2 s[94:95], %[ka], 64\n\t" \
  "s_load_dwordx2 s[28:29], %[ka], 104\n\t" \
  "s_lshl_b32 s23, %[b], 2\n\t" \
  "s_add_u32 s23, s23, s4\n\t" \
  "s_cmp_ge_u32 s23, 940\n\t" \
  "s_cselect_b32 s16, 940, 0\n\t" \
  "s_sub_u32 s23, s23, s16\n\t" \
  "s_cmp_ge_u32 s23, 540\n\t" \
  "s_cselect_b32 s20, 1, 0\n\t" \
  "s_cselect_b32 s16, 540, 0\n\t" \
  "s_mov_b32 s17, 1214\n\t" \
  "s_cmov_b32 s17, 1639\n\t" \
  "s_cselect_b32 s18, 40, 54\n\t" \
  "s_mov_b32 s19, 40960\n\t" \
  "s_cmov_b32 s19, 20480\n\t" \
  "s_mov_b32 s78, 2211840\n\t" \
  "s_cmov_b32 s78, 819200\n\t" \
  "s_cselect_b32 s85, 0, 20480\n\t" \
  "s_mov_b32 s69, 1024\n\t" \
  "s_cmov_b32 s69, 40960\n\t" \
  "s_sub_u32 s16, s23, s16\n\t" \
  "s_mul_i32 s21, s16, s17\n\t" \
  "s_lshr_b32 s21, s21, 16\n\t" \
  "s_mul_i32 s17, s21, s18\n\t" \
  "s_sub_u32 s22, s16, s17\n\t" \
  "s_mul_i32 s84, s22, s19\n\t" \
  "s_lshl_b32 s16, s21, 7\n\t" \
  "s_add_u32 s84, s84, s16\n\t" \
  "s_add_u32 s85, s85, s84\n\t" \
  "s_mov_b32 s82, s78\n\t" \
  "s_mov_b32 s79, 0x20000\n\t" \
  "s_mov_b32 s83, 0x20000\n\t" \
  "v_mul_u32_u24 v3, 1280, v8\n\t" \
  "v_and_b32 v11, 7, v1\n\t" \
  "v_lshl_add_u32 v3, v11, 4, v3\n\t" \
  "s_cmp_lg_u32 s20, 0\n\t" \
  "s_waitcnt lgkmcnt(0)\n\t" \
  "s_cselect_b64 s[76:77], s[24:25], s[60:61]\n\t" \
  "s_cselect_b64 s[80:81], s[26:27], s[60:61]\n\t" \
  "s_add_u32 m0, s88, 106496\n\t" \
  "s_add_u32 s55, s84, 9216\n\t" \
  "s_add_u32 s56, s85, 9216\n\t" \
  "s_mov_b32 s54, 1\n\t" \
  "s_lshl_b32 s52, s54, 7\n\t" \
  "s_add_u32 s52, s52, s42\n\t" \
  "s_lshl_b32 s53, s54, 7\n\t" \
  "s_add_u32 s53, s53, s43\n\t" \
  "s_add_u32 m0, s88, 53248\n\t" \
  "s_nop 0\n\t" \
  "buffer_load_dwordx4 v240, s[44:47], s52 offen offset:0 lds\n\t" \
  "v_and_b32 v11, 15, v1\n\t" \
  "v_lshrrev_b32 v12, 4, v1\n\t" \
  "v_lshrrev_b32 v13, 1, v11\n\t" \
  "v_xor_b32 v13, v12, v13\n\t" \
  "v_lshlrev_b32 v13, 4, v13\n\t" \
  "v_lshl_add_u32 v13, v11, 7, v13\n\t" \
  "v_xor_b32 v14, 64, v13\n\t" \
  "v_add_u32 v254, s88, v13\n\t" \
  "v_add_u32 v255, s88, v14\n\t" \
  "s_add_u32 s16, %[lb], 32768\n\t" \
  "v_add_u32 v252, s16, v13\n\t" \
  "v_add_u32 v253, s16, v14\n\t" \
  "v_lshlrev_b32 v4, 4, v1\n\t" \
  "v_mov_b32 v7, 0\n\t" \
  "v_mov_b32 v2, 0\n\t" \
  "v_lshlrev_b32 v13, 7, v11\n\t" \
  "v_lshl_add_u32 v13, v12, 3, v13\n\t" \
  "v_lshrrev_b32 v14, 2, v11\n\t" \
  "v_lshl_add_u32 v14, v12, 3, v14\n\t" \
  "buffer_load_dwordx4 v241, s[44:47], s52 offen offset:1024 lds\n\t" \
  "v_lshlrev_b32 v14, 7, v14\n\t" \
  "v_and_b32 v15, 3, v1\n\t" \
  "v_lshl_add_u32 v14, v15, 3, v14\n\t" \
  "s_cmp_lg_u32 s20, 0\n\t" \
  "s_cselect_b64 vcc, -1, 0\n\t" \
  "v_cndmask_b32 v5, v14, v13, vcc\n\t" \
  "v_add_u32 v5, s88, v5\n\t" \
  "s_lshl_b32 s9, s6, 2\n\t" \
  "s_add_u32 s9, s9, s4\n\t" \
  "s_and_b32 s10, %[b], 6\n\t" \
  "s_lshl_b32 s10, s10, 2\n\t" \
  "s_lshr_b32 s16, %[b], 5\n\t" \
  "s_add_u32 s10, s10, s16\n\t" \
  "s_and_b32 s11, %[b], 1\n\t" \
  "s_lshl_b32 s11, s11, 2\n\t" \
  "s_and_b32 s16, s7, 3\n\t" \
  "s_add_u32 s11, s11, s16\n\t" \
  "s_lshl_b32 s16, s4, 3\n\t" \
  "s_lshr_b32 s12, 0x2b201000, s16\n\t" \
  "buffer_load_dwordx4 v242, s[44:47], s52 offen offset:2048 lds\n\t" \
  "s_and_b32 s12, s12, 0xff\n\t" \
  "s_lshr_b32 s13, 0x07040200, s16\n\t" \
  "s_and_b32 s13, s13, 0xff\n\t" \
  "s_mul_i32 s16, s11, 5120\n\t" \
  "s_add_u32 s16, s16, 12320768\n\t" \
  "s_mul_i32 s14, s12, 40960\n\t" \
  "s_add_u32 s14, s14, s16\n\t" \
  "s_mul_i32 s15, s13, 163840\n\t" \
  "s_add_u32 s15, s15, s16\n\t" \
  "s_add_u32 s15, s15, 2211840\n\t" \
  "s_add_u32 s72, s14, 163840\n\t" \
  "s_add_u32 s16, s14, 491520\n\t" \
  "s_cmp_lg_u32 s5, 0\n\t" \
  "s_cselect_b32 s73, s15, s16\n\t" \
  "s_mov_b32 s16, 450560\n\t" \
  "s_cmov_b32 s16, 409600\n\t" \
  "s_add_u32 s96, s14, s16\n\t" \
  "s_mul_i32 s16, s4, 20480\n\t" \
  "s_add_u32 s16, s16, %[lb]\n\t" \
  "buffer_load_dwordx4 v243, s[44:47], s52 offen offset:3072 lds\n\t" \
  "s_add_u32 s91, s16, 61440\n\t" \
  "s_cmp_eq_u32 s4, 3\n\t" \
  "s_cselect_b32 s17, 81920, 0\n\t" \
  "s_add_u32 s16, s16, s17\n\t" \
  "v_writelane_b32 v2, s16, 1\n\t" \
  "v_writelane_b32 v2, s4, 0\n\t" \
  "v_writelane_b32 v2, s91, 17\n\t" \
  "v_writelane_b32 v2, s72, 14\n\t" \
  "v_writelane_b32 v2, s73, 15\n\t" \
  "v_writelane_b32 v2, s96, 16\n\t" \
  "v_writelane_b32 v2, s6, 25\n\t" \
  "v_writelane_b32 v2, %[lb], 29\n\t" \
  "v_writelane_b32 v2, 1, 2\n\t" \
  "s_mul_i32 s16, s5, 163840\n\t" \
  "s_add_u32 s16, s16, s15\n\t" \
  "v_writelane_b32 v2, s16, 3\n\t" \
  "s_add_u32 s16, s16, 163840\n\t" \
  "v_writelane_b32 v2, s16, 4\n\t" \
  "s_lshl_b32 s16, s21, 2\n\t" \
  "s_add_u32 m0, s88, 57344\n\t" \
  "s_nop 0\n\t" \
  "buffer_load_dwordx4 v244, s[44:47], s52 offen offset:0 lds\n\t" \
  "s_mul_i32 s17, s22, 40\n\t" \
  "s_add_u32 s17, s17, s16\n\t" \
  "s_add_u32 s16, s16, 54\n\t" \
  "s_mul_i32 s16, s16, 40\n\t" \
  "s_add_u32 s16, s16, s22\n\t" \
  "s_cmp_lg_u32 s20, 0\n\t" \
  "s_cselect_b32 s16, s16, s17\n\t" \
  "s_lshl_b32 s16, s16, 10\n\t" \
  "s_add_u32 s68, s16, 12320768\n\t" \
  "s_mov_b32 s70, s20\n\t" \
  "s_lshl_b32 s71, %[b], 2\n\t" \
  "s_add_u32 s71, s71, s4\n\t" \
  "s_lshl_b32 s71, s71, 7\n\t" \
  "s_add_u32 s71, s71, 16302080\n\t" \
  "s_mul_i32 s18, s10, 94\n\t" \
  "s_add_u32 s16, s18, s12\n\t" \
  "s_lshl_b32 s16, s16, 12\n\t" \
  "v_writelane_b32 v2, s16, 5\n\t" \
  "s_lshl_b32 s17, s13, 2\n\t" \
  "buffer_load_dwordx4 v245, s[44:47], s52 offen offset:1024 lds\n\t" \
  "s_add_u32 s17, s17, s18\n\t" \
  "s_add_u32 s17, s17, 54\n\t" \
  "s_lshl_b32 s17, s17, 12\n\t" \
  "s_add_u32 s16, s16, 49152\n\t" \
  "s_cmp_lg_u32 s5, 0\n\t" \
  "s_cselect_b32 s16, s17, s16\n\t" \
  "v_writelane_b32 v2, s16, 6\n\t" \
  "s_lshl_b32 s16, s5, 14\n\t" \
  "s_add_u32 s16, s16, s17\n\t" \
  "v_writelane_b32 v2, s16, 7\n\t" \
  "s_lshl_b32 s16, s9, 5\n\t" \
  "s_add_u32 s16, s16, s8\n\t" \
  "s_lshl_b32 s16, s16, 7\n\t" \
  "s_add_u32 s16, s16, 16173824\n\t" \
  "v_writelane_b32 v2, s16, 8\n\t" \
  "s_lshl_b32 s19, s4, 3\n\t" \
  "s_lshr_b32 s16, 0x04050606, s19\n\t" \
  "s_and_b32 s16, s16, 0xff\n\t" \
  "v_writelane_b32 v2, s16, 9\n\t" \
  "s_add_u32 m0, s88, 106496\n\t" \
  "s_nop 0\n\t" \
  "buffer_load_dwordx4 v3, s[76:79], s84 offen offset:0 lds\n\t" \
  "s_lshr_b32 s16, 0x110c0600, s19\n\t" \
  "s_and_b32 s16, s16, 0xff\n\t" \
  "s_lshl_b32 s18, s10, 5\n\t" \
  "s_add_u32 s16, s16, s18\n\t" \
  "s_lshl_b32 s16, s16, 7\n\t" \
  "s_add_u32 s16, s16, 16171008\n\t" \
  "v_writelane_b32 v2, s16, 10\n\t" \
  "s_add_u32 s16, s5, 1\n\t" \
  "v_writelane_b32 v2, s16, 11\n\t" \
  "s_add_u32 s16, s18, s13\n\t" \
  "s_lshl_b32 s16, s16, 7\n\t" \
  "s_add_u32 s16, s16, 16173824\n\t" \
  "v_writelane_b32 v2, s16, 12\n\t" \
  "s_lshl_b32 s16, s10, 9\n\t" \
  "s_add_u32 s16, s16, 16515072\n\t" \
  "v_writelane_b32 v2, s16, 13\n\t" \
  "s_mul_i32 s18, s11, 160\n\t" \
  "s_add_u32 s16, s94, s18\n\t" \
  "s_addc_u32 s17, s95, 0\n\t" \
  "buffer_load_dwordx4 v3, s[76:79], s55 offen offset:1024 lds\n\t" \
  "v_writelane_b32 v2, s16, 18\n\t" \
  "v_writelane_b32 v2, s17, 19\n\t" \
  "s_lshl_b32 s19, s10, 2\n\t" \
  "s_add_u32 s19, s19, s4\n\t" \
  "s_mul_i32 s19, s19, 20480\n\t" \
  "s_add_u32 s19, s19, s18\n\t" \
  "s_add_u32 s86, s36, s19\n\t" \
  "s_addc_u32 s87, s37, 0\n\t" \
  "s_lshl_b32 s19, s19, 1\n\t" \
  "s_add_u32 s16, s28, s19\n\t" \
  "s_addc_u32 s17, s29, 0\n\t" \
  "v_writelane_b32 v2, s16, 20\n\t" \
  "v_writelane_b32 v2, s17, 21\n\t" \
  "s_lshl_b32 s16, s8, 10\n\t" \
  "s_add_u32 s16, s16, 16433152\n\t" \
  "v_writelane_b32 v2, s16, 27\n\t" \
  "v_writelane_b32 v2, %[b], 23\n\t" \
  "s_mul_i32 s17, s6, 10240\n\t" \
  "s_add_u32 s16, s16, s17\n\t" \
  "s_add_u32 m0, m0, 2048\n\t" \
  "s_nop 0\n\t" \
  "buffer_load_dwordx4 v3, s[80:83], s85 offen offset:0 lds\n\t" \
  "v_writelane_b32 v2, s16, 26\n\t" \
  "s_mul_i32 s16, s9, 94\n\t" \
  "s_lshl_b32 s17, s8, 2\n\t" \
  "s_add_u32 s16, s16, s17\n\t" \
  "s_add_u32 s16, s16, 54\n\t" \
  "s_lshl_b32 s16, s16, 12\n\t" \
  "v_writelane_b32 v2, s16, 28\n\t" \
  "s_and_b32 s16, s10, 3\n\t" \
  "v_writelane_b32 v2, s16, 30\n\t" \
  "s_mul_i32 s16, s11, 5\n\t" \
  "s_lshr_b32 s17, s16, 2\n\t" \
  "s_mul_i32 s18, s5, 5\n\t" \
  "s_sub_u32 s18, 15, s18\n\t" \
  "v_min_u32 v11, 46, v1\n\t" \
  "v_and_b32 v12, 15, v11\n\t" \
  "v_min_u32 v12, s18, v12\n\t" \
  "v_lshrrev_b32 v13, 4, v11\n\t" \
  "v_add_u32 v13, s17, v13\n\t" \
  "v_mul_u32_u24 v13, 54, v13\n\t" \
  "buffer_load_dwordx4 v3, s[80:83], s56 offen offset:1024 lds\n\t" \
  "v_add3_u32 v12, v12, v13, s12\n\t" \
  "v_subrev_u32 v13, 32, v11\n\t" \
  "v_mul_u32_u24 v14, 52, v13\n\t" \
  "v_lshrrev_b32 v14, 8, v14\n\t" \
  "v_mul_u32_u24 v15, 5, v14\n\t" \
  "v_sub_u32 v13, v13, v15\n\t" \
  "s_add_u32 s18, s5, 1\n\t" \
  "v_min_u32 v14, s18, v14\n\t" \
  "v_add_u32 v14, s13, v14\n\t" \
  "v_mul_u32_u24 v14, 40, v14\n\t" \
  "s_add_u32 s16, s16, 540\n\t" \
  "v_add3_u32 v13, v13, v14, s16\n\t" \
  "v_cmp_gt_u32 vcc, 32, v11\n\t" \
  "s_nop 1\n\t" \
  "v_cndmask_b32 v6, v13, v12, vcc\n\t" \
  "v_lshlrev_b32 v6, 7, v6\n\t" \
  "v_add_u32 v6, 16302080, v6\n\t" \
  "s_mov_b64 s[64:65], s[40:41]\n\t" \
  "s_mov_b32 s66, 16531712\n\t" \
  "s_mov_b32 s67, 0x20000\n\t" \
  "s_mov_b32 s75, 0\n\t" \
  "s_waitcnt vmcnt(24)\n\t" \
  "s_nop 0\n\t"
#define P1_FFN \
  "s_nop 4\n\t" \
  "s_load_dwordx2 s[36:37], %[ka], 0\n\t" \
  "s_load_dwordx4 s[24:27], %[ka], 72\n\t" \
  "s_load_dwordx2 s[40:41], %[ka], 112\n\t" \
  "s_getpc_b64 s[30:31]\n\t" \
  "MIXK_PC_%=:\n\t" \
  "s_sub_u32 s30, s30, MIXK_PC_%=-mixk_entry\n\t" \
  "s_subb_u32 s31, s31, 0\n\t" \
  "s_lshr_b32 s16, %[b], 6\n\t" \
  "s_mov_b32 s4, %[w]\n\t" \
  "s_and_b32 s6, %[b], 4\n\t" \
  "s_add_u32 s6, s6, s16\n\t" \
  "s_bfe_u32 s7, %[b], 0x30003\n\t" \
  "v_mbcnt_lo_u32_b32 v1, -1, 0\n\t" \
  "v_mbcnt_hi_u32_b32 v1, -1, v1\n\t" \
  "s_add_u32 s90, s7, s16\n\t" \
  "s_and_b32 s90, s90, 1\n\t" \
  "s_mul_i32 s90, s90, 4\n\t" \
  "s_mul_i32 s42, s6, 327680\n\t" \
  "s_mul_i32 s16, s4, 81920\n\t" \
  "s_add_u32 s42, s42, s16\n\t" \
  "s_lshl_b32 s88, s4, 13\n\t" \
  "s_add_u32 s88, s88, %[lb]\n\t" \
  "s_lshr_b32 s5, s4, 1\n\t" \
  "v_lshrrev_b32 v8, 3, v1\n\t" \
  "v_lshrrev_b32 v9, 4, v1\n\t" \
  "v_xor_b32 v9, v9, v1\n\t" \
  "v_and_b32 v9, 7, v9\n\t" \
  "v_lshlrev_b32 v9, 4, v9\n\t" \
  "v_xor_b32 v10, 64, v9\n\t" \
  "v_mul_u32_u24 v11, 1280, v8\n\t" \
  "v_add_u32 v240, v11, v9\n\t" \
  "v_add_u32 v12, v11, v10\n\t" \
  "v_add_u32 v241, 9216, v12\n\t" \
  "v_add_u32 v242, 18432, v240\n\t" \
  "v_add_u32 v243, 27648, v12\n\t" \
  "v_add_u32 v244, 40960, v240\n\t" \
  "v_add_u32 v245, 50176, v12\n\t" \
  "v_add_u32 v246, 59392, v240\n\t" \
  "v_add_u32 v247, 68608, v12\n\t" \
  "s_bfe_u32 s16, %[b], 0x10001\n\t" \
  "s_mul_i32 s8, s16, 11\n\t" \
  "s_and_b32 s16, %[b], 1\n\t" \
  "s_mul_i32 s16, s16, 6\n\t" \
  "s_add_u32 s8, s8, s16\n\t" \
  "s_add_u32 s8, s8, s7\n\t" \
  "s_sub_u32 s8, s8, 3\n\t" \
  "s_waitcnt lgkmcnt(0)\n\t" \
  "s_mov_b64 s[44:45], s[36:37]\n\t" \
  "s_mov_b32 s46, 2621440\n\t" \
  "s_mov_b32 s47, 0x20000\n\t" \
  "s_mov_b32 s54, 0\n\t" \
  "s_lshl_b32 s52, s54, 7\n\t" \
  "s_add_u32 s52, s52, s42\n\t" \
  "s_add_u32 m0, s88, 0\n\t" \
  "s_nop 0\n\t" \
  "buffer_load_dwordx4 v240, s[44:47], s52 offen offset:0 lds\n\t" \
  "buffer_load_dwordx4 v241, s[44:47], s52 offen offset:1024 lds\n\t" \
  "buffer_load_dwordx4 v242, s[44:47], s52 offen offset:2048 lds\n\t" \
  "buffer_load_dwordx4 v243, s[44:47], s52 offen offset:3072 lds\n\t" \
  "s_add_u32 m0, s88, 4096\n\t" \
  "s_nop 0\n\t" \
  "buffer_load_dwordx4 v244, s[44:47], s52 offen offset:0 lds\n\t" \
  "buffer_load_dwordx4 v245, s[44:47], s52 offen offset:1024 lds\n\t" \
  "buffer_load_dwordx4 v246, s[44:47], s52 offen offset:2048 lds\n\t" \
  "buffer_load_dwordx4 v247, s[44:47], s52 offen offset:3072 lds\n\t" \
  "s_cmp_lg_u32 s5, 0\n\t" \
  "s_cselect_b64 s[38:39], s[26:27], s[24:25]\n\t" \
  "s_and_b32 s16, s4, 1\n\t" \
  "s_mul_i32 s97, s8, 10\n\t" \
  "s_min_u32 s97, s97, 206\n\t" \
  "s_lshl_b32 s43, s97, 4\n\t" \
  "s_mul_i32 s16, s16, 110592\n\t" \
  "s_add_u32 s43, s43, s16\n\t" \
  "s_mul_i32 s89, s4, 5120\n\t" \
  "v_mul_u32_u24 v11, 26, v1\n\t" \
  "v_lshrrev_b32 v11, 8, v11\n\t" \
  "v_mul_u32_u24 v12, 10, v11\n\t" \
  "v_sub_u32 v12, v1, v12\n\t" \
  "v_and_b32 v13, 19, v11\n\t" \
  "v_and_b32 v14, 4, v11\n\t" \
  "v_lshl_add_u32 v13, v14, 1, v13\n\t" \
  "v_bfe_u32 v14, v11, 3, 1\n\t" \
  "v_lshl_add_u32 v13, v14, 2, v13\n\t" \
  "v_mul_u32_u24 v13, 3456, v13\n\t" \
  "v_lshl_add_u32 v248, v12, 4, v13\n\t" \
  "v_add_u32 v13, 10, v12\n\t" \
  "v_lshrrev_b32 v13, 4, v13\n\t" \
  "v_add3_u32 v11, v11, v13, 6\n\t" \
  "v_mul_u32_u24 v13, 10, v13\n\t" \
  "v_sub_u32 v12, v12, v13\n\t" \
  "v_add_u32 v12, 4, v12\n\t" \
  "v_and_b32 v13, 19, v11\n\t" \
  "v_and_b32 v14, 4, v11\n\t" \
  "v_lshl_add_u32 v13, v14, 1, v13\n\t" \
  "v_bfe_u32 v14, v11, 3, 1\n\t" \
  "v_lshl_add_u32 v13, v14, 2, v13\n\t" \
  "v_mul_u32_u24 v13, 3456, v13\n\t" \
  "v_lshl_add_u32 v249, v12, 4, v13\n\t" \
  "v_subrev_u32 v249, 1024, v249\n\t" \
  "v_add_u32 v13, 10, v12\n\t" \
  "v_lshrrev_b32 v13, 4, v13\n\t" \
  "v_add3_u32 v11, v11, v13, 6\n\t" \
  "v_mul_u32_u24 v13, 10, v13\n\t" \
  "v_sub_u32 v12, v12, v13\n\t" \
  "v_add_u32 v12, 4, v12\n\t" \
  "v_and_b32 v13, 19, v11\n\t" \
  "v_and_b32 v14, 4, v11\n\t" \
  "v_lshl_add_u32 v13, v14, 1, v13\n\t" \
  "v_bfe_u32 v14, v11, 3, 1\n\t" \
  "v_lshl_add_u32 v13, v14, 2, v13\n\t" \
  "v_mul_u32_u24 v13, 3456, v13\n\t" \
  "v_lshl_add_u32 v250, v12, 4, v13\n\t" \
  "v_subrev_u32 v250, 2048, v250\n\t" \
  "v_add_u32 v13, 10, v12\n\t" \
  "v_lshrrev_b32 v13, 4, v13\n\t" \
  "v_add3_u32 v11, v11, v13, 6\n\t" \
  "v_mul_u32_u24 v13, 10, v13\n\t" \
  "v_sub_u32 v12, v12, v13\n\t" \
  "v_add_u32 v12, 4, v12\n\t" \
  "v_and_b32 v13, 19, v11\n\t" \
  "v_and_b32 v14, 4, v11\n\t" \
  "v_lshl_add_u32 v13, v14, 1, v13\n\t" \
  "v_bfe_u32 v14, v11, 3, 1\n\t" \
  "v_lshl_add_u32 v13, v14, 2, v13\n\t" \
  "v_mul_u32_u24 v13, 3456, v13\n\t" \
  "v_lshl_add_u32 v251, v12, 4, v13\n\t" \
  "v_subrev_u32 v251, 3072, v251\n\t" \
  "v_add_u32 v13, 10, v12\n\t" \
  "v_lshrrev_b32 v13, 4, v13\n\t" \
  "v_add3_u32 v11, v11, v13, 6\n\t" \
  "v_mul_u32_u24 v13, 10, v13\n\t" \
  "v_sub_u32 v12, v12, v13\n\t" \
  "v_add_u32 v12, 4, v12\n\t" \
  "v_and_b32 v13, 19, v11\n\t" \
  "v_and_b32 v14, 4, v11\n\t" \
  "v_lshl_add_u32 v13, v14, 1, v13\n\t" \
  "v_bfe_u32 v14, v11, 3, 1\n\t" \
  "v_lshl_add_u32 v13, v14, 2, v13\n\t" \
  "v_mul_u32_u24 v13, 3456, v13\n\t" \
  "v_lshl_add_u32 v252, v12, 4, v13\n\t" \
  "s_add_u32 s89, s89, %[lb]\n\t" \
  "s_add_u32 s89, s89, 32768\n\t" \
  "s_and_b32 s16, %[b], 3\n\t" \
  "s_lshl_b32 s16, s16, 3\n\t" \
  "s_add_u32 s16, s16, s7\n\t" \
  "s_mul_i32 s16, s16, 2560\n\t" \
  "s_add_u32 s16, s16, s42\n\t" \
  "v_min_u32 v13, 19, v1\n\t" \
  "v_lshlrev_b32 v13, 7, v13\n\t" \
  "v_add_u32 v13, s16, v13\n\t" \
  "s_lshr_b32 s16, %[b], 3\n\t" \
  "s_lshl_b32 s16, s16, 2\n\t" \
  "s_add_u32 s16, s16, s4\n\t" \
  "s_lshl_b32 s16, s16, 9\n\t" \
  "v_min_u32 v14, 3, v1\n\t" \
  "v_lshlrev_b32 v14, 7, v14\n\t" \
  "v_add_u32 v14, s16, v14\n\t" \
  "v_min_u32 v14, %[cb], v14\n\t" \
  "s_mov_b64 s[48:49], s[38:39]\n\t" \
  "s_mov_b32 s50, 2211840\n\t" \
  "s_mov_b32 s51, 0x20000\n\t" \
  "s_mul_i32 s53, s54, 221184\n\t" \
  "s_add_u32 s53, s53, s43\n\t" \
  "s_add_u32 m0, s89, 0\n\t" \
  "s_nop 0\n\t" \
  "buffer_load_dwordx4 v248, s[48:51], s53 offen offset:0 lds\n\t" \
  "buffer_load_dwordx4 v249, s[48:51], s53 offen offset:1024 lds\n\t" \
  "buffer_load_dwordx4 v250, s[48:51], s53 offen offset:2048 lds\n\t" \
  "buffer_load_dwordx4 v251, s[48:51], s53 offen offset:3072 lds\n\t" \
  "s_add_u32 m0, s89, 4096\n\t" \
  "s_nop 0\n\t" \
  "buffer_load_dwordx4 v252, s[48:51], s53 offen offset:0 lds\n\t" \
  "global_load_dword a242, v13, s[36:37]\n\t" \
  "global_load_dword a243, v14, s[30:31]\n\t" \
  "s_load_dwordx4 s[24:27], %[ka], 40\n\t" \
  "s_load_dwordx2 s[60:61], %[ka], 88\n\t" \
  "s_load_dwordx2 s[92:93], %[ka], 96\n\t" \
  "s_load_dwordx2 s[94:95], %[ka], 64\n\t" \
  "s_load_dwordx2 s[28:29], %[ka], 104\n\t" \
  "s_lshl_b32 s23, %[b], 2\n\t" \
  "s_add_u32 s23, s23, s4\n\t" \
  "s_cmp_ge_u32 s23, 940\n\t" \
  "s_cselect_b32 s16, 940, 0\n\t" \
  "s_sub_u32 s23, s23, s16\n\t" \
  "s_cmp_ge_u32 s23, 540\n\t" \
  "s_cselect_b32 s20, 1, 0\n\t" \
  "s_cselect_b32 s16, 540, 0\n\t" \
  "s_mov_b32 s17, 1214\n\t" \
  "s_cmov_b32 s17, 1639\n\t" \
  "s_cselect_b32 s18, 40, 54\n\t" \
  "s_mov_b32 s19, 40960\n\t" \
  "s_cmov_b32 s19, 20480\n\t" \
  "s_mov_b32 s78, 2211840\n\t" \
  "s_cmov_b32 s78, 819200\n\t" \
  "s_cselect_b32 s85, 0, 20480\n\t" \
  "s_mov_b32 s69, 1024\n\t" \
  "s_cmov_b32 s69, 40960\n\t" \
  "s_sub_u32 s16, s23, s16\n\t" \
  "s_mul_i32 s21, s16, s17\n\t" \
  "s_lshr_b32 s21, s21, 16\n\t" \
  "s_mul_i32 s17, s21, s18\n\t" \
  "s_sub_u32 s22, s16, s17\n\t" \
  "s_mul_i32 s84, s22, s19\n\t" \
  "s_lshl_b32 s16, s21, 7\n\t" \
  "s_add_u32 s84, s84, s16\n\t" \
  "s_add_u32 s85, s85, s84\n\t" \
  "s_mov_b32 s82, s78\n\t" \
  "s_mov_b32 s79, 0x20000\n\t" \
  "s_mov_b32 s83, 0x20000\n\t" \
  "v_mul_u32_u24 v3, 1280, v8\n\t" \
  "v_and_b32 v11, 7, v1\n\t" \
  "v_lshl_add_u32 v3, v11, 4, v3\n\t" \
  "s_cmp_lg_u32 s20, 0\n\t" \
  "s_waitcnt lgkmcnt(0)\n\t" \
  "s_cselect_b64 s[76:77], s[24:25], s[60:61]\n\t" \
  "s_cselect_b64 s[80:81], s[26:27], s[60:61]\n\t" \
  "s_add_u32 m0, s88, 106496\n\t" \
  "s_add_u32 s55, s84, 9216\n\t" \
  "s_add_u32 s56, s85, 9216\n\t" \
  "s_mov_b32 s54, 1\n\t" \
  "s_lshl_b32 s52, s54, 7\n\t" \
  "s_add_u32 s52, s52, s42\n\t" \
  "s_mul_i32 s53, s54, 221184\n\t" \
  "s_add_u32 s53, s53, s43\n\t" \
  "s_add_u32 m0, s88, 53248\n\t" \
  "s_nop 0\n\t" \
  "buffer_load_dwordx4 v240, s[44:47], s52 offen offset:0 lds\n\t" \
  "v_and_b32 v11, 15, v1\n\t" \
  "v_lshrrev_b32 v12, 4, v1\n\t" \
  "v_lshrrev_b32 v13, 1, v11\n\t" \
  "v_xor_b32 v13, v12, v13\n\t" \
  "v_lshlrev_b32 v13, 4, v13\n\t" \
  "v_lshl_add_u32 v13, v11, 7, v13\n\t" \
  "v_xor_b32 v14, 64, v13\n\t" \
  "v_add_u32 v253, s88, v13\n\t" \
  "v_add_u32 v254, s88, v14\n\t" \
  "v_lshrrev_b32 v13, 5, v1\n\t" \
  "v_lshlrev_b32 v13, 4, v13\n\t" \
  "v_and_b32 v14, 1, v12\n\t" \
  "v_lshl_add_u32 v13, v14, 2, v13\n\t" \
  "v_lshrrev_b32 v14, 2, v11\n\t" \
  "v_add_u32 v13, v13, v14\n\t" \
  "v_mul_u32_u24 v13, 160, v13\n\t" \
  "v_and_b32 v14, 3, v1\n\t" \
  "v_lshl_add_u32 v13, v14, 3, v13\n\t" \
  "s_add_u32 s16, %[lb], 32768\n\t" \
  "v_add_u32 v255, s16, v13\n\t" \
  "v_lshlrev_b32 v4, 4, v1\n\t" \
  "buffer_load_dwordx4 v241, s[44:47], s52 offen offset:1024 lds\n\t" \
  "v_mov_b32 v7, 0\n\t" \
  "v_mov_b32 v2, 0\n\t" \
  "v_lshlrev_b32 v13, 7, v11\n\t" \
  "v_lshl_add_u32 v13, v12, 3, v13\n\t" \
  "v_lshrrev_b32 v14, 2, v11\n\t" \
  "v_lshl_add_u32 v14, v12, 3, v14\n\t" \
  "v_lshlrev_b32 v14, 7, v14\n\t" \
  "v_and_b32 v15, 3, v1\n\t" \
  "v_lshl_add_u32 v14, v15, 3, v14\n\t" \
  "s_cmp_lg_u32 s20, 0\n\t" \
  "s_cselect_b64 vcc, -1, 0\n\t" \
  "v_cndmask_b32 v5, v14, v13, vcc\n\t" \
  "v_add_u32 v5, s88, v5\n\t" \
  "s_lshl_b32 s9, s6, 2\n\t" \
  "s_add_u32 s9, s9, s4\n\t" \
  "s_and_b32 s10, %[b], 6\n\t" \
  "s_lshl_b32 s10, s10, 2\n\t" \
  "s_lshr_b32 s16, %[b], 5\n\t" \
  "s_add_u32 s10, s10, s16\n\t" \
  "s_and_b32 s11, %[b], 1\n\t" \
  "s_lshl_b32 s11, s11, 2\n\t" \
  "buffer_load_dwordx4 v242, s[44:47], s52 offen offset:2048 lds\n\t" \
  "s_and_b32 s16, s7, 3\n\t" \
  "s_add_u32 s11, s11, s16\n\t" \
  "s_lshl_b32 s16, s4, 3\n\t" \
  "s_lshr_b32 s12, 0x2b201000, s16\n\t" \
  "s_and_b32 s12, s12, 0xff\n\t" \
  "s_lshr_b32 s13, 0x07040200, s16\n\t" \
  "s_and_b32 s13, s13, 0xff\n\t" \
  "s_mul_i32 s16, s11, 5120\n\t" \
  "s_add_u32 s16, s16, 12320768\n\t" \
  "s_mul_i32 s14, s12, 40960\n\t" \
  "s_add_u32 s14, s14, s16\n\t" \
  "s_mul_i32 s15, s13, 163840\n\t" \
  "s_add_u32 s15, s15, s16\n\t" \
  "s_add_u32 s15, s15, 2211840\n\t" \
  "s_add_u32 s72, s14, 163840\n\t" \
  "s_add_u32 s16, s14, 491520\n\t" \
  "s_cmp_lg_u32 s5, 0\n\t" \
  "s_cselect_b32 s73, s15, s16\n\t" \
  "s_mov_b32 s16, 450560\n\t" \
  "s_cmov_b32 s16, 409600\n\t" \
  "s_add_u32 s96, s14, s16\n\t" \
  "buffer_load_dwordx4 v243, s[44:47], s52 offen offset:3072 lds\n\t" \
  "s_mul_i32 s16, s4, 20480\n\t" \
  "s_add_u32 s16, s16, %[lb]\n\t" \
  "s_add_u32 s91, s16, 61440\n\t" \
  "s_cmp_eq_u32 s4, 3\n\t" \
  "s_cselect_b32 s17, 81920, 0\n\t" \
  "s_add_u32 s16, s16, s17\n\t" \
  "v_writelane_b32 v2, s16, 1\n\t" \
  "v_writelane_b32 v2, s4, 0\n\t" \
  "v_writelane_b32 v2, s91, 17\n\t" \
  "v_writelane_b32 v2, s72, 14\n\t" \
  "v_writelane_b32 v2, s73, 15\n\t" \
  "v_writelane_b32 v2, s96, 16\n\t" \
  "v_writelane_b32 v2, s6, 25\n\t" \
  "v_writelane_b32 v2, %[lb], 29\n\t" \
  "v_writelane_b32 v2, 0, 2\n\t" \
  "s_mul_i32 s16, s5, 163840\n\t" \
  "s_add_u32 s16, s16, s15\n\t" \
  "v_writelane_b32 v2, s16, 3\n\t" \
  "s_add_u32 s16, s16, 163840\n\t" \
  "v_writelane_b32 v2, s16, 4\n\t" \
  "s_lshl_b32 s16, s21, 2\n\t" \
  "s_add_u32 m0, s88, 57344\n\t" \
  "s_nop 0\n\t" \
  "buffer_load_dwordx4 v244, s[44:47], s52 offen offset:0 lds\n\t" \
  "s_mul_i32 s17, s22, 40\n\t" \
  "s_add_u32 s17, s17, s16\n\t" \
  "s_add_u32 s16, s16, 54\n\t" \
  "s_mul_i32 s16, s16, 40\n\t" \
  "s_add_u32 s16, s16, s22\n\t" \
  "s_cmp_lg_u32 s20, 0\n\t" \
  "s_cselect_b32 s16, s16, s17\n\t" \
  "s_lshl_b32 s16, s16, 10\n\t" \
  "s_add_u32 s68, s16, 12320768\n\t" \
  "s_mov_b32 s70, s20\n\t" \
  "s_lshl_b32 s71, %[b], 2\n\t" \
  "s_add_u32 s71, s71, s4\n\t" \
  "s_lshl_b32 s71, s71, 7\n\t" \
  "s_add_u32 s71, s71, 16302080\n\t" \
  "s_mul_i32 s18, s10, 94\n\t" \
  "s_add_u32 s16, s18, s12\n\t" \
  "s_lshl_b32 s16, s16, 12\n\t" \
  "v_writelane_b32 v2, s16, 5\n\t" \
  "s_lshl_b32 s17, s13, 2\n\t" \
  "s_add_u32 s17, s17, s18\n\t" \
  "s_add_u32 s17, s17, 54\n\t" \
  "buffer_load_dwordx4 v245, s[44:47], s52 offen offset:1024 lds\n\t" \
  "s_lshl_b32 s17, s17, 12\n\t" \
  "s_add_u32 s16, s16, 49152\n\t" \
  "s_cmp_lg_u32 s5, 0\n\t" \
  "s_cselect_b32 s16, s17, s16\n\t" \
  "v_writelane_b32 v2, s16, 6\n\t" \
  "s_lshl_b32 s16, s5, 14\n\t" \
  "s_add_u32 s16, s16, s17\n\t" \
  "v_writelane_b32 v2, s16, 7\n\t" \
  "s_lshl_b32 s16, s9, 5\n\t" \
  "s_add_u32 s16, s16, s8\n\t" \
  "s_lshl_b32 s16, s16, 7\n\t" \
  "s_add_u32 s16, s16, 16171008\n\t" \
  "v_writelane_b32 v2, s16, 8\n\t" \
  "s_lshl_b32 s19, s4, 3\n\t" \
  "s_lshr_b32 s16, 0x04050606, s19\n\t" \
  "s_and_b32 s16, s16, 0xff\n\t" \
  "v_writelane_b32 v2, s16, 9\n\t" \
  "s_lshr_b32 s16, 0x110c0600, s19\n\t" \
  "s_and_b32 s16, s16, 0xff\n\t" \
  "s_lshl_b32 s18, s10, 5\n\t" \
  "s_add_u32 s16, s16, s18\n\t" \
  "s_add_u32 m0, s88, 106496\n\t" \
  "s_nop 0\n\t" \
  "buffer_load_dwordx4 v3, s[76:79], s84 offen offset:0 lds\n\t" \
  "s_lshl_b32 s16, s16, 7\n\t" \
  "s_add_u32 s16, s16, 16171008\n\t" \
  "v_writelane_b32 v2, s16, 10\n\t" \
  "s_add_u32 s16, s5, 1\n\t" \
  "v_writelane_b32 v2, s16, 11\n\t" \
  "s_add_u32 s16, s18, s13\n\t" \
  "s_lshl_b32 s16, s16, 7\n\t" \
  "s_add_u32 s16, s16, 16173824\n\t" \
  "v_writelane_b32 v2, s16, 12\n\t" \
  "s_lshl_b32 s16, s10, 9\n\t" \
  "s_add_u32 s16, s16, 16515072\n\t" \
  "v_writelane_b32 v2, s16, 13\n\t" \
  "s_mul_i32 s18, s11, 160\n\t" \
  "s_add_u32 s16, s94, s18\n\t" \
  "s_addc_u32 s17, s95, 0\n\t" \
  "v_writelane_b32 v2, s16, 18\n\t" \
  "v_writelane_b32 v2, s17, 19\n\t" \
  "s_lshl_b32 s19, s10, 2\n\t" \
  "s_add_u32 s19, s19, s4\n\t" \
  "s_mul_i32 s19, s19, 20480\n\t" \
  "s_add_u32 s19, s19, s18\n\t" \
  "buffer_load_dwordx4 v3, s[76:79], s55 offen offset:1024 lds\n\t" \
  "s_add_u32 s86, s36, s19\n\t" \
  "s_addc_u32 s87, s37, 0\n\t" \
  "s_lshl_b32 s19, s19, 1\n\t" \
  "s_add_u32 s16, s28, s19\n\t" \
  "s_addc_u32 s17, s29, 0\n\t" \
  "v_writelane_b32 v2, s16, 20\n\t" \
  "v_writelane_b32 v2, s17, 21\n\t" \
  "s_mul_i32 s35, s9, 385024\n\t" \
  "s_lshr_b32 s16, 0x21870, s8\n\t" \
  "s_and_b32 s16, s16, 1\n\t" \
  "v_writelane_b32 v2, s16, 23\n\t" \
  "s_mov_b32 s16, s8\n\t" \
  "s_cmp_ge_u32 s8, 7\n\t" \
  "s_cselect_b32 s17, 3, 0\n\t" \
  "s_sub_u32 s16, s16, s17\n\t" \
  "s_cmp_ge_u32 s8, 13\n\t" \
  "s_cselect_b32 s17, 2, 0\n\t" \
  "s_sub_u32 s16, s16, s17\n\t" \
  "s_cmp_ge_u32 s8, 18\n\t" \
  "s_cselect_b32 s17, 1, 0\n\t" \
  "s_sub_u32 s16, s16, s17\n\t" \
  "s_add_u32 m0, m0, 2048\n\t" \
  "s_nop 0\n\t" \
  "buffer_load_dwordx4 v3, s[80:83], s85 offen offset:0 lds\n\t" \
  "s_and_b32 s16, s16, 15\n\t" \
  "s_lshl_b32 s17, s9, 4\n\t" \
  "s_add_u32 s16, s16, s17\n\t" \
  "s_mul_i32 s17, s16, 5120\n\t" \
  "v_writelane_b32 v2, s17, 22\n\t" \
  "s_lshl_b32 s16, s16, 5\n\t" \
  "s_add_u32 s16, s16, 16515072\n\t" \
  "v_writelane_b32 v2, s16, 24\n\t" \
  "s_and_b32 s16, s10, 3\n\t" \
  "v_writelane_b32 v2, s16, 30\n\t" \
  "s_mul_i32 s16, s11, 5\n\t" \
  "s_lshr_b32 s17, s16, 2\n\t" \
  "s_mul_i32 s18, s5, 5\n\t" \
  "s_sub_u32 s18, 15, s18\n\t" \
  "v_min_u32 v11, 46, v1\n\t" \
  "v_and_b32 v12, 15, v11\n\t" \
  "v_min_u32 v12, s18, v12\n\t" \
  "v_lshrrev_b32 v13, 4, v11\n\t" \
  "v_add_u32 v13, s17, v13\n\t" \
  "v_mul_u32_u24 v13, 54, v13\n\t" \
  "v_add3_u32 v12, v12, v13, s12\n\t" \
  "buffer_load_dwordx4 v3, s[80:83], s56 offen offset:1024 lds\n\t" \
  "v_subrev_u32 v13, 32, v11\n\t" \
  "v_mul_u32_u24 v14, 52, v13\n\t" \
  "v_lshrrev_b32 v14, 8, v14\n\t" \
  "v_mul_u32_u24 v15, 5, v14\n\t" \
  "v_sub_u32 v13, v13, v15\n\t" \
  "s_add_u32 s18, s5, 1\n\t" \
  "v_min_u32 v14, s18, v14\n\t" \
  "v_add_u32 v14, s13, v14\n\t" \
  "v_mul_u32_u24 v14, 40, v14\n\t" \
  "s_add_u32 s16, s16, 540\n\t" \
  "v_add3_u32 v13, v13, v14, s16\n\t" \
  "v_cmp_gt_u32 vcc, 32, v11\n\t" \
  "s_nop 1\n\t" \
  "v_cndmask_b32 v6, v13, v12, vcc\n\t" \
  "v_lshlrev_b32 v6, 7, v6\n\t" \
  "v_add_u32 v6, 16302080, v6\n\t" \
  "s_mov_b64 s[64:65], s[40:41]\n\t" \
  "s_mov_b32 s66, 16531712\n\t" \
  "s_mov_b32 s67, 0x20000\n\t" \
  "s_mov_b32 s75, 0\n\t" \
  "s_waitcnt vmcnt(12)\n\t" \
  "s_barrier\n\t" \
  "ds_read_b64_tr_b16 v[200:201], v255 offset:0\n\t" \
  "ds_read_b64_tr_b16 v[202:203], v255 offset:1280\n\t" \
  "ds_read_b64_tr_b16 v[204:205], v255 offset:5120\n\t" \
  "ds_read_b64_tr_b16 v[206:207], v255 offset:6400\n\t" \
  "ds_read_b64_tr_b16 v[208:209], v255 offset:10240\n\t" \
  "ds_read_b64_tr_b16 v[210:211], v255 offset:11520\n\t" \
  "ds_read_b64_tr_b16 v[212:213], v255 offset:15360\n\t" \
  "ds_read_b64_tr_b16 v[214:215], v255 offset:16640\n\t" \
  "ds_read_b64_tr_b16 v[216:217], v255 offset:32\n\t" \
  "ds_read_b64_tr_b16 v[218:219], v255 offset:1312\n\t" \
  "ds_read_b64_tr_b16 v[220:221], v255 offset:5152\n\t" \
  "ds_read_b64_tr_b16 v[222:223], v255 offset:6432\n\t" \
  "ds_read_b128 v[168:171], v253 offset:0\n\t" \
  "ds_read_b128 v[172:175], v254 offset:0\n\t" \
  "ds_read_b128 v[176:179], v253 offset:2048\n\t" \
  "ds_read_b128 v[180:183], v254 offset:2048\n\t" \
  "ds_read_b128 v[184:187], v253 offset:4096\n\t" \
  "ds_read_b128 v[188:191], v254 offset:4096\n\t" \
  "ds_read_b128 v[192:195], v253 offset:6144\n\t" \
  "ds_read_b128 v[196:199], v254 offset:6144\n\t" \
  "s_waitcnt lgkmcnt(7)\n\t" \
  "v_mfma_f32_16x16x32_bf16 v[8:11], v[200:203], v[168:171], 0\n\t" \
  "ds_read_b64_tr_b16 v[224:225], v255 offset:10272\n\t" \
  "s_waitcnt lgkmcnt(7)\n\t" \
  "v_mfma_f32_16x16x32_bf16 v[8:11], v[204:207], v[172:175], v[8:11]\n\t" \
  "ds_read_b64_tr_b16 v[226:227], v255 offset:11552\n\t" \
  "s_waitcnt lgkmcnt(7)\n\t" \
  "v_mfma_f32_16x16x32_bf16 v[12:15], v[200:203], v[176:179], 0\n\t" \
  "ds_read_b64_tr_b16 v[228:229], v255 offset:15392\n\t" \
  "v_add_u32 v5, 106496, v5\n\t" \
  "s_waitcnt lgkmcnt(7)\n\t" \
  "v_mfma_f32_16x16x32_bf16 v[12:15], v[204:207], v[180:183], v[12:15]\n\t" \
  "ds_read_b64_tr_b16 v[230:231], v255 offset:16672\n\t" \
  "s_add_u32 m0, s88, 57344\n\t" \
  "s_waitcnt lgkmcnt(7)\n\t" \
  "v_mfma_f32_16x16x32_bf16 v[16:19], v[200:203], v[184:187], 0\n\t" \
  "buffer_load_dwordx4 v246, s[44:47], s52 offen offset:2048 lds\n\t" \
  "s_waitcnt lgkmcnt(6)\n\t" \
  "v_mfma_f32_16x16x32_bf16 v[16:19], v[204:207], v[188:191], v[16:19]\n\t" \
  "s_waitcnt lgkmcnt(5)\n\t" \
  "v_mfma_f32_16x16x32_bf16 v[20:23], v[200:203], v[192:195], 0\n\t" \
  "buffer_load_dwordx4 v247, s[44:47], s52 offen offset:3072 lds\n\t" \
  "s_add_u32 m0, s89, 53248\n\t" \
  "s_waitcnt lgkmcnt(4)\n\t" \
  "v_mfma_f32_16x16x32_bf16 v[20:23], v[204:207], v[196:199], v[20:23]\n\t" \
  "buffer_load_dwordx4 v248, s[48:51], s53 offen offset:0 lds\n\t" \
  "v_mfma_f32_16x16x32_bf16 v[24:27], v[208:211], v[168:171], 0\n\t" \
  "ds_read_b64_tr_b16 v[232:233], v255 offset:64\n\t" \
  "v_mfma_f32_16x16x32_bf16 v[24:27], v[212:215], v[172:175], v[24:27]\n\t" \
  "ds_read_b64_tr_b16 v[234:235], v255 offset:1344\n\t" \
  "v_mfma_f32_16x16x32_bf16 v[28:31], v[208:211], v[176:179], 0\n\t" \
  "ds_read_b64_tr_b16 v[236:237], v255 offset:5184\n\t" \
  "v_mfma_f32_16x16x32_bf16 v[28:31], v[212:215], v[180:183], v[28:31]\n\t" \
  "ds_read_b64_tr_b16 v[238:239], v255 offset:6464\n\t" \
  "v_mfma_f32_16x16x32_bf16 v[32:35], v[208:211], v[184:187], 0\n\t" \
  "buffer_load_dwordx4 v249, s[48:51], s53 offen offset:1024 lds\n\t" \
  "v_mfma_f32_16x16x32_bf16 v[32:35], v[212:215], v[188:191], v[32:35]\n\t" \
  "v_mfma_f32_16x16x32_bf16 v[36:39], v[208:211], v[192:195], 0\n\t" \
  "buffer_load_dwordx4 v250, s[48:51], s53 offen offset:2048 lds\n\t" \
  "v_mfma_f32_16x16x32_bf16 v[36:39], v[212:215], v[196:199], v[36:39]\n\t" \
  "buffer_load_dwordx4 v251, s[48:51], s53 offen offset:3072 lds\n\t" \
  "v_mfma_f32_16x16x32_bf16 v[40:43], v[216:219], v[168:171], 0\n\t" \
  "ds_read_b64_tr_b16 v[200:201], v255 offset:10304\n\t" \
  "s_waitcnt vmcnt(6)\n\t" \
  "s_cmp_eq_u32 s70, 0\n\t" \
  "s_cbranch_scc0 P1ffn_RS_%=\n\t" \
  "ds_read_b64_tr_b16 a[0:1], v5 offset:0\n\t" \
  "ds_read_b64_tr_b16 a[2:3], v5 offset:512\n\t" \
  "ds_read_b64_tr_b16 a[4:5], v5 offset:32\n\t" \
  "ds_read_b64_tr_b16 a[6:7], v5 offset:544\n\t" \
  "ds_read_b64_tr_b16 a[8:9], v5 offset:64\n\t" \
  "ds_read_b64_tr_b16 a[10:11], v5 offset:576\n\t" \
  "ds_read_b64_tr_b16 a[12:13], v5 offset:96\n\t" \
  "ds_read_b64_tr_b16 a[14:15], v5 offset:608\n\t" \
  "s_branch P1ffn_RE_%=\n\t" \
  "P1ffn_RS_%=:\n\t" \
  "ds_read_b64 a[0:1], v5 offset:0\n\t" \
  "ds_read_b64 a[2:3], v5 offset:2048\n\t" \
  "ds_read_b64 a[4:5], v5 offset:32\n\t" \
  "ds_read_b64 a[6:7], v5 offset:2080\n\t" \
  "ds_read_b64 a[8:9], v5 offset:64\n\t" \
  "ds_read_b64 a[10:11], v5 offset:2112\n\t" \
  "ds_read_b64 a[12:13], v5 offset:96\n\t" \
  "ds_read_b64 a[14:15], v5 offset:2144\n\t" \
  "P1ffn_RE_%=:\n\t" \
  "v_mfma_f32_16x16x32_bf16 v[40:43], v[220:223], v[172:175], v[40:43]\n\t" \
  "ds_read_b64_tr_b16 v[202:203], v255 offset:11584\n\t" \
  "v_mfma_f32_16x16x32_bf16 v[44:47], v[216:219], v[176:179], 0\n\t" \
  "ds_read_b64_tr_b16 v[204:205], v255 offset:15424\n\t" \
  "v_mfma_f32_16x16x32_bf16 v[44:47], v[220:223], v[180:183], v[44:47]\n\t" \
  "ds_read_b64_tr_b16 v[206:207], v255 offset:16704\n\t" \
  "s_add_u32 m0, s89, 57344\n\t" \
  "v_mfma_f32_16x16x32_bf16 v[48:51], v[216:219], v[184:187], 0\n\t" \
  "buffer_load_dwordx4 v252, s[48:51], s53 offen offset:0 lds\n\t" \
  "v_mfma_f32_16x16x32_bf16 v[48:51], v[220:223], v[188:191], v[48:51]\n\t" \
  "s_add_u32 s54, s90, 2\n\t" \
  "s_lshl_b32 s52, s54, 7\n\t" \
  "s_add_u32 s52, s52, s42\n\t" \
  "s_mul_i32 s53, s54, 221184\n\t" \
  "s_add_u32 s53, s53, s43\n\t" \
  "s_add_u32 m0, s89, 106496\n\t" \
  "v_mfma_f32_16x16x32_bf16 v[52:55], v[216:219], v[192:195], 0\n\t" \
  "buffer_load_dwordx4 v248, s[48:51], s53 offen offset:0 lds\n\t" \
  "v_mfma_f32_16x16x32_bf16 v[52:55], v[220:223], v[196:199], v[52:55]\n\t" \
  "buffer_load_dwordx4 v249, s[48:51], s53 offen offset:1024 lds\n\t" \
  "s_waitcnt lgkmcnt(15)\n\t" \
  "v_mfma_f32_16x16x32_bf16 v[56:59], v[224:227], v[168:171], 0\n\t" \
  "ds_read_b64_tr_b16 v[208:209], v255 offset:96\n\t" \
  "s_waitcnt lgkmcnt(15)\n\t" \
  "v_mfma_f32_16x16x32_bf16 v[56:59], v[228:231], v[172:175], v[56:59]\n\t" \
  "ds_read_b64_tr_b16 v[210:211], v255 offset:1376\n\t" \
  "v_mfma_f32_16x16x32_bf16 v[60:63], v[224:227], v[176:179], 0\n\t" \
  "ds_read_b64_tr_b16 v[212:213], v255 offset:5216\n\t" \
  "v_mfma_f32_16x16x32_bf16 v[60:63], v[228:231], v[180:183], v[60:63]\n\t" \
  "ds_read_b64_tr_b16 v[214:215], v255 offset:6496\n\t" \
  "v_mfma_f32_16x16x32_bf16 v[64:67], v[224:227], v[184:187], 0\n\t" \
  "buffer_load_dwordx4 v250, s[48:51], s53 offen offset:2048 lds\n\t" \
  "v_mfma_f32_16x16x32_bf16 v[64:67], v[228:231], v[188:191], v[64:67]\n\t" \
  "v_mfma_f32_16x16x32_bf16 v[68:71], v[224:227], v[192:195], 0\n\t" \
  "buffer_load_dwordx4 v251, s[48:51], s53 offen offset:3072 lds\n\t" \
  "s_add_u32 m0, s89, 110592\n\t" \
  "v_mfma_f32_16x16x32_bf16 v[68:71], v[228:231], v[196:199], v[68:71]\n\t" \
  "buffer_load_dwordx4 v252, s[48:51], s53 offen offset:0 lds\n\t" \
  "s_waitcnt lgkmcnt(15)\n\t" \
  "v_mfma_f32_16x16x32_bf16 v[72:75], v[232:235], v[168:171], 0\n\t" \
  "ds_read_b64_tr_b16 v[216:217], v255 offset:10336\n\t" \
  "s_waitcnt lgkmcnt(15)\n\t" \
  "v_mfma_f32_16x16x32_bf16 v[72:75], v[236:239], v[172:175], v[72:75]\n\t" \
  "ds_read_b64_tr_b16 v[218:219], v255 offset:11616\n\t" \
  "v_mfma_f32_16x16x32_bf16 v[76:79], v[232:235], v[176:179], 0\n\t" \
  "ds_read_b64_tr_b16 v[220:221], v255 offset:15456\n\t" \
  "v_mfma_f32_16x16x32_bf16 v[76:79], v[236:239], v[180:183], v[76:79]\n\t" \
  "ds_read_b64_tr_b16 v[222:223], v255 offset:16736\n\t" \
  "s_add_u32 m0, s88, 106496\n\t" \
  "v_mfma_f32_16x16x32_bf16 v[80:83], v[232:235], v[184:187], 0\n\t" \
  "s_waitcnt lgkmcnt(11)\n\t" \
  "buffer_load_dwordx4 v240, s[44:47], s52 offen offset:0 lds\n\t" \
  "v_mfma_f32_16x16x32_bf16 v[80:83], v[236:239], v[188:191], v[80:83]\n\t" \
  "v_mfma_f32_16x16x32_bf16 v[84:87], v[232:235], v[192:195], 0\n\t" \
  "buffer_load_dwordx4 v241, s[44:47], s52 offen offset:1024 lds\n\t" \
  "v_mfma_f32_16x16x32_bf16 v[84:87], v[236:239], v[196:199], v[84:87]\n\t" \
  "buffer_load_dwordx4 v242, s[44:47], s52 offen offset:2048 lds\n\t" \
  "s_waitcnt lgkmcnt(10)\n\t" \
  "v_mfma_f32_16x16x32_bf16 v[88:91], v[200:203], v[168:171], 0\n\t" \
  "ds_read_b64_tr_b16 v[224:225], v255 offset:128\n\t" \
  "s_waitcnt lgkmcnt(9)\n\t" \
  "v_mfma_f32_16x16x32_bf16 v[88:91], v[204:207], v[172:175], v[88:91]\n\t" \
  "ds_read_b64_tr_b16 v[226:227], v255 offset:1408\n\t" \
  "v_mfma_f32_16x16x32_bf16 v[92:95], v[200:203], v[176:179], 0\n\t" \
  "ds_read_b64_tr_b16 v[228:229], v255 offset:5248\n\t" \
  "v_mfma_f32_16x16x32_bf16 v[92:95], v[204:207], v[180:183], v[92:95]\n\t" \
  "ds_read_b64_tr_b16 v[230:231], v255 offset:6528\n\t" \
  "v_mfma_f32_16x16x32_bf16 v[96:99], v[200:203], v[184:187], 0\n\t" \
  "buffer_load_dwordx4 v243, s[44:47], s52 offen offset:3072 lds\n\t" \
  "v_mfma_f32_16x16x32_bf16 v[96:99], v[204:207], v[188:191], v[96:99]\n\t" \
  "s_add_u32 m0, s88, 110592\n\t" \
  "v_mfma_f32_16x16x32_bf16 v[100:103], v[200:203], v[192:195], 0\n\t" \
  "buffer_load_dwordx4 v244, s[44:47], s52 offen offset:0 lds\n\t" \
  "v_mfma_f32_16x16x32_bf16 v[100:103], v[204:207], v[196:199], v[100:103]\n\t" \
  "buffer_load_dwordx4 v245, s[44:47], s52 offen offset:1024 lds\n\t" \
  "s_waitcnt lgkmcnt(10)\n\t" \
  "v_mfma_f32_16x16x32_bf16 v[104:107], v[208:211], v[168:171], 0\n\t" \
  "ds_read_b64_tr_b16 v[232:233], v255 offset:10368\n\t" \
  "s_waitcnt lgkmcnt(9)\n\t" \
  "v_mfma_f32_16x16x32_bf16 v[104:107], v[212:215], v[172:175], v[104:107]\n\t" \
  "ds_read_b64_tr_b16 v[234:235], v255 offset:11648\n\t" \
  "v_mfma_f32_16x16x32_bf16 v[108:111], v[208:211], v[176:179], 0\n\t" \
  "ds_read_b64_tr_b16 v[236:237], v255 offset:15488\n\t" \
  "v_mfma_f32_16x16x32_bf16 v[108:111], v[212:215], v[180:183], v[108:111]\n\t" \
  "ds_read_b64_tr_b16 v[238:239], v255 offset:16768\n\t" \
  "v_mfma_f32_16x16x32_bf16 v[112:115], v[208:211], v[184:187], 0\n\t" \
  "buffer_load_dwordx4 v246, s[44:47], s52 offen offset:2048 lds\n\t" \
  "v_mfma_f32_16x16x32_bf16 v[112:115], v[212:215], v[188:191], v[112:115]\n\t" \
  "v_mfma_f32_16x16x32_bf16 v[116:119], v[208:211], v[192:195], 0\n\t" \
  "buffer_load_dwordx4 v247, s[44:47], s52 offen offset:3072 lds\n\t" \
  "v_mfma_f32_16x16x32_bf16 v[116:119], v[212:215], v[196:199], v[116:119]\n\t" \
  "s_mov_b32 s55, 16531456\n\t" \
  "buffer_load_dword a240, v7, s[64:67], s55 offen sc1\n\t" \
  "s_waitcnt vmcnt(14) lgkmcnt(0)\n\t" \
  "s_barrier\n\t" \
  "v_add_u32 v255, 53248, v255\n\t" \
  "v_mfma_f32_16x16x32_bf16 v[120:123], v[216:219], v[168:171], 0\n\t" \
  "ds_read_b64_tr_b16 v[200:201], v255 offset:0\n\t" \
  "v_mfma_f32_16x16x32_bf16 v[120:123], v[220:223], v[172:175], v[120:123]\n\t" \
  "ds_read_b64_tr_b16 v[202:203], v255 offset:1280\n\t" \
  "v_mfma_f32_16x16x32_bf16 v[124:127], v[216:219], v[176:179], 0\n\t" \
  "ds_read_b64_tr_b16 v[204:205], v255 offset:5120\n\t" \
  "v_mfma_f32_16x16x32_bf16 v[124:127], v[220:223], v[180:183], v[124:127]\n\t" \
  "ds_read_b64_tr_b16 v[206:207], v255 offset:6400\n\t" \
  "v_mfma_f32_16x16x32_bf16 v[128:131], v[216:219], v[184:187], 0\n\t" \
  "s_mov_b32 s55, s68\n\t" \
  "buffer_store_dwordx4 a[0:3], v4, s[64:67], s55 offen sc1\n\t" \
  "v_mfma_f32_16x16x32_bf16 v[128:131], v[220:223], v[188:191], v[128:131]\n\t" \
  "v_mfma_f32_16x16x32_bf16 v[132:135], v[216:219], v[192:195], 0\n\t" \
  "s_add_u32 s55, s55, s69\n\t" \
  "buffer_store_dwordx4 a[4:7], v4, s[64:67], s55 offen sc1\n\t" \
  "v_mfma_f32_16x16x32_bf16 v[132:135], v[220:223], v[196:199], v[132:135]\n\t" \
  "s_add_u32 s55, s55, s69\n\t" \
  "buffer_store_dwordx4 a[8:11], v4, s[64:67], s55 offen sc1\n\t" \
  "v_mfma_f32_16x16x32_bf16 v[136:139], v[224:227], v[168:171], 0\n\t" \
  "ds_read_b64_tr_b16 v[208:209], v255 offset:10240\n\t" \
  "v_mfma_f32_16x16x32_bf16 v[136:139], v[228:231], v[172:175], v[136:139]\n\t" \
  "ds_read_b64_tr_b16 v[210:211], v255 offset:11520\n\t" \
  "s_add_u32 s54, s54, 1\n\t" \
  "s_cmp_eq_u32 s54, 10\n\t" \
  "s_cselect_b32 s54, 2, s54\n\t" \
  "v_mfma_f32_16x16x32_bf16 v[140:143], v[224:227], v[176:179], 0\n\t" \
  "ds_read_b64_tr_b16 v[212:213], v255 offset:15360\n\t" \
  "v_mfma_f32_16x16x32_bf16 v[140:143], v[228:231], v[180:183], v[140:143]\n\t" \
  "ds_read_b64_tr_b16 v[214:215], v255 offset:16640\n\t" \
  "s_lshl_b32 s52, s54, 7\n\t" \
  "s_add_u32 s52, s52, s42\n\t" \
  "v_mfma_f32_16x16x32_bf16 v[144:147], v[224:227], v[184:187], 0\n\t" \
  "s_add_u32 s55, s55, s69\n\t" \
  "buffer_store_dwordx4 a[12:15], v4, s[64:67], s55 offen sc1\n\t" \
  "s_mul_i32 s53, s54, 221184\n\t" \
  "s_add_u32 s53, s53, s43\n\t" \
  "v_mfma_f32_16x16x32_bf16 v[144:147], v[228:231], v[188:191], v[144:147]\n\t" \
  "v_mfma_f32_16x16x32_bf16 v[148:151], v[224:227], v[192:195], 0\n\t" \
  "v_add_u32 v253, 53248, v253\n\t" \
  "v_add_u32 v254, 53248, v254\n\t" \
  "v_mfma_f32_16x16x32_bf16 v[148:151], v[228:231], v[196:199], v[148:151]\n\t" \
  "v_mfma_f32_16x16x32_bf16 v[152:155], v[232:235], v[168:171], 0\n\t" \
  "ds_read_b64_tr_b16 v[216:217], v255 offset:32\n\t" \
  "v_mfma_f32_16x16x32_bf16 v[152:155], v[236:239], v[172:175], v[152:155]\n\t" \
  "ds_read_b64_tr_b16 v[218:219], v255 offset:1312\n\t" \
  "ds_read_b128 v[168:171], v253 offset:0\n\t" \
  "ds_read_b128 v[172:175], v254 offset:0\n\t" \
  "v_mfma_f32_16x16x32_bf16 v[156:159], v[232:235], v[176:179], 0\n\t" \
  "ds_read_b64_tr_b16 v[220:221], v255 offset:5152\n\t" \
  "v_mfma_f32_16x16x32_bf16 v[156:159], v[236:239], v[180:183], v[156:159]\n\t" \
  "ds_read_b64_tr_b16 v[222:223], v255 offset:6432\n\t" \
  "ds_read_b128 v[176:179], v253 offset:2048\n\t" \
  "ds_read_b128 v[180:183], v254 offset:2048\n\t" \
  "v_mfma_f32_16x16x32_bf16 v[160:163], v[232:235], v[184:187], 0\n\t" \
  "v_mfma_f32_16x16x32_bf16 v[160:163], v[236:239], v[188:191], v[160:163]\n\t" \
  "ds_read_b128 v[184:187], v253 offset:4096\n\t" \
  "ds_read_b128 v[188:191], v254 offset:4096\n\t" \
  "v_mfma_f32_16x16x32_bf16 v[164:167], v[232:235], v[192:195], 0\n\t" \
  "v_mfma_f32_16x16x32_bf16 v[164:167], v[236:239], v[196:199], v[164:167]\n\t" \
  "ds_read_b128 v[192:195], v253 offset:6144\n\t" \
  "ds_read_b128 v[196:199], v254 offset:6144\n\t" \
  "s_waitcnt lgkmcnt(9)\n\t" \
  "v_mfma_f32_16x16x32_bf16 v[8:11], v[200:203], v[168:171], v[8:11]\n\t" \
  "ds_read_b64_tr_b16 v[224:225], v255 offset:10272\n\t" \
  "s_waitcnt lgkmcnt(9)\n\t" \
  "v_mfma_f32_16x16x32_bf16 v[8:11], v[204:207], v[172:175], v[8:11]\n\t" \
  "ds_read_b64_tr_b16 v[226:227], v255 offset:11552\n\t" \
  "s_waitcnt lgkmcnt(7)\n\t" \
  "v_mfma_f32_16x16x32_bf16 v[12:15], v[200:203], v[176:179], v[12:15]\n\t" \
  "ds_read_b64_tr_b16 v[228:229], v255 offset:15392\n\t" \
  "s_waitcnt lgkmcnt(7)\n\t" \
  "v_mfma_f32_16x16x32_bf16 v[12:15], v[204:207], v[180:183], v[12:15]\n\t" \
  "ds_read_b64_tr_b16 v[230:231], v255 offset:16672\n\t" \
  "s_waitcnt lgkmcnt(7)\n\t" \
  "v_mfma_f32_16x16x32_bf16 v[16:19], v[200:203], v[184:187], v[16:19]\n\t" \
  "s_add_u32 m0, s88, 0\n\t" \
  "s_waitcnt lgkmcnt(6)\n\t" \
  "v_mfma_f32_16x16x32_bf16 v[16:19], v[204:207], v[188:191], v[16:19]\n\t" \
  "buffer_load_dwordx4 v240, s[44:47], s52 offen offset:0 lds\n\t" \
  "s_waitcnt lgkmcnt(5)\n\t" \
  "v_mfma_f32_16x16x32_bf16 v[20:23], v[200:203], v[192:195], v[20:23]\n\t" \
  "s_waitcnt lgkmcnt(4)\n\t" \
  "v_mfma_f32_16x16x32_bf16 v[20:23], v[204:207], v[196:199], v[20:23]\n\t" \
  "buffer_load_dwordx4 v241, s[44:47], s52 offen offset:1024 lds\n\t" \
  "v_mfma_f32_16x16x32_bf16 v[24:27], v[208:211], v[168:171], v[24:27]\n\t" \
  "ds_read_b64_tr_b16 v[232:233], v255 offset:64\n\t" \
  "v_mfma_f32_16x16x32_bf16 v[24:27], v[212:215], v[172:175], v[24:27]\n\t" \
  "ds_read_b64_tr_b16 v[234:235], v255 offset:1344\n\t" \
  "v_mfma_f32_16x16x32_bf16 v[28:31], v[208:211], v[176:179], v[28:31]\n\t" \
  "ds_read_b64_tr_b16 v[236:237], v255 offset:5184\n\t" \
  "v_mfma_f32_16x16x32_bf16 v[28:31], v[212:215], v[180:183], v[28:31]\n\t" \
  "ds_read_b64_tr_b16 v[238:239], v255 offset:6464\n\t" \
  "v_mfma_f32_16x16x32_bf16 v[32:35], v[208:211], v[184:187], v[32:35]\n\t" \
  "v_mfma_f32_16x16x32_bf16 v[32:35], v[212:215], v[188:191], v[32:35]\n\t" \
  "buffer_load_dwordx4 v242, s[44:47], s52 offen offset:2048 lds\n\t" \
  "v_mfma_f32_16x16x32_bf16 v[36:39], v[208:211], v[192:195], v[36:39]\n\t" \
  "v_mfma_f32_16x16x32_bf16 v[36:39], v[212:215], v[196:199], v[36:39]\n\t" \
  "buffer_load_dwordx4 v243, s[44:47], s52 offen offset:3072 lds\n\t" \
  "v_mfma_f32_16x16x32_bf16 v[40:43], v[216:219], v[168:171], v[40:43]\n\t" \
  "ds_read_b64_tr_b16 v[200:201], v255 offset:10304\n\t" \
  "v_mfma_f32_16x16x32_bf16 v[40:43], v[220:223], v[172:175], v[40:43]\n\t" \
  "ds_read_b64_tr_b16 v[202:203], v255 offset:11584\n\t" \
  "v_mfma_f32_16x16x32_bf16 v[44:47], v[216:219], v[176:179], v[44:47]\n\t" \
  "ds_read_b64_tr_b16 v[204:205], v255 offset:15424\n\t" \
  "v_mfma_f32_16x16x32_bf16 v[44:47], v[220:223], v[180:183], v[44:47]\n\t" \
  "ds_read_b64_tr_b16 v[206:207], v255 offset:16704\n\t" \
  "v_mfma_f32_16x16x32_bf16 v[48:51], v[216:219], v[184:187], v[48:51]\n\t" \
  "s_add_u32 m0, s88, 4096\n\t" \
  "v_mfma_f32_16x16x32_bf16 v[48:51], v[220:223], v[188:191], v[48:51]\n\t" \
  "buffer_load_dwordx4 v244, s[44:47], s52 offen offset:0 lds\n\t" \
  "v_mfma_f32_16x16x32_bf16 v[52:55], v[216:219], v[192:195], v[52:55]\n\t" \
  "v_mfma_f32_16x16x32_bf16 v[52:55], v[220:223], v[196:199], v[52:55]\n\t" \
  "buffer_load_dwordx4 v245, s[44:47], s52 offen offset:1024 lds\n\t" \
  "s_waitcnt lgkmcnt(10)\n\t" \
  "v_mfma_f32_16x16x32_bf16 v[56:59], v[224:227], v[168:171], v[56:59]\n\t" \
  "ds_read_b64_tr_b16 v[208:209], v255 offset:96\n\t" \
  "s_waitcnt lgkmcnt(9)\n\t" \
  "v_mfma_f32_16x16x32_bf16 v[56:59], v[228:231], v[172:175], v[56:59]\n\t" \
  "ds_read_b64_tr_b16 v[210:211], v255 offset:1376\n\t" \
  "v_mfma_f32_16x16x32_bf16 v[60:63], v[224:227], v[176:179], v[60:63]\n\t" \
  "ds_read_b64_tr_b16 v[212:213], v255 offset:5216\n\t" \
  "v_mfma_f32_16x16x32_bf16 v[60:63], v[228:231], v[180:183], v[60:63]\n\t" \
  "ds_read_b64_tr_b16 v[214:215], v255 offset:6496\n\t" \
  "v_mfma_f32_16x16x32_bf16 v[64:67], v[224:227], v[184:187], v[64:67]\n\t" \
  "v_mfma_f32_16x16x32_bf16 v[64:67], v[228:231], v[188:191], v[64:67]\n\t" \
  "buffer_load_dwordx4 v246, s[44:47], s52 offen offset:2048 lds\n\t" \
  "v_mfma_f32_16x16x32_bf16 v[68:71], v[224:227], v[192:195], v[68:71]\n\t" \
  "v_mfma_f32_16x16x32_bf16 v[68:71], v[228:231], v[196:199], v[68:71]\n\t" \
  "buffer_load_dwordx4 v247, s[44:47], s52 offen offset:3072 lds\n\t" \
  "s_waitcnt lgkmcnt(10)\n\t" \
  "v_mfma_f32_16x16x32_bf16 v[72:75], v[232:235], v[168:171], v[72:75]\n\t" \
  "ds_read_b64_tr_b16 v[216:217], v255 offset:10336\n\t" \
  "s_waitcnt lgkmcnt(9)\n\t" \
  "v_mfma_f32_16x16x32_bf16 v[72:75], v[236:239], v[172:175], v[72:75]\n\t" \
  "ds_read_b64_tr_b16 v[218:219], v255 offset:11616\n\t" \
  "v_mfma_f32_16x16x32_bf16 v[76:79], v[232:235], v[176:179], v[76:79]\n\t" \
  "ds_read_b64_tr_b16 v[220:221], v255 offset:15456\n\t" \
  "v_mfma_f32_16x16x32_bf16 v[76:79], v[236:239], v[180:183], v[76:79]\n\t" \
  "ds_read_b64_tr_b16 v[222:223], v255 offset:16736\n\t" \
  "v_mfma_f32_16x16x32_bf16 v[80:83], v[232:235], v[184:187], v[80:83]\n\t" \
  "s_add_u32 m0, s89, 0\n\t" \
  "v_mfma_f32_16x16x32_bf16 v[80:83], v[236:239], v[188:191], v[80:83]\n\t" \
  "buffer_load_dwordx4 v248, s[48:51], s53 offen offset:0 lds\n\t" \
  "v_mfma_f32_16x16x32_bf16 v[84:87], v[232:235], v[192:195], v[84:87]\n\t" \
  "v_mfma_f32_16x16x32_bf16 v[84:87], v[236:239], v[196:199], v[84:87]\n\t" \
  "buffer_load_dwordx4 v249, s[48:51], s53 offen offset:1024 lds\n\t" \
  "s_waitcnt lgkmcnt(10)\n\t" \
  "v_mfma_f32_16x16x32_bf16 v[88:91], v[200:203], v[168:171], v[88:91]\n\t" \
  "ds_read_b64_tr_b16 v[224:225], v255 offset:128\n\t" \
  "s_waitcnt lgkmcnt(9)\n\t" \
  "v_mfma_f32_16x16x32_bf16 v[88:91], v[204:207], v[172:175], v[88:91]\n\t" \
  "ds_read_b64_tr_b16 v[226:227], v255 offset:1408\n\t" \
  "v_mfma_f32_16x16x32_bf16 v[92:95], v[200:203], v[176:179], v[92:95]\n\t" \
  "ds_read_b64_tr_b16 v[228:229], v255 offset:5248\n\t" \
  "v_mfma_f32_16x16x32_bf16 v[92:95], v[204:207], v[180:183], v[92:95]\n\t" \
  "ds_read_b64_tr_b16 v[230:231], v255 offset:6528\n\t" \
  "v_mfma_f32_16x16x32_bf16 v[96:99], v[200:203], v[184:187], v[96:99]\n\t" \
  "v_mfma_f32_16x16x32_bf16 v[96:99], v[204:207], v[188:191], v[96:99]\n\t" \
  "buffer_load_dwordx4 v250, s[48:51], s53 offen offset:2048 lds\n\t" \
  "v_mfma_f32_16x16x32_bf16 v[100:103], v[200:203], v[192:195], v[100:103]\n\t" \
  "v_mfma_f32_16x16x32_bf16 v[100:103], v[204:207], v[196:199], v[100:103]\n\t" \
  "buffer_load_dwordx4 v251, s[48:51], s53 offen offset:3072 lds\n\t" \
  "s_waitcnt lgkmcnt(10)\n\t" \
  "v_mfma_f32_16x16x32_bf16 v[104:107], v[208:211], v[168:171], v[104:107]\n\t" \
  "ds_read_b64_tr_b16 v[232:233], v255 offset:10368\n\t" \
  "s_waitcnt lgkmcnt(9)\n\t" \
  "v_mfma_f32_16x16x32_bf16 v[104:107], v[212:215], v[172:175], v[104:107]\n\t" \
  "ds_read_b64_tr_b16 v[234:235], v255 offset:11648\n\t" \
  "v_mfma_f32_16x16x32_bf16 v[108:111], v[208:211], v[176:179], v[108:111]\n\t" \
  "ds_read_b64_tr_b16 v[236:237], v255 offset:15488\n\t" \
  "v_mfma_f32_16x16x32_bf16 v[108:111], v[212:215], v[180:183], v[108:111]\n\t" \
  "ds_read_b64_tr_b16 v[238:239], v255 offset:16768\n\t" \
  "v_mfma_f32_16x16x32_bf16 v[112:115], v[208:211], v[184:187], v[112:115]\n\t" \
  "s_add_u32 m0, s89, 4096\n\t" \
  "v_mfma_f32_16x16x32_bf16 v[112:115], v[212:215], v[188:191], v[112:115]\n\t" \
  "buffer_load_dwordx4 v252, s[48:51], s53 offen offset:0 lds\n\t" \
  "v_mfma_f32_16x16x32_bf16 v[116:119], v[208:211], v[192:195], v[116:119]\n\t" \
  "v_mfma_f32_16x16x32_bf16 v[116:119], v[212:215], v[196:199], v[116:119]\n\t" \
  "s_waitcnt vmcnt(18) lgkmcnt(0)\n\t" \
  "s_barrier\n\t" \
  "v_add_u32 v255, 53248, v255\n\t" \
  "v_mfma_f32_16x16x32_bf16 v[120:123], v[216:219], v[168:171], v[120:123]\n\t" \
  "ds_read_b64_tr_b16 v[200:201], v255 offset:0\n\t" \
  "v_mfma_f32_16x16x32_bf16 v[120:123], v[220:223], v[172:175], v[120:123]\n\t" \
  "ds_read_b64_tr_b16 v[202:203], v255 offset:1280\n\t" \
  "v_mfma_f32_16x16x32_bf16 v[124:127], v[216:219], v[176:179], v[124:127]\n\t" \
  "ds_read_b64_tr_b16 v[204:205], v255 offset:5120\n\t" \
  "v_mfma_f32_16x16x32_bf16 v[124:127], v[220:223], v[180:183], v[124:127]\n\t" \
  "ds_read_b64_tr_b16 v[206:207], v255 offset:6400\n\t" \
  "v_mfma_f32_16x16x32_bf16 v[128:131], v[216:219], v[184:187], v[128:131]\n\t" \
  "v_mfma_f32_16x16x32_bf16 v[128:131], v[220:223], v[188:191], v[128:131]\n\t" \
  "v_mfma_f32_16x16x32_bf16 v[132:135], v[216:219], v[192:195], v[132:135]\n\t" \
  "v_mfma_f32_16x16x32_bf16 v[132:135], v[220:223], v[196:199], v[132:135]\n\t" \
  "v_mfma_f32_16x16x32_bf16 v[136:139], v[224:227], v[168:171], v[136:139]\n\t" \
  "ds_read_b64_tr_b16 v[208:209], v255 offset:10240\n\t" \
  "v_mfma_f32_16x16x32_bf16 v[136:139], v[228:231], v[172:175], v[136:139]\n\t" \
  "ds_read_b64_tr_b16 v[210:211], v255 offset:11520\n\t" \
  "s_add_u32 s54, s54, 1\n\t" \
  "s_cmp_eq_u32 s54, 10\n\t" \
  "s_cselect_b32 s54, 2, s54\n\t" \
  "v_mfma_f32_16x16x32_bf16 v[140:143], v[224:227], v[176:179], v[140:143]\n\t" \
  "ds_read_b64_tr_b16 v[212:213], v255 offset:15360\n\t" \
  "v_mfma_f32_16x16x32_bf16 v[140:143], v[228:231], v[180:183], v[140:143]\n\t" \
  "ds_read_b64_tr_b16 v[214:215], v255 offset:16640\n\t" \
  "s_lshl_b32 s52, s54, 7\n\t" \
  "s_add_u32 s52, s52, s42\n\t" \
  "v_mfma_f32_16x16x32_bf16 v[144:147], v[224:227], v[184:187], v[144:147]\n\t" \
  "s_mul_i32 s53, s54, 221184\n\t" \
  "s_add_u32 s53, s53, s43\n\t" \
  "v_mfma_f32_16x16x32_bf16 v[144:147], v[228:231], v[188:191], v[144:147]\n\t" \
  "v_mfma_f32_16x16x32_bf16 v[148:151], v[224:227], v[192:195], v[148:151]\n\t" \
  "v_add_u32 v253, 53248, v253\n\t" \
  "v_add_u32 v254, 53248, v254\n\t" \
  "v_mfma_f32_16x16x32_bf16 v[148:151], v[228:231], v[196:199], v[148:151]\n\t" \
  "v_mfma_f32_16x16x32_bf16 v[152:155], v[232:235], v[168:171], v[152:155]\n\t" \
  "ds_read_b64_tr_b16 v[216:217], v255 offset:32\n\t" \
  "v_mfma_f32_16x16x32_bf16 v[152:155], v[236:239], v[172:175], v[152:155]\n\t" \
  "ds_read_b64_tr_b16 v[218:219], v255 offset:1312\n\t" \
  "ds_read_b128 v[168:171], v253 offset:0\n\t" \
  "ds_read_b128 v[172:175], v254 offset:0\n\t" \
  "v_mfma_f32_16x16x32_bf16 v[156:159], v[232:235], v[176:179], v[156:159]\n\t" \
  "ds_read_b64_tr_b16 v[220:221], v255 offset:5152\n\t" \
  "v_mfma_f32_16x16x32_bf16 v[156:159], v[236:239], v[180:183], v[156:159]\n\t" \
  "ds_read_b64_tr_b16 v[222:223], v255 offset:6432\n\t" \
  "ds_read_b128 v[176:179], v253 offset:2048\n\t" \
  "ds_read_b128 v[180:183], v254 offset:2048\n\t" \
  "v_mfma_f32_16x16x32_bf16 v[160:163], v[232:235], v[184:187], v[160:163]\n\t" \
  "v_mfma_f32_16x16x32_bf16 v[160:163], v[236:239], v[188:191], v[160:163]\n\t" \
  "ds_read_b128 v[184:187], v253 offset:4096\n\t" \
  "ds_read_b128 v[188:191], v254 offset:4096\n\t" \
  "v_mfma_f32_16x16x32_bf16 v[164:167], v[232:235], v[192:195], v[164:167]\n\t" \
  "v_mfma_f32_16x16x32_bf16 v[164:167], v[236:239], v[196:199], v[164:167]\n\t" \
  "ds_read_b128 v[192:195], v253 offset:6144\n\t" \
  "ds_read_b128 v[196:199], v254 offset:6144\n\t" \
  "s_waitcnt lgkmcnt(9)\n\t" \
  "v_mfma_f32_16x16x32_bf16 v[8:11], v[200:203], v[168:171], v[8:11]\n\t" \
  "ds_read_b64_tr_b16 v[224:225], v255 offset:10272\n\t" \
  "s_waitcnt lgkmcnt(9)\n\t" \
  "v_mfma_f32_16x16x32_bf16 v[8:11], v[204:207], v[172:175], v[8:11]\n\t" \
  "ds_read_b64_tr_b16 v[226:227], v255 offset:11552\n\t" \
  "s_waitcnt lgkmcnt(7)\n\t" \
  "v_mfma_f32_16x16x32_bf16 v[12:15], v[200:203], v[176:179], v[12:15]\n\t" \
  "ds_read_b64_tr_b16 v[228:229], v255 offset:15392\n\t" \
  "s_waitcnt lgkmcnt(7)\n\t" \
  "v_mfma_f32_16x16x32_bf16 v[12:15], v[204:207], v[180:183], v[12:15]\n\t" \
  "ds_read_b64_tr_b16 v[230:231], v255 offset:16672\n\t" \
  "s_waitcnt lgkmcnt(7)\n\t" \
  "v_mfma_f32_16x16x32_bf16 v[16:19], v[200:203], v[184:187], v[16:19]\n\t" \
  "s_add_u32 m0, s88, 53248\n\t" \
  "s_waitcnt lgkmcnt(6)\n\t" \
  "v_mfma_f32_16x16x32_bf16 v[16:19], v[204:207], v[188:191], v[16:19]\n\t" \
  "buffer_load_dwordx4 v240, s[44:47], s52 offen offset:0 lds\n\t" \
  "s_waitcnt lgkmcnt(5)\n\t" \
  "v_mfma_f32_16x16x32_bf16 v[20:23], v[200:203], v[192:195], v[20:23]\n\t" \
  "s_waitcnt lgkmcnt(4)\n\t" \
  "v_mfma_f32_16x16x32_bf16 v[20:23], v[204:207], v[196:199], v[20:23]\n\t" \
  "buffer_load_dwordx4 v241, s[44:47], s52 offen offset:1024 lds\n\t" \
  "v_mfma_f32_16x16x32_bf16 v[24:27], v[208:211], v[168:171], v[24:27]\n\t" \
  "ds_read_b64_tr_b16 v[232:233], v255 offset:64\n\t" \
  "v_mfma_f32_16x16x32_bf16 v[24:27], v[212:215], v[172:175], v[24:27]\n\t" \
  "ds_read_b64_tr_b16 v[234:235], v255 offset:1344\n\t" \
  "v_mfma_f32_16x16x32_bf16 v[28:31], v[208:211], v[176:179], v[28:31]\n\t" \
  "ds_read_b64_tr_b16 v[236:237], v255 offset:5184\n\t" \
  "v_mfma_f32_16x16x32_bf16 v[28:31], v[212:215], v[180:183], v[28:31]\n\t" \
  "ds_read_b64_tr_b16 v[238:239], v255 offset:6464\n\t" \
  "v_mfma_f32_16x16x32_bf16 v[32:35], v[208:211], v[184:187], v[32:35]\n\t" \
  "v_mfma_f32_16x16x32_bf16 v[32:35], v[212:215], v[188:191], v[32:35]\n\t" \
  "buffer_load_dwordx4 v242, s[44:47], s52 offen offset:2048 lds\n\t" \
  "v_mfma_f32_16x16x32_bf16 v[36:39], v[208:211], v[192:195], v[36:39]\n\t" \
  "v_mfma_f32_16x16x32_bf16 v[36:39], v[212:215], v[196:199], v[36:39]\n\t" \
  "buffer_load_dwordx4 v243, s[44:47], s52 offen offset:3072 lds\n\t" \
  "v_mfma_f32_16x16x32_bf16 v[40:43], v[216:219], v[168:171], v[40:43]\n\t" \
  "ds_read_b64_tr_b16 v[200:201], v255 offset:10304\n\t" \
  "v_mfma_f32_16x16x32_bf16 v[40:43], v[220:223], v[172:175], v[40:43]\n\t" \
  "ds_read_b64_tr_b16 v[202:203], v255 offset:11584\n\t" \
  "v_mfma_f32_16x16x32_bf16 v[44:47], v[216:219], v[176:179], v[44:47]\n\t" \
  "ds_read_b64_tr_b16 v[204:205], v255 offset:15424\n\t" \
  "v_mfma_f32_16x16x32_bf16 v[44:47], v[220:223], v[180:183], v[44:47]\n\t" \
  "ds_read_b64_tr_b16 v[206:207], v255 offset:16704\n\t" \
  "v_mfma_f32_16x16x32_bf16 v[48:51], v[216:219], v[184:187], v[48:51]\n\t" \
  "s_add_u32 m0, s88, 57344\n\t" \
  "v_mfma_f32_16x16x32_bf16 v[48:51], v[220:223], v[188:191], v[48:51]\n\t" \
  "buffer_load_dwordx4 v244, s[44:47], s52 offen offset:0 lds\n\t" \
  "v_mfma_f32_16x16x32_bf16 v[52:55], v[216:219], v[192:195], v[52:55]\n\t" \
  "v_mfma_f32_16x16x32_bf16 v[52:55], v[220:223], v[196:199], v[52:55]\n\t" \
  "buffer_load_dwordx4 v245, s[44:47], s52 offen offset:1024 lds\n\t" \
  "s_waitcnt lgkmcnt(10)\n\t" \
  "v_mfma_f32_16x16x32_bf16 v[56:59], v[224:227], v[168:171], v[56:59]\n\t" \
  "ds_read_b64_tr_b16 v[208:209], v255 offset:96\n\t" \
  "s_waitcnt lgkmcnt(9)\n\t" \
  "v_mfma_f32_16x16x32_bf16 v[56:59], v[228:231], v[172:175], v[56:59]\n\t" \
  "ds_read_b64_tr_b16 v[210:211], v255 offset:1376\n\t" \
  "v_mfma_f32_16x16x32_bf16 v[60:63], v[224:227], v[176:179], v[60:63]\n\t" \
  "ds_read_b64_tr_b16 v[212:213], v255 offset:5216\n\t" \
  "v_mfma_f32_16x16x32_bf16 v[60:63], v[228:231], v[180:183], v[60:63]\n\t" \
  "ds_read_b64_tr_b16 v[214:215], v255 offset:6496\n\t" \
  "v_mfma_f32_16x16x32_bf16 v[64:67], v[224:227], v[184:187], v[64:67]\n\t" \
  "v_mfma_f32_16x16x32_bf16 v[64:67], v[228:231], v[188:191], v[64:67]\n\t" \
  "buffer_load_dwordx4 v246, s[44:47], s52 offen offset:2048 lds\n\t" \
  "v_mfma_f32_16x16x32_bf16 v[68:71], v[224:227], v[192:195], v[68:71]\n\t" \
  "v_mfma_f32_16x16x32_bf16 v[68:71], v[228:231], v[196:199], v[68:71]\n\t" \
  "buffer_load_dwordx4 v247, s[44:47], s52 offen offset:3072 lds\n\t" \
  "s_waitcnt lgkmcnt(10)\n\t" \
  "v_mfma_f32_16x16x32_bf16 v[72:75], v[232:235], v[168:171], v[72:75]\n\t" \
  "ds_read_b64_tr_b16 v[216:217], v255 offset:10336\n\t" \
  "s_waitcnt lgkmcnt(9)\n\t" \
  "v_mfma_f32_16x16x32_bf16 v[72:75], v[236:239], v[172:175], v[72:75]\n\t" \
  "ds_read_b64_tr_b16 v[218:219], v255 offset:11616\n\t" \
  "v_mfma_f32_16x16x32_bf16 v[76:79], v[232:235], v[176:179], v[76:79]\n\t" \
  "ds_read_b64_tr_b16 v[220:221], v255 offset:15456\n\t" \
  "v_mfma_f32_16x16x32_bf16 v[76:79], v[236:239], v[180:183], v[76:79]\n\t" \
  "ds_read_b64_tr_b16 v[222:223], v255 offset:16736\n\t" \
  "v_mfma_f32_16x16x32_bf16 v[80:83], v[232:235], v[184:187], v[80:83]\n\t" \
  "s_add_u32 m0, s89, 53248\n\t" \
  "v_mfma_f32_16x16x32_bf16 v[80:83], v[236:239], v[188:191], v[80:83]\n\t" \
  "buffer_load_dwordx4 v248, s[48:51], s53 offen offset:0 lds\n\t" \
  "v_mfma_f32_16x16x32_bf16 v[84:87], v[232:235], v[192:195], v[84:87]\n\t" \
  "v_mfma_f32_16x16x32_bf16 v[84:87], v[236:239], v[196:199], v[84:87]\n\t" \
  "buffer_load_dwordx4 v249, s[48:51], s53 offen offset:1024 lds\n\t" \
  "s_waitcnt lgkmcnt(10)\n\t" \
  "v_mfma_f32_16x16x32_bf16 v[88:91], v[200:203], v[168:171], v[88:91]\n\t" \
  "ds_read_b64_tr_b16 v[224:225], v255 offset:128\n\t" \
  "s_waitcnt lgkmcnt(9)\n\t" \
  "v_mfma_f32_16x16x32_bf16 v[88:91], v[204:207], v[172:175], v[88:91]\n\t" \
  "ds_read_b64_tr_b16 v[226:227], v255 offset:1408\n\t" \
  "v_mfma_f32_16x16x32_bf16 v[92:95], v[200:203], v[176:179], v[92:95]\n\t" \
  "ds_read_b64_tr_b16 v[228:229], v255 offset:5248\n\t" \
  "v_mfma_f32_16x16x32_bf16 v[92:95], v[204:207], v[180:183], v[92:95]\n\t" \
  "ds_read_b64_tr_b16 v[230:231], v255 offset:6528\n\t" \
  "v_mfma_f32_16x16x32_bf16 v[96:99], v[200:203], v[184:187], v[96:99]\n\t" \
  "v_mfma_f32_16x16x32_bf16 v[96:99], v[204:207], v[188:191], v[96:99]\n\t" \
  "buffer_load_dwordx4 v250, s[48:51], s53 offen offset:2048 lds\n\t" \
  "v_mfma_f32_16x16x32_bf16 v[100:103], v[200:203], v[192:195], v[100:103]\n\t" \
  "v_mfma_f32_16x16x32_bf16 v[100:103], v[204:207], v[196:199], v[100:103]\n\t" \
  "buffer_load_dwordx4 v251, s[48:51], s53 offen offset:3072 lds\n\t" \
  "s_waitcnt lgkmcnt(10)\n\t" \
  "v_mfma_f32_16x16x32_bf16 v[104:107], v[208:211], v[168:171], v[104:107]\n\t" \
  "ds_read_b64_tr_b16 v[232:233], v255 offset:10368\n\t" \
  "s_waitcnt lgkmcnt(9)\n\t" \
  "v_mfma_f32_16x16x32_bf16 v[104:107], v[212:215], v[172:175], v[104:107]\n\t" \
  "ds_read_b64_tr_b16 v[234:235], v255 offset:11648\n\t" \
  "v_mfma_f32_16x16x32_bf16 v[108:111], v[208:211], v[176:179], v[108:111]\n\t" \
  "ds_read_b64_tr_b16 v[236:237], v255 offset:15488\n\t" \
  "v_mfma_f32_16x16x32_bf16 v[108:111], v[212:215], v[180:183], v[108:111]\n\t" \
  "ds_read_b64_tr_b16 v[238:239], v255 offset:16768\n\t" \
  "v_mfma_f32_16x16x32_bf16 v[112:115], v[208:211], v[184:187], v[112:115]\n\t" \
  "s_add_u32 m0, s89, 57344\n\t" \
  "v_mfma_f32_16x16x32_bf16 v[112:115], v[212:215], v[188:191], v[112:115]\n\t" \
  "buffer_load_dwordx4 v252, s[48:51], s53 offen offset:0 lds\n\t" \
  "v_mfma_f32_16x16x32_bf16 v[116:119], v[208:211], v[192:195], v[116:119]\n\t" \
  "v_mfma_f32_16x16x32_bf16 v[116:119], v[212:215], v[196:199], v[116:119]\n\t" \
  "s_waitcnt vmcnt(13) lgkmcnt(0)\n\t" \
  "s_barrier\n\t" \
  "v_add_u32 v255, 4294860800, v255\n\t" \
  "v_mfma_f32_16x16x32_bf16 v[120:123], v[216:219], v[168:171], v[120:123]\n\t" \
  "ds_read_b64_tr_b16 v[200:201], v255 offset:0\n\t" \
  "v_mfma_f32_16x16x32_bf16 v[120:123], v[220:223], v[172:175], v[120:123]\n\t" \
  "ds_read_b64_tr_b16 v[202:203], v255 offset:1280\n\t" \
  "v_accvgpr_read_b32 v7, a240\n\t" \
  "s_nop 0\n\t" \
  "v_readfirstlane_b32 s74, v7\n\t" \
  "v_mfma_f32_16x16x32_bf16 v[124:127], v[216:219], v[176:179], v[124:127]\n\t" \
  "ds_read_b64_tr_b16 v[204:205], v255 offset:5120\n\t" \
  "v_mfma_f32_16x16x32_bf16 v[124:127], v[220:223], v[180:183], v[124:127]\n\t" \
  "ds_read_b64_tr_b16 v[206:207], v255 offset:6400\n\t" \
  "s_mul_i32 s74, s74, 1664525\n\t" \
  "s_add_u32 s74, s74, 1013904223\n\t" \
  "s_not_b32 s55, s74\n\t" \
  "v_mfma_f32_16x16x32_bf16 v[128:131], v[216:219], v[184:187], v[128:131]\n\t" \
  "v_accvgpr_write_b32 a0, s74\n\t" \
  "v_accvgpr_write_b32 a1, s74\n\t" \
  "v_mfma_f32_16x16x32_bf16 v[128:131], v[220:223], v[188:191], v[128:131]\n\t" \
  "v_accvgpr_write_b32 a2, s74\n\t" \
  "v_accvgpr_write_b32 a3, s74\n\t" \
  "v_mfma_f32_16x16x32_bf16 v[132:135], v[216:219], v[192:195], v[132:135]\n\t" \
  "v_mov_b32 v7, s55\n\t" \
  "s_mov_b64 exec, 0xff\n\t" \
  "buffer_store_dwordx4 a[0:3], v4, s[64:67], s71 offen sc1\n\t" \
  "s_mov_b64 exec, -1\n\t" \
  "v_mfma_f32_16x16x32_bf16 v[132:135], v[220:223], v[196:199], v[132:135]\n\t" \
  "v_mfma_f32_16x16x32_bf16 v[136:139], v[224:227], v[168:171], v[136:139]\n\t" \
  "ds_read_b64_tr_b16 v[208:209], v255 offset:10240\n\t" \
  "v_mfma_f32_16x16x32_bf16 v[136:139], v[228:231], v[172:175], v[136:139]\n\t" \
  "ds_read_b64_tr_b16 v[210:211], v255 offset:11520\n\t" \
  "s_add_u32 s54, s54, 1\n\t" \
  "s_cmp_eq_u32 s54, 10\n\t" \
  "s_cselect_b32 s54, 2, s54\n\t" \
  "v_mfma_f32_16x16x32_bf16 v[140:143], v[224:227], v[176:179], v[140:143]\n\t" \
  "ds_read_b64_tr_b16 v[212:213], v255 offset:15360\n\t" \
  "v_mfma_f32_16x16x32_bf16 v[140:143], v[228:231], v[180:183], v[140:143]\n\t" \
  "ds_read_b64_tr_b16 v[214:215], v255 offset:16640\n\t" \
  "s_lshl_b32 s52, s54, 7\n\t" \
  "s_add_u32 s52, s52, s42\n\t" \
  "v_mfma_f32_16x16x32_bf16 v[144:147], v[224:227], v[184:187], v[144:147]\n\t" \
  "s_mul_i32 s53, s54, 221184\n\t" \
  "s_add_u32 s53, s53, s43\n\t" \
  "v_mfma_f32_16x16x32_bf16 v[144:147], v[228:231], v[188:191], v[144:147]\n\t" \
  "v_mfma_f32_16x16x32_bf16 v[148:151], v[224:227], v[192:195], v[148:151]\n\t" \
  "v_add_u32 v253, 4294860800, v253\n\t" \
  "v_add_u32 v254, 4294860800, v254\n\t" \
  "v_mfma_f32_16x16x32_bf16 v[148:151], v[228:231], v[196:199], v[148:151]\n\t" \
  "v_mfma_f32_16x16x32_bf16 v[152:155], v[232:235], v[168:171], v[152:155]\n\t" \
  "ds_read_b64_tr_b16 v[216:217], v255 offset:32\n\t" \
  "v_mfma_f32_16x16x32_bf16 v[152:155], v[236:239], v[172:175], v[152:155]\n\t" \
  "ds_read_b64_tr_b16 v[218:219], v255 offset:1312\n\t" \
  "ds_read_b128 v[168:171], v253 offset:0\n\t" \
  "ds_read_b128 v[172:175], v254 offset:0\n\t" \
  "v_mfma_f32_16x16x32_bf16 v[156:159], v[232:235], v[176:179], v[156:159]\n\t" \
  "ds_read_b64_tr_b16 v[220:221], v255 offset:5152\n\t" \
  "v_mfma_f32_16x16x32_bf16 v[156:159], v[236:239], v[180:183], v[156:159]\n\t" \
  "ds_read_b64_tr_b16 v[222:223], v255 offset:6432\n\t" \
  "ds_read_b128 v[176:179], v253 offset:2048\n\t" \
  "ds_read_b128 v[180:183], v254 offset:2048\n\t" \
  "v_mfma_f32_16x16x32_bf16 v[160:163], v[232:235], v[184:187], v[160:163]\n\t" \
  "v_mfma_f32_16x16x32_bf16 v[160:163], v[236:239], v[188:191], v[160:163]\n\t" \
  "ds_read_b128 v[184:187], v253 offset:4096\n\t" \
  "ds_read_b128 v[188:191], v254 offset:4096\n\t" \
  "v_mfma_f32_16x16x32_bf16 v[164:167], v[232:235], v[192:195], v[164:167]\n\t" \
  "v_mfma_f32_16x16x32_bf16 v[164:167], v[236:239], v[196:199], v[164:167]\n\t" \
  "ds_read_b128 v[192:195], v253 offset:6144\n\t" \
  "ds_read_b128 v[196:199], v254 offset:6144\n\t" \
  "s_waitcnt lgkmcnt(9)\n\t" \
  "v_mfma_f32_16x16x32_bf16 v[8:11], v[200:203], v[168:171], v[8:11]\n\t" \
  "ds_read_b64_tr_b16 v[224:225], v255 offset:10272\n\t" \
  "s_waitcnt lgkmcnt(9)\n\t" \
  "v_mfma_f32_16x16x32_bf16 v[8:11], v[204:207], v[172:175], v[8:11]\n\t" \
  "ds_read_b64_tr_b16 v[226:227], v255 offset:11552\n\t" \
  "s_waitcnt lgkmcnt(7)\n\t" \
  "v_mfma_f32_16x16x32_bf16 v[12:15], v[200:203], v[176:179], v[12:15]\n\t" \
  "ds_read_b64_tr_b16 v[228:229], v255 offset:15392\n\t" \
  "s_waitcnt lgkmcnt(7)\n\t" \
  "v_mfma_f32_16x16x32_bf16 v[12:15], v[204:207], v[180:183], v[12:15]\n\t" \
  "ds_read_b64_tr_b16 v[230:231], v255 offset:16672\n\t" \
  "s_waitcnt lgkmcnt(7)\n\t" \
  "v_mfma_f32_16x16x32_bf16 v[16:19], v[200:203], v[184:187], v[16:19]\n\t" \
  "s_add_u32 m0, s88, 106496\n\t" \
  "s_waitcnt lgkmcnt(6)\n\t" \
  "v_mfma_f32_16x16x32_bf16 v[16:19], v[204:207], v[188:191], v[16:19]\n\t" \
  "buffer_load_dwordx4 v240, s[44:47], s52 offen offset:0 lds\n\t" \
  "s_waitcnt lgkmcnt(5)\n\t" \
  "v_mfma_f32_16x16x32_bf16 v[20:23], v[200:203], v[192:195], v[20:23]\n\t" \
  "s_waitcnt lgkmcnt(4)\n\t" \
  "v_mfma_f32_16x16x32_bf16 v[20:23], v[204:207], v[196:199], v[20:23]\n\t" \
  "buffer_load_dwordx4 v241, s[44:47], s52 offen offset:1024 lds\n\t" \
  "v_mfma_f32_16x16x32_bf16 v[24:27], v[208:211], v[168:171], v[24:27]\n\t" \
  "ds_read_b64_tr_b16 v[232:233], v255 offset:64\n\t" \
  "v_mfma_f32_16x16x32_bf16 v[24:27], v[212:215], v[172:175], v[24:27]\n\t" \
  "ds_read_b64_tr_b16 v[234:235], v255 offset:1344\n\t" \
  "v_mfma_f32_16x16x32_bf16 v[28:31], v[208:211], v[176:179], v[28:31]\n\t" \
  "ds_read_b64_tr_b16 v[236:237], v255 offset:5184\n\t" \
  "v_mfma_f32_16x16x32_bf16 v[28:31], v[212:215], v[180:183], v[28:31]\n\t" \
  "ds_read_b64_tr_b16 v[238:239], v255 offset:6464\n\t" \
  "v_mfma_f32_16x16x32_bf16 v[32:35], v[208:211], v[184:187], v[32:35]\n\t" \
  "v_mfma_f32_16x16x32_bf16 v[32:35], v[212:215], v[188:191], v[32:35]\n\t" \
  "buffer_load_dwordx4 v242, s[44:47], s52 offen offset:2048 lds\n\t" \
  "v_mfma_f32_16x16x32_bf16 v[36:39], v[208:211], v[192:195], v[36:39]\n\t" \
  "v_mfma_f32_16x16x32_bf16 v[36:39], v[212:215], v[196:199], v[36:39]\n\t" \
  "buffer_load_dwordx4 v243, s[44:47], s52 offen offset:3072 lds\n\t" \
  "v_mfma_f32_16x16x32_bf16 v[40:43], v[216:219], v[168:171], v[40:43]\n\t" \
  "ds_read_b64_tr_b16 v[200:201], v255 offset:10304\n\t" \
  "v_mfma_f32_16x16x32_bf16 v[40:43], v[220:223], v[172:175], v[40:43]\n\t" \
  "ds_read_b64_tr_b16 v[202:203], v255 offset:11584\n\t" \
  "v_mfma_f32_16x16x32_bf16 v[44:47], v[216:219], v[176:179], v[44:47]\n\t" \
  "ds_read_b64_tr_b16 v[204:205], v255 offset:15424\n\t" \
  "v_mfma_f32_16x16x32_bf16 v[44:47], v[220:223], v[180:183], v[44:47]\n\t" \
  "ds_read_b64_tr_b16 v[206:207], v255 offset:16704\n\t" \
  "v_mfma_f32_16x16x32_bf16 v[48:51], v[216:219], v[184:187], v[48:51]\n\t" \
  "s_add_u32 m0, s88, 110592\n\t" \
  "v_mfma_f32_16x16x32_bf16 v[48:51], v[220:223], v[188:191], v[48:51]\n\t" \
  "buffer_load_dwordx4 v244, s[44:47], s52 offen offset:0 lds\n\t" \
  "v_mfma_f32_16x16x32_bf16 v[52:55], v[216:219], v[192:195], v[52:55]\n\t" \
  "v_mfma_f32_16x16x32_bf16 v[52:55], v[220:223], v[196:199], v[52:55]\n\t" \
  "buffer_load_dwordx4 v245, s[44:47], s52 offen offset:1024 lds\n\t" \
  "s_waitcnt lgkmcnt(10)\n\t" \
  "v_mfma_f32_16x16x32_bf16 v[56:59], v[224:227], v[168:171], v[56:59]\n\t" \
  "ds_read_b64_tr_b16 v[208:209], v255 offset:96\n\t" \
  "s_waitcnt lgkmcnt(9)\n\t" \
  "v_mfma_f32_16x16x32_bf16 v[56:59], v[228:231], v[172:175], v[56:59]\n\t" \
  "ds_read_b64_tr_b16 v[210:211], v255 offset:1376\n\t" \
  "v_mfma_f32_16x16x32_bf16 v[60:63], v[224:227], v[176:179], v[60:63]\n\t" \
  "ds_read_b64_tr_b16 v[212:213], v255 offset:5216\n\t" \
  "v_mfma_f32_16x16x32_bf16 v[60:63], v[228:231], v[180:183], v[60:63]\n\t" \
  "ds_read_b64_tr_b16 v[214:215], v255 offset:6496\n\t" \
  "v_mfma_f32_16x16x32_bf16 v[64:67], v[224:227], v[184:187], v[64:67]\n\t" \
  "v_mfma_f32_16x16x32_bf16 v[64:67], v[228:231], v[188:191], v[64:67]\n\t" \
  "buffer_load_dwordx4 v246, s[44:47], s52 offen offset:2048 lds\n\t" \
  "v_mfma_f32_16x16x32_bf16 v[68:71], v[224:227], v[192:195], v[68:71]\n\t" \
  "v_mfma_f32_16x16x32_bf16 v[68:71], v[228:231], v[196:199], v[68:71]\n\t" \
  "buffer_load_dwordx4 v247, s[44:47], s52 offen offset:3072 lds\n\t" \
  "s_waitcnt lgkmcnt(10)\n\t" \
  "v_mfma_f32_16x16x32_bf16 v[72:75], v[232:235], v[168:171], v[72:75]\n\t" \
  "ds_read_b64_tr_b16 v[216:217], v255 offset:10336\n\t" \
  "s_waitcnt lgkmcnt(9)\n\t" \
  "v_mfma_f32_16x16x32_bf16 v[72:75], v[236:239], v[172:175], v[72:75]\n\t" \
  "ds_read_b64_tr_b16 v[218:219], v255 offset:11616\n\t" \
  "v_mfma_f32_16x16x32_bf16 v[76:79], v[232:235], v[176:179], v[76:79]\n\t" \
  "ds_read_b64_tr_b16 v[220:221], v255 offset:15456\n\t" \
  "v_mfma_f32_16x16x32_bf16 v[76:79], v[236:239], v[180:183], v[76:79]\n\t" \
  "ds_read_b64_tr_b16 v[222:223], v255 offset:16736\n\t" \
  "v_mfma_f32_16x16x32_bf16 v[80:83], v[232:235], v[184:187], v[80:83]\n\t" \
  "s_add_u32 m0, s89, 106496\n\t" \
  "v_mfma_f32_16x16x32_bf16 v[80:83], v[236:239], v[188:191], v[80:83]\n\t" \
  "buffer_load_dwordx4 v248, s[48:51], s53 offen offset:0 lds\n\t" \
  "v_mfma_f32_16x16x32_bf16 v[84:87], v[232:235], v[192:195], v[84:87]\n\t" \
  "v_mfma_f32_16x16x32_bf16 v[84:87], v[236:239], v[196:199], v[84:87]\n\t" \
  "buffer_load_dwordx4 v249, s[48:51], s53 offen offset:1024 lds\n\t" \
  "s_waitcnt lgkmcnt(10)\n\t" \
  "v_mfma_f32_16x16x32_bf16 v[88:91], v[200:203], v[168:171], v[88:91]\n\t" \
  "ds_read_b64_tr_b16 v[224:225], v255 offset:128\n\t" \
  "s_waitcnt lgkmcnt(9)\n\t" \
  "v_mfma_f32_16x16x32_bf16 v[88:91], v[204:207], v[172:175], v[88:91]\n\t" \
  "ds_read_b64_tr_b16 v[226:227], v255 offset:1408\n\t" \
  "v_mfma_f32_16x16x32_bf16 v[92:95], v[200:203], v[176:179], v[92:95]\n\t" \
  "ds_read_b64_tr_b16 v[228:229], v255 offset:5248\n\t" \
  "v_mfma_f32_16x16x32_bf16 v[92:95], v[204:207], v[180:183], v[92:95]\n\t" \
  "ds_read_b64_tr_b16 v[230:231], v255 offset:6528\n\t" \
  "v_mfma_f32_16x16x32_bf16 v[96:99], v[200:203], v[184:187], v[96:99]\n\t" \
  "v_mfma_f32_16x16x32_bf16 v[96:99], v[204:207], v[188:191], v[96:99]\n\t" \
  "buffer_load_dwordx4 v250, s[48:51], s53 offen offset:2048 lds\n\t" \
  "v_mfma_f32_16x16x32_bf16 v[100:103], v[200:203], v[192:195], v[100:103]\n\t" \
  "v_mfma_f32_16x16x32_bf16 v[100:103], v[204:207], v[196:199], v[100:103]\n\t" \
  "buffer_load_dwordx4 v251, s[48:51], s53 offen offset:3072 lds\n\t" \
  "s_waitcnt lgkmcnt(10)\n\t" \
  "v_mfma_f32_16x16x32_bf16 v[104:107], v[208:211], v[168:171], v[104:107]\n\t" \
  "ds_read_b64_tr_b16 v[232:233], v255 offset:10368\n\t" \
  "s_waitcnt lgkmcnt(9)\n\t" \
  "v_mfma_f32_16x16x32_bf16 v[104:107], v[212:215], v[172:175], v[104:107]\n\t" \
  "ds_read_b64_tr_b16 v[234:235], v255 offset:11648\n\t" \
  "v_mfma_f32_16x16x32_bf16 v[108:111], v[208:211], v[176:179], v[108:111]\n\t" \
  "ds_read_b64_tr_b16 v[236:237], v255 offset:15488\n\t" \
  "v_mfma_f32_16x16x32_bf16 v[108:111], v[212:215], v[180:183], v[108:111]\n\t" \
  "ds_read_b64_tr_b16 v[238:239], v255 offset:16768\n\t" \
  "v_mfma_f32_16x16x32_bf16 v[112:115], v[208:211], v[184:187], v[112:115]\n\t" \
  "s_add_u32 m0, s89, 110592\n\t" \
  "v_mfma_f32_16x16x32_bf16 v[112:115], v[212:215], v[188:191], v[112:115]\n\t" \
  "buffer_load_dwordx4 v252, s[48:51], s53 offen offset:0 lds\n\t" \
  "v_mfma_f32_16x16x32_bf16 v[116:119], v[208:211], v[192:195], v[116:119]\n\t" \
  "v_mfma_f32_16x16x32_bf16 v[116:119], v[212:215], v[196:199], v[116:119]\n\t" \
  "s_waitcnt vmcnt(14) lgkmcnt(0)\n\t" \
  "s_barrier\n\t" \
  "v_add_u32 v255, 53248, v255\n\t" \
  "v_mfma_f32_16x16x32_bf16 v[120:123], v[216:219], v[168:171], v[120:123]\n\t" \
  "ds_read_b64_tr_b16 v[200:201], v255 offset:0\n\t" \
  "v_mfma_f32_16x16x32_bf16 v[120:123], v[220:223], v[172:175], v[120:123]\n\t" \
  "ds_read_b64_tr_b16 v[202:203], v255 offset:1280\n\t" \
  "v_mfma_f32_16x16x32_bf16 v[124:127], v[216:219], v[176:179], v[124:127]\n\t" \
  "ds_read_b64_tr_b16 v[204:205], v255 offset:5120\n\t" \
  "v_mfma_f32_16x16x32_bf16 v[124:127], v[220:223], v[180:183], v[124:127]\n\t" \
  "ds_read_b64_tr_b16 v[206:207], v255 offset:6400\n\t" \
  "v_mfma_f32_16x16x32_bf16 v[128:131], v[216:219], v[184:187], v[128:131]\n\t" \
  "v_mfma_f32_16x16x32_bf16 v[128:131], v[220:223], v[188:191], v[128:131]\n\t" \
  "v_mfma_f32_16x16x32_bf16 v[132:135], v[216:219], v[192:195], v[132:135]\n\t" \
  "v_mfma_f32_16x16x32_bf16 v[132:135], v[220:223], v[196:199], v[132:135]\n\t" \
  "v_mfma_f32_16x16x32_bf16 v[136:139], v[224:227], v[168:171], v[136:139]\n\t" \
  "ds_read_b64_tr_b16 v[208:209], v255 offset:10240\n\t" \
  "v_mfma_f32_16x16x32_bf16 v[136:139], v[228:231], v[172:175], v[136:139]\n\t" \
  "ds_read_b64_tr_b16 v[210:211], v255 offset:11520\n\t" \
  "s_add_u32 s54, s54, 1\n\t" \
  "s_cmp_eq_u32 s54, 10\n\t" \
  "s_cselect_b32 s54, 2, s54\n\t" \
  "v_mfma_f32_16x16x32_bf16 v[140:143], v[224:227], v[176:179], v[140:143]\n\t" \
  "ds_read_b64_tr_b16 v[212:213], v255 offset:15360\n\t" \
  "v_mfma_f32_16x16x32_bf16 v[140:143], v[228:231], v[180:183], v[140:143]\n\t" \
  "ds_read_b64_tr_b16 v[214:215], v255 offset:16640\n\t" \
  "s_lshl_b32 s52, s54, 7\n\t" \
  "s_add_u32 s52, s52, s42\n\t" \
  "v_mfma_f32_16x16x32_bf16 v[144:147], v[224:227], v[184:187], v[144:147]\n\t" \
  "s_mul_i32 s53, s54, 221184\n\t" \
  "s_add_u32 s53, s53, s43\n\t" \
  "v_mfma_f32_16x16x32_bf16 v[144:147], v[228:231], v[188:191], v[144:147]\n\t" \
  "v_mfma_f32_16x16x32_bf16 v[148:151], v[224:227], v[192:195], v[148:151]\n\t" \
  "v_add_u32 v253, 53248, v253\n\t" \
  "v_add_u32 v254, 53248, v254\n\t" \
  "v_mfma_f32_16x16x32_bf16 v[148:151], v[228:231], v[196:199], v[148:151]\n\t" \
  "v_mfma_f32_16x16x32_bf16 v[152:155], v[232:235], v[168:171], v[152:155]\n\t" \
  "ds_read_b64_tr_b16 v[216:217], v255 offset:32\n\t" \
  "v_mfma_f32_16x16x32_bf16 v[152:155], v[236:239], v[172:175], v[152:155]\n\t" \
  "ds_read_b64_tr_b16 v[218:219], v255 offset:1312\n\t" \
  "ds_read_b128 v[168:171], v253 offset:0\n\t" \
  "ds_read_b128 v[172:175], v254 offset:0\n\t" \
  "v_mfma_f32_16x16x32_bf16 v[156:159], v[232:235], v[176:179], v[156:159]\n\t" \
  "ds_read_b64_tr_b16 v[220:221], v255 offset:5152\n\t" \
  "v_mfma_f32_16x16x32_bf16 v[156:159], v[236:239], v[180:183], v[156:159]\n\t" \
  "ds_read_b64_tr_b16 v[222:223], v255 offset:6432\n\t" \
  "ds_read_b128 v[176:179], v253 offset:2048\n\t" \
  "ds_read_b128 v[180:183], v254 offset:2048\n\t" \
  "v_mfma_f32_16x16x32_bf16 v[160:163], v[232:235], v[184:187], v[160:163]\n\t" \
  "v_mfma_f32_16x16x32_bf16 v[160:163], v[236:239], v[188:191], v[160:163]\n\t" \
  "ds_read_b128 v[184:187], v253 offset:4096\n\t" \
  "ds_read_b128 v[188:191], v254 offset:4096\n\t" \
  "v_mfma_f32_16x16x32_bf16 v[164:167], v[232:235], v[192:195], v[164:167]\n\t" \
  "v_mfma_f32_16x16x32_bf16 v[164:167], v[236:239], v[196:199], v[164:167]\n\t" \
  "ds_read_b128 v[192:195], v253 offset:6144\n\t" \
  "ds_read_b128 v[196:199], v254 offset:6144\n\t" \
  "s_waitcnt lgkmcnt(9)\n\t" \
  "v_mfma_f32_16x16x32_bf16 v[8:11], v[200:203], v[168:171], v[8:11]\n\t" \
  "ds_read_b64_tr_b16 v[224:225], v255 offset:10272\n\t" \
  "s_waitcnt lgkmcnt(9)\n\t" \
  "v_mfma_f32_16x16x32_bf16 v[8:11], v[204:207], v[172:175], v[8:11]\n\t" \
  "ds_read_b64_tr_b16 v[226:227], v255 offset:11552\n\t" \
  "s_waitcnt lgkmcnt(7)\n\t" \
  "v_mfma_f32_16x16x32_bf16 v[12:15], v[200:203], v[176:179], v[12:15]\n\t" \
  "ds_read_b64_tr_b16 v[228:229], v255 offset:15392\n\t" \
  "s_waitcnt lgkmcnt(7)\n\t" \
  "v_mfma_f32_16x16x32_bf16 v[12:15], v[204:207], v[180:183], v[12:15]\n\t" \
  "ds_read_b64_tr_b16 v[230:231], v255 offset:16672\n\t" \
  "s_waitcnt lgkmcnt(7)\n\t" \
  "v_mfma_f32_16x16x32_bf16 v[16:19], v[200:203], v[184:187], v[16:19]\n\t" \
  "s_add_u32 m0, s88, 0\n\t" \
  "s_waitcnt lgkmcnt(6)\n\t" \
  "v_mfma_f32_16x16x32_bf16 v[16:19], v[204:207], v[188:191], v[16:19]\n\t" \
  "buffer_load_dwordx4 v240, s[44:47], s52 offen offset:0 lds\n\t" \
  "s_waitcnt lgkmcnt(5)\n\t" \
  "v_mfma_f32_16x16x32_bf16 v[20:23], v[200:203], v[192:195], v[20:23]\n\t" \
  "s_waitcnt lgkmcnt(4)\n\t" \
  "v_mfma_f32_16x16x32_bf16 v[20:23], v[204:207], v[196:199], v[20:23]\n\t" \
  "buffer_load_dwordx4 v241, s[44:47], s52 offen offset:1024 lds\n\t" \
  "v_mfma_f32_16x16x32_bf16 v[24:27], v[208:211], v[168:171], v[24:27]\n\t" \
  "ds_read_b64_tr_b16 v[232:233], v255 offset:64\n\t" \
  "v_mfma_f32_16x16x32_bf16 v[24:27], v[212:215], v[172:175], v[24:27]\n\t" \
  "ds_read_b64_tr_b16 v[234:235], v255 offset:1344\n\t" \
  "v_mfma_f32_16x16x32_bf16 v[28:31], v[208:211], v[176:179], v[28:31]\n\t" \
  "ds_read_b64_tr_b16 v[236:237], v255 offset:5184\n\t" \
  "v_mfma_f32_16x16x32_bf16 v[28:31], v[212:215], v[180:183], v[28:31]\n\t" \
  "ds_read_b64_tr_b16 v[238:239], v255 offset:6464\n\t" \
  "v_mfma_f32_16x16x32_bf16 v[32:35], v[208:211], v[184:187], v[32:35]\n\t" \
  "v_mfma_f32_16x16x32_bf16 v[32:35], v[212:215], v[188:191], v[32:35]\n\t" \
  "buffer_load_dwordx4 v242, s[44:47], s52 offen offset:2048 lds\n\t" \
  "v_mfma_f32_16x16x32_bf16 v[36:39], v[208:211], v[192:195], v[36:39]\n\t" \
  "v_mfma_f32_16x16x32_bf16 v[36:39], v[212:215], v[196:199], v[36:39]\n\t" \
  "buffer_load_dwordx4 v243, s[44:47], s52 offen offset:3072 lds\n\t" \
  "v_mfma_f32_16x16x32_bf16 v[40:43], v[216:219], v[168:171], v[40:43]\n\t" \
  "ds_read_b64_tr_b16 v[200:201], v255 offset:10304\n\t" \
  "v_mfma_f32_16x16x32_bf16 v[40:43], v[220:223], v[172:175], v[40:43]\n\t" \
  "ds_read_b64_tr_b16 v[202:203], v255 offset:11584\n\t" \
  "v_mfma_f32_16x16x32_bf16 v[44:47], v[216:219], v[176:179], v[44:47]\n\t" \
  "ds_read_b64_tr_b16 v[204:205], v255 offset:15424\n\t" \
  "v_mfma_f32_16x16x32_bf16 v[44:47], v[220:223], v[180:183], v[44:47]\n\t" \
  "ds_read_b64_tr_b16 v[206:207], v255 offset:16704\n\t" \
  "v_mfma_f32_16x16x32_bf16 v[48:51], v[216:219], v[184:187], v[48:51]\n\t" \
  "s_add_u32 m0, s88, 4096\n\t" \
  "v_mfma_f32_16x16x32_bf16 v[48:51], v[220:223], v[188:191], v[48:51]\n\t" \
  "buffer_load_dwordx4 v244, s[44:47], s52 offen offset:0 lds\n\t" \
  "v_mfma_f32_16x16x32_bf16 v[52:55], v[216:219], v[192:195], v[52:55]\n\t" \
  "v_mfma_f32_16x16x32_bf16 v[52:55], v[220:223], v[196:199], v[52:55]\n\t" \
  "buffer_load_dwordx4 v245, s[44:47], s52 offen offset:1024 lds\n\t" \
  "s_waitcnt lgkmcnt(10)\n\t" \
  "v_mfma_f32_16x16x32_bf16 v[56:59], v[224:227], v[168:171], v[56:59]\n\t" \
  "ds_read_b64_tr_b16 v[208:209], v255 offset:96\n\t" \
  "s_waitcnt lgkmcnt(9)\n\t" \
  "v_mfma_f32_16x16x32_bf16 v[56:59], v[228:231], v[172:175], v[56:59]\n\t" \
  "ds_read_b64_tr_b16 v[210:211], v255 offset:1376\n\t" \
  "v_mfma_f32_16x16x32_bf16 v[60:63], v[224:227], v[176:179], v[60:63]\n\t" \
  "ds_read_b64_tr_b16 v[212:213], v255 offset:5216\n\t" \
  "v_mfma_f32_16x16x32_bf16 v[60:63], v[228:231], v[180:183], v[60:63]\n\t" \
  "ds_read_b64_tr_b16 v[214:215], v255 offset:6496\n\t" \
  "v_mfma_f32_16x16x32_bf16 v[64:67], v[224:227], v[184:187], v[64:67]\n\t" \
  "v_mfma_f32_16x16x32_bf16 v[64:67], v[228:231], v[188:191], v[64:67]\n\t" \
  "buffer_load_dwordx4 v246, s[44:47], s52 offen offset:2048 lds\n\t" \
  "v_mfma_f32_16x16x32_bf16 v[68:71], v[224:227], v[192:195], v[68:71]\n\t" \
  "v_mfma_f32_16x16x32_bf16 v[68:71], v[228:231], v[196:199], v[68:71]\n\t" \
  "buffer_load_dwordx4 v247, s[44:47], s52 offen offset:3072 lds\n\t" \
  "s_waitcnt lgkmcnt(10)\n\t" \
  "v_mfma_f32_16x16x32_bf16 v[72:75], v[232:235], v[168:171], v[72:75]\n\t" \
  "ds_read_b64_tr_b16 v[216:217], v255 offset:10336\n\t" \
  "s_waitcnt lgkmcnt(9)\n\t" \
  "v_mfma_f32_16x16x32_bf16 v[72:75], v[236:239], v[172:175], v[72:75]\n\t" \
  "ds_read_b64_tr_b16 v[218:219], v255 offset:11616\n\t" \
  "v_mfma_f32_16x16x32_bf16 v[76:79], v[232:235], v[176:179], v[76:79]\n\t" \
  "ds_read_b64_tr_b16 v[220:221], v255 offset:15456\n\t" \
  "buffer_load_dword v7, v6, s[64:67], 0 offen sc1\n\t" \
  "v_mfma_f32_16x16x32_bf16 v[76:79], v[236:239], v[180:183], v[76:79]\n\t" \
  "ds_read_b64_tr_b16 v[222:223], v255 offset:16736\n\t" \
  "v_mfma_f32_16x16x32_bf16 v[80:83], v[232:235], v[184:187], v[80:83]\n\t" \
  "s_add_u32 m0, s89, 0\n\t" \
  "v_mfma_f32_16x16x32_bf16 v[80:83], v[236:239], v[188:191], v[80:83]\n\t" \
  "buffer_load_dwordx4 v248, s[48:51], s53 offen offset:0 lds\n\t" \
  "v_mfma_f32_16x16x32_bf16 v[84:87], v[232:235], v[192:195], v[84:87]\n\t" \
  "v_mfma_f32_16x16x32_bf16 v[84:87], v[236:239], v[196:199], v[84:87]\n\t" \
  "buffer_load_dwordx4 v249, s[48:51], s53 offen offset:1024 lds\n\t" \
  "s_waitcnt lgkmcnt(10)\n\t" \
  "v_mfma_f32_16x16x32_bf16 v[88:91], v[200:203], v[168:171], v[88:91]\n\t" \
  "ds_read_b64_tr_b16 v[224:225], v255 offset:128\n\t" \
  "s_waitcnt lgkmcnt(9)\n\t" \
  "v_mfma_f32_16x16x32_bf16 v[88:91], v[204:207], v[172:175], v[88:91]\n\t" \
  "ds_read_b64_tr_b16 v[226:227], v255 offset:1408\n\t" \
  "v_mfma_f32_16x16x32_bf16 v[92:95], v[200:203], v[176:179], v[92:95]\n\t" \
  "ds_read_b64_tr_b16 v[228:229], v255 offset:5248\n\t" \
  "v_mfma_f32_16x16x32_bf16 v[92:95], v[204:207], v[180:183], v[92:95]\n\t" \
  "ds_read_b64_tr_b16 v[230:231], v255 offset:6528\n\t" \
  "v_mfma_f32_16x16x32_bf16 v[96:99], v[200:203], v[184:187], v[96:99]\n\t" \
  "v_mfma_f32_16x16x32_bf16 v[96:99], v[204:207], v[188:191], v[96:99]\n\t" \
  "buffer_load_dwordx4 v250, s[48:51], s53 offen offset:2048 lds\n\t" \
  "v_mfma_f32_16x16x32_bf16 v[100:103], v[200:203], v[192:195], v[100:103]\n\t" \
  "v_mfma_f32_16x16x32_bf16 v[100:103], v[204:207], v[196:199], v[100:103]\n\t" \
  "buffer_load_dwordx4 v251, s[48:51], s53 offen offset:3072 lds\n\t" \
  "s_waitcnt lgkmcnt(10)\n\t" \
  "v_mfma_f32_16x16x32_bf16 v[104:107], v[208:211], v[168:171], v[104:107]\n\t" \
  "ds_read_b64_tr_b16 v[232:233], v255 offset:10368\n\t" \
  "s_waitcnt lgkmcnt(9)\n\t" \
  "v_mfma_f32_16x16x32_bf16 v[104:107], v[212:215], v[172:175], v[104:107]\n\t" \
  "ds_read_b64_tr_b16 v[234:235], v255 offset:11648\n\t" \
  "v_mfma_f32_16x16x32_bf16 v[108:111], v[208:211], v[176:179], v[108:111]\n\t" \
  "ds_read_b64_tr_b16 v[236:237], v255 offset:15488\n\t" \
  "v_mfma_f32_16x16x32_bf16 v[108:111], v[212:215], v[180:183], v[108:111]\n\t" \
  "ds_read_b64_tr_b16 v[238:239], v255 offset:16768\n\t" \
  "v_mfma_f32_16x16x32_bf16 v[112:115], v[208:211], v[184:187], v[112:115]\n\t" \
  "s_add_u32 m0, s89, 4096\n\t" \
  "v_mfma_f32_16x16x32_bf16 v[112:115], v[212:215], v[188:191], v[112:115]\n\t" \
  "buffer_load_dwordx4 v252, s[48:51], s53 offen offset:0 lds\n\t" \
  "v_mfma_f32_16x16x32_bf16 v[116:119], v[208:211], v[192:195], v[116:119]\n\t" \
  "v_mfma_f32_16x16x32_bf16 v[116:119], v[212:215], v[196:199], v[116:119]\n\t" \
  "s_waitcnt vmcnt(14) lgkmcnt(0)\n\t" \
  "s_barrier\n\t" \
  "v_add_u32 v255, 53248, v255\n\t" \
  "v_mfma_f32_16x16x32_bf16 v[120:123], v[216:219], v[168:171], v[120:123]\n\t" \
  "ds_read_b64_tr_b16 v[200:201], v255 offset:0\n\t" \
  "v_mfma_f32_16x16x32_bf16 v[120:123], v[220:223], v[172:175], v[120:123]\n\t" \
  "ds_read_b64_tr_b16 v[202:203], v255 offset:1280\n\t" \
  "v_mfma_f32_16x16x32_bf16 v[124:127], v[216:219], v[176:179], v[124:127]\n\t" \
  "ds_read_b64_tr_b16 v[204:205], v255 offset:5120\n\t" \
  "v_mfma_f32_16x16x32_bf16 v[124:127], v[220:223], v[180:183], v[124:127]\n\t" \
  "ds_read_b64_tr_b16 v[206:207], v255 offset:6400\n\t" \
  "v_mfma_f32_16x16x32_bf16 v[128:131], v[216:219], v[184:187], v[128:131]\n\t" \
  "v_mfma_f32_16x16x32_bf16 v[128:131], v[220:223], v[188:191], v[128:131]\n\t" \
  "v_mfma_f32_16x16x32_bf16 v[132:135], v[216:219], v[192:195], v[132:135]\n\t" \
  "v_mfma_f32_16x16x32_bf16 v[132:135], v[220:223], v[196:199], v[132:135]\n\t" \
  "v_mfma_f32_16x16x32_bf16 v[136:139], v[224:227], v[168:171], v[136:139]\n\t" \
  "ds_read_b64_tr_b16 v[208:209], v255 offset:10240\n\t" \
  "v_mfma_f32_16x16x32_bf16 v[136:139], v[228:231], v[172:175], v[136:139]\n\t" \
  "ds_read_b64_tr_b16 v[210:211], v255 offset:11520\n\t" \
  "s_add_u32 s54, s54, 1\n\t" \
  "s_cmp_eq_u32 s54, 10\n\t" \
  "s_cselect_b32 s54, 2, s54\n\t" \
  "v_mfma_f32_16x16x32_bf16 v[140:143], v[224:227], v[176:179], v[140:143]\n\t" \
  "ds_read_b64_tr_b16 v[212:213], v255 offset:15360\n\t" \
  "v_mfma_f32_16x16x32_bf16 v[140:143], v[228:231], v[180:183], v[140:143]\n\t" \
  "ds_read_b64_tr_b16 v[214:215], v255 offset:16640\n\t" \
  "s_lshl_b32 s52, s54, 7\n\t" \
  "s_add_u32 s52, s52, s42\n\t" \
  "v_mfma_f32_16x16x32_bf16 v[144:147], v[224:227], v[184:187], v[144:147]\n\t" \
  "s_mul_i32 s53, s54, 221184\n\t" \
  "s_add_u32 s53, s53, s43\n\t" \
  "v_mfma_f32_16x16x32_bf16 v[144:147], v[228:231], v[188:191], v[144:147]\n\t" \
  "v_mfma_f32_16x16x32_bf16 v[148:151], v[224:227], v[192:195], v[148:151]\n\t" \
  "v_add_u32 v253, 53248, v253\n\t" \
  "v_add_u32 v254, 53248, v254\n\t" \
  "v_mfma_f32_16x16x32_bf16 v[148:151], v[228:231], v[196:199], v[148:151]\n\t" \
  "v_mfma_f32_16x16x32_bf16 v[152:155], v[232:235], v[168:171], v[152:155]\n\t" \
  "ds_read_b64_tr_b16 v[216:217], v255 offset:32\n\t" \
  "v_mfma_f32_16x16x32_bf16 v[152:155], v[236:239], v[172:175], v[152:155]\n\t" \
  "ds_read_b64_tr_b16 v[218:219], v255 offset:1312\n\t" \
  "ds_read_b128 v[168:171], v253 offset:0\n\t" \
  "ds_read_b128 v[172:175], v254 offset:0\n\t" \
  "v_mfma_f32_16x16x32_bf16 v[156:159], v[232:235], v[176:179], v[156:159]\n\t" \
  "ds_read_b64_tr_b16 v[220:221], v255 offset:5152\n\t" \
  "v_mfma_f32_16x16x32_bf16 v[156:159], v[236:239], v[180:183], v[156:159]\n\t" \
  "ds_read_b64_tr_b16 v[222:223], v255 offset:6432\n\t" \
  "ds_read_b128 v[176:179], v253 offset:2048\n\t" \
  "ds_read_b128 v[180:183], v254 offset:2048\n\t" \
  "v_mfma_f32_16x16x32_bf16 v[160:163], v[232:235], v[184:187], v[160:163]\n\t" \
  "v_mfma_f32_16x16x32_bf16 v[160:163], v[236:239], v[188:191], v[160:163]\n\t" \
  "ds_read_b128 v[184:187], v253 offset:4096\n\t" \
  "ds_read_b128 v[188:191], v254 offset:4096\n\t" \
  "v_mfma_f32_16x16x32_bf16 v[164:167], v[232:235], v[192:195], v[164:167]\n\t" \
  "v_mfma_f32_16x16x32_bf16 v[164:167], v[236:239], v[196:199], v[164:167]\n\t" \
  "ds_read_b128 v[192:195], v253 offset:6144\n\t" \
  "ds_read_b128 v[196:199], v254 offset:6144\n\t" \
  "v_cmp_ne_u32 vcc, s74, v7\n\t" \
  "v_add_u32 v3, 4096, v4\n\t" \
  "s_nop 3\n\t" \
  "s_cbranch_vccz P1ffn_WX_%=\n\t" \
  "P1ffn_W_%=:\n\t" \
  "buffer_load_dword v7, v6, s[64:67], 0 offen sc1\n\t" \
  "s_sleep 4\n\t" \
  "s_add_u32 s75, s75, 1\n\t" \
  "s_cmp_gt_u32 s75, 1048576\n\t" \
  "s_cbranch_scc1 P1ffn_WX_%=\n\t" \
  "v_cmp_ne_u32 vcc, s74, v7\n\t" \
  "s_nop 4\n\t" \
  "s_cbranch_vccnz P1ffn_W_%=\n\t" \
  "P1ffn_WX_%=:\n\t" \
  "s_waitcnt lgkmcnt(9)\n\t" \
  "v_mfma_f32_16x16x32_bf16 v[8:11], v[200:203], v[168:171], v[8:11]\n\t" \
  "ds_read_b64_tr_b16 v[224:225], v255 offset:10272\n\t" \
  "s_waitcnt lgkmcnt(9)\n\t" \
  "v_mfma_f32_16x16x32_bf16 v[8:11], v[204:207], v[172:175], v[8:11]\n\t" \
  "ds_read_b64_tr_b16 v[226:227], v255 offset:11552\n\t" \
  "s_waitcnt lgkmcnt(7)\n\t" \
  "v_mfma_f32_16x16x32_bf16 v[12:15], v[200:203], v[176:179], v[12:15]\n\t" \
  "ds_read_b64_tr_b16 v[228:229], v255 offset:15392\n\t" \
  "s_waitcnt lgkmcnt(7)\n\t" \
  "v_mfma_f32_16x16x32_bf16 v[12:15], v[204:207], v[180:183], v[12:15]\n\t" \
  "ds_read_b64_tr_b16 v[230:231], v255 offset:16672\n\t" \
  "s_waitcnt lgkmcnt(7)\n\t" \
  "v_mfma_f32_16x16x32_bf16 v[16:19], v[200:203], v[184:187], v[16:19]\n\t" \
  "s_add_u32 m0, s88, 53248\n\t" \
  "s_waitcnt lgkmcnt(6)\n\t" \
  "v_mfma_f32_16x16x32_bf16 v[16:19], v[204:207], v[188:191], v[16:19]\n\t" \
  "buffer_load_dwordx4 v240, s[44:47], s52 offen offset:0 lds\n\t" \
  "s_waitcnt lgkmcnt(5)\n\t" \
  "v_mfma_f32_16x16x32_bf16 v[20:23], v[200:203], v[192:195], v[20:23]\n\t" \
  "s_waitcnt lgkmcnt(4)\n\t" \
  "v_mfma_f32_16x16x32_bf16 v[20:23], v[204:207], v[196:199], v[20:23]\n\t" \
  "buffer_load_dwordx4 v241, s[44:47], s52 offen offset:1024 lds\n\t" \
  "v_mfma_f32_16x16x32_bf16 v[24:27], v[208:211], v[168:171], v[24:27]\n\t" \
  "ds_read_b64_tr_b16 v[232:233], v255 offset:64\n\t" \
  "v_mfma_f32_16x16x32_bf16 v[24:27], v[212:215], v[172:175], v[24:27]\n\t" \
  "ds_read_b64_tr_b16 v[234:235], v255 offset:1344\n\t" \
  "v_mfma_f32_16x16x32_bf16 v[28:31], v[208:211], v[176:179], v[28:31]\n\t" \
  "ds_read_b64_tr_b16 v[236:237], v255 offset:5184\n\t" \
  "v_mfma_f32_16x16x32_bf16 v[28:31], v[212:215], v[180:183], v[28:31]\n\t" \
  "ds_read_b64_tr_b16 v[238:239], v255 offset:6464\n\t" \
  "v_mfma_f32_16x16x32_bf16 v[32:35], v[208:211], v[184:187], v[32:35]\n\t" \
  "v_mfma_f32_16x16x32_bf16 v[32:35], v[212:215], v[188:191], v[32:35]\n\t" \
  "buffer_load_dwordx4 v242, s[44:47], s52 offen offset:2048 lds\n\t" \
  "v_mfma_f32_16x16x32_bf16 v[36:39], v[208:211], v[192:195], v[36:39]\n\t" \
  "v_mfma_f32_16x16x32_bf16 v[36:39], v[212:215], v[196:199], v[36:39]\n\t" \
  "buffer_load_dwordx4 v243, s[44:47], s52 offen offset:3072 lds\n\t" \
  "v_mfma_f32_16x16x32_bf16 v[40:43], v[216:219], v[168:171], v[40:43]\n\t" \
  "ds_read_b64_tr_b16 v[200:201], v255 offset:10304\n\t" \
  "v_mfma_f32_16x16x32_bf16 v[40:43], v[220:223], v[172:175], v[40:43]\n\t" \
  "ds_read_b64_tr_b16 v[202:203], v255 offset:11584\n\t" \
  "v_mfma_f32_16x16x32_bf16 v[44:47], v[216:219], v[176:179], v[44:47]\n\t" \
  "ds_read_b64_tr_b16 v[204:205], v255 offset:15424\n\t" \
  "v_mfma_f32_16x16x32_bf16 v[44:47], v[220:223], v[180:183], v[44:47]\n\t" \
  "ds_read_b64_tr_b16 v[206:207], v255 offset:16704\n\t" \
  "v_mfma_f32_16x16x32_bf16 v[48:51], v[216:219], v[184:187], v[48:51]\n\t" \
  "s_add_u32 m0, s88, 57344\n\t" \
  "v_mfma_f32_16x16x32_bf16 v[48:51], v[220:223], v[188:191], v[48:51]\n\t" \
  "buffer_load_dwordx4 v244, s[44:47], s52 offen offset:0 lds\n\t" \
  "v_mfma_f32_16x16x32_bf16 v[52:55], v[216:219], v[192:195], v[52:55]\n\t" \
  "v_mfma_f32_16x16x32_bf16 v[52:55], v[220:223], v[196:199], v[52:55]\n\t" \
  "buffer_load_dwordx4 v245, s[44:47], s52 offen offset:1024 lds\n\t" \
  "s_waitcnt lgkmcnt(10)\n\t" \
  "v_mfma_f32_16x16x32_bf16 v[56:59], v[224:227], v[168:171], v[56:59]\n\t" \
  "ds_read_b64_tr_b16 v[208:209], v255 offset:96\n\t" \
  "s_waitcnt lgkmcnt(9)\n\t" \
  "v_mfma_f32_16x16x32_bf16 v[56:59], v[228:231], v[172:175], v[56:59]\n\t" \
  "ds_read_b64_tr_b16 v[210:211], v255 offset:1376\n\t" \
  "v_mfma_f32_16x16x32_bf16 v[60:63], v[224:227], v[176:179], v[60:63]\n\t" \
  "ds_read_b64_tr_b16 v[212:213], v255 offset:5216\n\t" \
  "v_mfma_f32_16x16x32_bf16 v[60:63], v[228:231], v[180:183], v[60:63]\n\t" \
  "ds_read_b64_tr_b16 v[214:215], v255 offset:6496\n\t" \
  "v_mfma_f32_16x16x32_bf16 v[64:67], v[224:227], v[184:187], v[64:67]\n\t" \
  "v_mfma_f32_16x16x32_bf16 v[64:67], v[228:231], v[188:191], v[64:67]\n\t" \
  "buffer_load_dwordx4 v246, s[44:47], s52 offen offset:2048 lds\n\t" \
  "v_mfma_f32_16x16x32_bf16 v[68:71], v[224:227], v[192:195], v[68:71]\n\t" \
  "v_mfma_f32_16x16x32_bf16 v[68:71], v[228:231], v[196:199], v[68:71]\n\t" \
  "buffer_load_dwordx4 v247, s[44:47], s52 offen offset:3072 lds\n\t" \
  "s_waitcnt lgkmcnt(10)\n\t" \
  "v_mfma_f32_16x16x32_bf16 v[72:75], v[232:235], v[168:171], v[72:75]\n\t" \
  "ds_read_b64_tr_b16 v[216:217], v255 offset:10336\n\t" \
  "s_waitcnt lgkmcnt(9)\n\t" \
  "v_mfma_f32_16x16x32_bf16 v[72:75], v[236:239], v[172:175], v[72:75]\n\t" \
  "ds_read_b64_tr_b16 v[218:219], v255 offset:11616\n\t" \
  "v_mfma_f32_16x16x32_bf16 v[76:79], v[232:235], v[176:179], v[76:79]\n\t" \
  "ds_read_b64_tr_b16 v[220:221], v255 offset:15456\n\t" \
  "v_mfma_f32_16x16x32_bf16 v[76:79], v[236:239], v[180:183], v[76:79]\n\t" \
  "ds_read_b64_tr_b16 v[222:223], v255 offset:16736\n\t" \
  "v_mfma_f32_16x16x32_bf16 v[80:83], v[232:235], v[184:187], v[80:83]\n\t" \
  "s_add_u32 m0, s89, 53248\n\t" \
  "v_mfma_f32_16x16x32_bf16 v[80:83], v[236:239], v[188:191], v[80:83]\n\t" \
  "buffer_load_dwordx4 v248, s[48:51], s53 offen offset:0 lds\n\t" \
  "v_mfma_f32_16x16x32_bf16 v[84:87], v[232:235], v[192:195], v[84:87]\n\t" \
  "v_mfma_f32_16x16x32_bf16 v[84:87], v[236:239], v[196:199], v[84:87]\n\t" \
  "buffer_load_dwordx4 v249, s[48:51], s53 offen offset:1024 lds\n\t" \
  "s_waitcnt lgkmcnt(10)\n\t" \
  "v_mfma_f32_16x16x32_bf16 v[88:91], v[200:203], v[168:171], v[88:91]\n\t" \
  "ds_read_b64_tr_b16 v[224:225], v255 offset:128\n\t" \
  "s_waitcnt lgkmcnt(9)\n\t" \
  "v_mfma_f32_16x16x32_bf16 v[88:91], v[204:207], v[172:175], v[88:91]\n\t" \
  "ds_read_b64_tr_b16 v[226:227], v255 offset:1408\n\t" \
  "v_mfma_f32_16x16x32_bf16 v[92:95], v[200:203], v[176:179], v[92:95]\n\t" \
  "ds_read_b64_tr_b16 v[228:229], v255 offset:5248\n\t" \
  "v_mfma_f32_16x16x32_bf16 v[92:95], v[204:207], v[180:183], v[92:95]\n\t" \
  "ds_read_b64_tr_b16 v[230:231], v255 offset:6528\n\t" \
  "v_mfma_f32_16x16x32_bf16 v[96:99], v[200:203], v[184:187], v[96:99]\n\t" \
  "v_mfma_f32_16x16x32_bf16 v[96:99], v[204:207], v[188:191], v[96:99]\n\t" \
  "buffer_load_dwordx4 v250, s[48:51], s53 offen offset:2048 lds\n\t" \
  "v_mfma_f32_16x16x32_bf16 v[100:103], v[200:203], v[192:195], v[100:103]\n\t" \
  "v_mfma_f32_16x16x32_bf16 v[100:103], v[204:207], v[196:199], v[100:103]\n\t" \
  "buffer_load_dwordx4 v251, s[48:51], s53 offen offset:3072 lds\n\t" \
  "s_waitcnt lgkmcnt(10)\n\t" \
  "v_mfma_f32_16x16x32_bf16 v[104:107], v[208:211], v[168:171], v[104:107]\n\t" \
  "ds_read_b64_tr_b16 v[232:233], v255 offset:10368\n\t" \
  "s_waitcnt lgkmcnt(9)\n\t" \
  "v_mfma_f32_16x16x32_bf16 v[104:107], v[212:215], v[172:175], v[104:107]\n\t" \
  "ds_read_b64_tr_b16 v[234:235], v255 offset:11648\n\t" \
  "v_mfma_f32_16x16x32_bf16 v[108:111], v[208:211], v[176:179], v[108:111]\n\t" \
  "ds_read_b64_tr_b16 v[236:237], v255 offset:15488\n\t" \
  "v_mfma_f32_16x16x32_bf16 v[108:111], v[212:215], v[180:183], v[108:111]\n\t" \
  "ds_read_b64_tr_b16 v[238:239], v255 offset:16768\n\t" \
  "v_mfma_f32_16x16x32_bf16 v[112:115], v[208:211], v[184:187], v[112:115]\n\t" \
  "s_add_u32 m0, s89, 57344\n\t" \
  "v_mfma_f32_16x16x32_bf16 v[112:115], v[212:215], v[188:191], v[112:115]\n\t" \
  "buffer_load_dwordx4 v252, s[48:51], s53 offen offset:0 lds\n\t" \
  "v_mfma_f32_16x16x32_bf16 v[116:119], v[208:211], v[192:195], v[116:119]\n\t" \
  "v_mfma_f32_16x16x32_bf16 v[116:119], v[212:215], v[196:199], v[116:119]\n\t" \
  "s_add_u32 s55, s72, 0\n\t" \
  "buffer_load_dwordx4 a[0:3], v4, s[64:67], s55 offen offset:0 sc1\n\t" \
  "s_waitcnt vmcnt(14) lgkmcnt(0)\n\t" \
  "s_barrier\n\t" \
  "v_add_u32 v255, 4294860800, v255\n\t" \
  "v_mfma_f32_16x16x32_bf16 v[120:123], v[216:219], v[168:171], v[120:123]\n\t" \
  "ds_read_b64_tr_b16 v[200:201], v255 offset:0\n\t" \
  "v_mfma_f32_16x16x32_bf16 v[120:123], v[220:223], v[172:175], v[120:123]\n\t" \
  "ds_read_b64_tr_b16 v[202:203], v255 offset:1280\n\t" \
  "v_mfma_f32_16x16x32_bf16 v[124:127], v[216:219], v[176:179], v[124:127]\n\t" \
  "ds_read_b64_tr_b16 v[204:205], v255 offset:5120\n\t" \
  "v_mfma_f32_16x16x32_bf16 v[124:127], v[220:223], v[180:183], v[124:127]\n\t" \
  "ds_read_b64_tr_b16 v[206:207], v255 offset:6400\n\t" \
  "v_mfma_f32_16x16x32_bf16 v[128:131], v[216:219], v[184:187], v[128:131]\n\t" \
  "v_mfma_f32_16x16x32_bf16 v[128:131], v[220:223], v[188:191], v[128:131]\n\t" \
  "buffer_load_dwordx4 a[4:7], v4, s[64:67], s55 offen offset:1024 sc1\n\t" \
  "v_mfma_f32_16x16x32_bf16 v[132:135], v[216:219], v[192:195], v[132:135]\n\t" \
  "v_mfma_f32_16x16x32_bf16 v[132:135], v[220:223], v[196:199], v[132:135]\n\t" \
  "buffer_load_dwordx4 a[8:11], v4, s[64:67], s55 offen offset:2048 sc1\n\t" \
  "v_mfma_f32_16x16x32_bf16 v[136:139], v[224:227], v[168:171], v[136:139]\n\t" \
  "ds_read_b64_tr_b16 v[208:209], v255 offset:10240\n\t" \
  "v_mfma_f32_16x16x32_bf16 v[136:139], v[228:231], v[172:175], v[136:139]\n\t" \
  "ds_read_b64_tr_b16 v[210:211], v255 offset:11520\n\t" \
  "s_add_u32 s54, s54, 1\n\t" \
  "s_cmp_eq_u32 s54, 10\n\t" \
  "s_cselect_b32 s54, 2, s54\n\t" \
  "v_mfma_f32_16x16x32_bf16 v[140:143], v[224:227], v[176:179], v[140:143]\n\t" \
  "ds_read_b64_tr_b16 v[212:213], v255 offset:15360\n\t" \
  "v_mfma_f32_16x16x32_bf16 v[140:143], v[228:231], v[180:183], v[140:143]\n\t" \
  "ds_read_b64_tr_b16 v[214:215], v255 offset:16640\n\t" \
  "s_lshl_b32 s52, s54, 7\n\t" \
  "s_add_u32 s52, s52, s42\n\t" \
  "v_mfma_f32_16x16x32_bf16 v[144:147], v[224:227], v[184:187], v[144:147]\n\t" \
  "s_mul_i32 s53, s54, 221184\n\t" \
  "s_add_u32 s53, s53, s43\n\t" \
  "v_mfma_f32_16x16x32_bf16 v[144:147], v[228:231], v[188:191], v[144:147]\n\t" \
  "buffer_load_dwordx4 a[12:15], v4, s[64:67], s55 offen offset:3072 sc1\n\t" \
  "v_mfma_f32_16x16x32_bf16 v[148:151], v[224:227], v[192:195], v[148:151]\n\t" \
  "v_add_u32 v253, 4294860800, v253\n\t" \
  "v_add_u32 v254, 4294860800, v254\n\t" \
  "v_mfma_f32_16x16x32_bf16 v[148:151], v[228:231], v[196:199], v[148:151]\n\t" \
  "buffer_load_dwordx4 a[16:19], v3, s[64:67], s55 offen offset:0 sc1\n\t" \
  "v_mfma_f32_16x16x32_bf16 v[152:155], v[232:235], v[168:171], v[152:155]\n\t" \
  "ds_read_b64_tr_b16 v[216:217], v255 offset:32\n\t" \
  "v_mfma_f32_16x16x32_bf16 v[152:155], v[236:239], v[172:175], v[152:155]\n\t" \
  "ds_read_b64_tr_b16 v[218:219], v255 offset:1312\n\t" \
  "ds_read_b128 v[168:171], v253 offset:0\n\t" \
  "ds_read_b128 v[172:175], v254 offset:0\n\t" \
  "v_mfma_f32_16x16x32_bf16 v[156:159], v[232:235], v[176:179], v[156:159]\n\t" \
  "ds_read_b64_tr_b16 v[220:221], v255 offset:5152\n\t" \
  "v_mfma_f32_16x16x32_bf16 v[156:159], v[236:239], v[180:183], v[156:159]\n\t" \
  "ds_read_b64_tr_b16 v[222:223], v255 offset:6432\n\t" \
  "ds_read_b128 v[176:179], v253 offset:2048\n\t" \
  "ds_read_b128 v[180:183], v254 offset:2048\n\t" \
  "v_mfma_f32_16x16x32_bf16 v[160:163], v[232:235], v[184:187], v[160:163]\n\t" \
  "v_mfma_f32_16x16x32_bf16 v[160:163], v[236:239], v[188:191], v[160:163]\n\t" \
  "s_add_u32 s55, s72, 40960\n\t" \
  "buffer_load_dwordx4 a[20:23], v4, s[64:67], s55 offen offset:0 sc1\n\t" \
  "ds_read_b128 v[184:187], v253 offset:4096\n\t" \
  "ds_read_b128 v[188:191], v254 offset:4096\n\t" \
  "v_mfma_f32_16x16x32_bf16 v[164:167], v[232:235], v[192:195], v[164:167]\n\t" \
  "v_mfma_f32_16x16x32_bf16 v[164:167], v[236:239], v[196:199], v[164:167]\n\t" \
  "buffer_load_dwordx4 a[24:27], v4, s[64:67], s55 offen offset:1024 sc1\n\t" \
  "ds_read_b128 v[192:195], v253 offset:6144\n\t" \
  "ds_read_b128 v[196:199], v254 offset:6144\n\t" \
  "s_waitcnt lgkmcnt(9)\n\t" \
  "v_mfma_f32_16x16x32_bf16 v[8:11], v[200:203], v[168:171], v[8:11]\n\t" \
  "ds_read_b64_tr_b16 v[224:225], v255 offset:10272\n\t" \
  "s_waitcnt lgkmcnt(9)\n\t" \
  "v_mfma_f32_16x16x32_bf16 v[8:11], v[204:207], v[172:175], v[8:11]\n\t" \
  "ds_read_b64_tr_b16 v[226:227], v255 offset:11552\n\t" \
  "s_waitcnt lgkmcnt(7)\n\t" \
  "v_mfma_f32_16x16x32_bf16 v[12:15], v[200:203], v[176:179], v[12:15]\n\t" \
  "ds_read_b64_tr_b16 v[228:229], v255 offset:15392\n\t" \
  "s_waitcnt lgkmcnt(7)\n\t" \
  "v_mfma_f32_16x16x32_bf16 v[12:15], v[204:207], v[180:183], v[12:15]\n\t" \
  "ds_read_b64_tr_b16 v[230:231], v255 offset:16672\n\t" \
  "s_waitcnt lgkmcnt(7)\n\t" \
  "v_mfma_f32_16x16x32_bf16 v[16:19], v[200:203], v[184:187], v[16:19]\n\t" \
  "s_add_u32 m0, s88, 106496\n\t" \
  "s_waitcnt lgkmcnt(6)\n\t" \
  "v_mfma_f32_16x16x32_bf16 v[16:19], v[204:207], v[188:191], v[16:19]\n\t" \
  "buffer_load_dwordx4 v240, s[44:47], s52 offen offset:0 lds\n\t" \
  "s_waitcnt lgkmcnt(5)\n\t" \
  "v_mfma_f32_16x16x32_bf16 v[20:23], v[200:203], v[192:195], v[20:23]\n\t" \
  "s_waitcnt lgkmcnt(4)\n\t" \
  "v_mfma_f32_16x16x32_bf16 v[20:23], v[204:207], v[196:199], v[20:23]\n\t" \
  "buffer_load_dwordx4 v241, s[44:47], s52 offen offset:1024 lds\n\t" \
  "v_mfma_f32_16x16x32_bf16 v[24:27], v[208:211], v[168:171], v[24:27]\n\t" \
  "ds_read_b64_tr_b16 v[232:233], v255 offset:64\n\t" \
  "v_mfma_f32_16x16x32_bf16 v[24:27], v[212:215], v[172:175], v[24:27]\n\t" \
  "ds_read_b64_tr_b16 v[234:235], v255 offset:1344\n\t" \
  "v_mfma_f32_16x16x32_bf16 v[28:31], v[208:211], v[176:179], v[28:31]\n\t" \
  "ds_read_b64_tr_b16 v[236:237], v255 offset:5184\n\t" \
  "v_mfma_f32_16x16x32_bf16 v[28:31], v[212:215], v[180:183], v[28:31]\n\t" \
  "ds_read_b64_tr_b16 v[238:239], v255 offset:6464\n\t" \
  "v_mfma_f32_16x16x32_bf16 v[32:35], v[208:211], v[184:187], v[32:35]\n\t" \
  "v_mfma_f32_16x16x32_bf16 v[32:35], v[212:215], v[188:191], v[32:35]\n\t" \
  "buffer_load_dwordx4 v242, s[44:47], s52 offen offset:2048 lds\n\t" \
  "v_mfma_f32_16x16x32_bf16 v[36:39], v[208:211], v[192:195], v[36:39]\n\t" \
  "v_mfma_f32_16x16x32_bf16 v[36:39], v[212:215], v[196:199], v[36:39]\n\t" \
  "buffer_load_dwordx4 v243, s[44:47], s52 offen offset:3072 lds\n\t" \
  "v_mfma_f32_16x16x32_bf16 v[40:43], v[216:219], v[168:171], v[40:43]\n\t" \
  "ds_read_b64_tr_b16 v[200:201], v255 offset:10304\n\t" \
  "v_mfma_f32_16x16x32_bf16 v[40:43], v[220:223], v[172:175], v[40:43]\n\t" \
  "ds_read_b64_tr_b16 v[202:203], v255 offset:11584\n\t" \
  "v_mfma_f32_16x16x32_bf16 v[44:47], v[216:219], v[176:179], v[44:47]\n\t" \
  "ds_read_b64_tr_b16 v[204:205], v255 offset:15424\n\t" \
  "v_mfma_f32_16x16x32_bf16 v[44:47], v[220:223], v[180:183], v[44:47]\n\t" \
  "ds_read_b64_tr_b16 v[206:207], v255 offset:16704\n\t" \
  "v_mfma_f32_16x16x32_bf16 v[48:51], v[216:219], v[184:187], v[48:51]\n\t" \
  "s_add_u32 m0, s88, 110592\n\t" \
  "v_mfma_f32_16x16x32_bf16 v[48:51], v[220:223], v[188:191], v[48:51]\n\t" \
  "buffer_load_dwordx4 v244, s[44:47], s52 offen offset:0 lds\n\t" \
  "v_mfma_f32_16x16x32_bf16 v[52:55], v[216:219], v[192:195], v[52:55]\n\t" \
  "v_mfma_f32_16x16x32_bf16 v[52:55], v[220:223], v[196:199], v[52:55]\n\t" \
  "buffer_load_dwordx4 v245, s[44:47], s52 offen offset:1024 lds\n\t" \
  "s_waitcnt lgkmcnt(10)\n\t" \
  "v_mfma_f32_16x16x32_bf16 v[56:59], v[224:227], v[168:171], v[56:59]\n\t" \
  "ds_read_b64_tr_b16 v[208:209], v255 offset:96\n\t" \
  "s_waitcnt lgkmcnt(9)\n\t" \
  "v_mfma_f32_16x16x32_bf16 v[56:59], v[228:231], v[172:175], v[56:59]\n\t" \
  "ds_read_b64_tr_b16 v[210:211], v255 offset:1376\n\t" \
  "v_mfma_f32_16x16x32_bf16 v[60:63], v[224:227], v[176:179], v[60:63]\n\t" \
  "ds_read_b64_tr_b16 v[212:213], v255 offset:5216\n\t" \
  "v_mfma_f32_16x16x32_bf16 v[60:63], v[228:231], v[180:183], v[60:63]\n\t" \
  "ds_read_b64_tr_b16 v[214:215], v255 offset:6496\n\t" \
  "v_mfma_f32_16x16x32_bf16 v[64:67], v[224:227], v[184:187], v[64:67]\n\t" \
  "v_mfma_f32_16x16x32_bf16 v[64:67], v[228:231], v[188:191], v[64:67]\n\t" \
  "buffer_load_dwordx4 v246, s[44:47], s52 offen offset:2048 lds\n\t" \
  "v_mfma_f32_16x16x32_bf16 v[68:71], v[224:227], v[192:195], v[68:71]\n\t" \
  "v_mfma_f32_16x16x32_bf16 v[68:71], v[228:231], v[196:199], v[68:71]\n\t" \
  "buffer_load_dwordx4 v247, s[44:47], s52 offen offset:3072 lds\n\t" \
  "s_waitcnt lgkmcnt(10)\n\t" \
  "v_mfma_f32_16x16x32_bf16 v[72:75], v[232:235], v[168:171], v[72:75]\n\t" \
  "ds_read_b64_tr_b16 v[216:217], v255 offset:10336\n\t" \
  "s_waitcnt lgkmcnt(9)\n\t" \
  "v_mfma_f32_16x16x32_bf16 v[72:75], v[236:239], v[172:175], v[72:75]\n\t" \
  "ds_read_b64_tr_b16 v[218:219], v255 offset:11616\n\t" \
  "v_mfma_f32_16x16x32_bf16 v[76:79], v[232:235], v[176:179], v[76:79]\n\t" \
  "ds_read_b64_tr_b16 v[220:221], v255 offset:15456\n\t" \
  "v_mfma_f32_16x16x32_bf16 v[76:79], v[236:239], v[180:183], v[76:79]\n\t" \
  "ds_read_b64_tr_b16 v[222:223], v255 offset:16736\n\t" \
  "v_mfma_f32_16x16x32_bf16 v[80:83], v[232:235], v[184:187], v[80:83]\n\t" \
  "s_add_u32 m0, s89, 106496\n\t" \
  "v_mfma_f32_16x16x32_bf16 v[80:83], v[236:239], v[188:191], v[80:83]\n\t" \
  "buffer_load_dwordx4 v248, s[48:51], s53 offen offset:0 lds\n\t" \
  "v_mfma_f32_16x16x32_bf16 v[84:87], v[232:235], v[192:195], v[84:87]\n\t" \
  "v_mfma_f32_16x16x32_bf16 v[84:87], v[236:239], v[196:199], v[84:87]\n\t" \
  "buffer_load_dwordx4 v249, s[48:51], s53 offen offset:1024 lds\n\t" \
  "s_waitcnt lgkmcnt(10)\n\t" \
  "v_mfma_f32_16x16x32_bf16 v[88:91], v[200:203], v[168:171], v[88:91]\n\t" \
  "ds_read_b64_tr_b16 v[224:225], v255 offset:128\n\t" \
  "s_waitcnt lgkmcnt(9)\n\t" \
  "v_mfma_f32_16x16x32_bf16 v[88:91], v[204:207], v[172:175], v[88:91]\n\t" \
  "ds_read_b64_tr_b16 v[226:227], v255 offset:1408\n\t" \
  "v_mfma_f32_16x16x32_bf16 v[92:95], v[200:203], v[176:179], v[92:95]\n\t" \
  "ds_read_b64_tr_b16 v[228:229], v255 offset:5248\n\t" \
  "v_mfma_f32_16x16x32_bf16 v[92:95], v[204:207], v[180:183], v[92:95]\n\t" \
  "ds_read_b64_tr_b16 v[230:231], v255 offset:6528\n\t" \
  "v_mfma_f32_16x16x32_bf16 v[96:99], v[200:203], v[184:187], v[96:99]\n\t" \
  "v_mfma_f32_16x16x32_bf16 v[96:99], v[204:207], v[188:191], v[96:99]\n\t" \
  "buffer_load_dwordx4 v250, s[48:51], s53 offen offset:2048 lds\n\t" \
  "v_mfma_f32_16x16x32_bf16 v[100:103], v[200:203], v[192:195], v[100:103]\n\t" \
  "v_mfma_f32_16x16x32_bf16 v[100:103], v[204:207], v[196:199], v[100:103]\n\t" \
  "buffer_load_dwordx4 v251, s[48:51], s53 offen offset:3072 lds\n\t" \
  "s_waitcnt lgkmcnt(10)\n\t" \
  "v_mfma_f32_16x16x32_bf16 v[104:107], v[208:211], v[168:171], v[104:107]\n\t" \
  "ds_read_b64_tr_b16 v[232:233], v255 offset:10368\n\t" \
  "s_waitcnt lgkmcnt(9)\n\t" \
  "v_mfma_f32_16x16x32_bf16 v[104:107], v[212:215], v[172:175], v[104:107]\n\t" \
  "ds_read_b64_tr_b16 v[234:235], v255 offset:11648\n\t" \
  "v_mfma_f32_16x16x32_bf16 v[108:111], v[208:211], v[176:179], v[108:111]\n\t" \
  "ds_read_b64_tr_b16 v[236:237], v255 offset:15488\n\t" \
  "v_mfma_f32_16x16x32_bf16 v[108:111], v[212:215], v[180:183], v[108:111]\n\t" \
  "ds_read_b64_tr_b16 v[238:239], v255 offset:16768\n\t" \
  "v_mfma_f32_16x16x32_bf16 v[112:115], v[208:211], v[184:187], v[112:115]\n\t" \
  "s_add_u32 m0, s89, 110592\n\t" \
  "v_mfma_f32_16x16x32_bf16 v[112:115], v[212:215], v[188:191], v[112:115]\n\t" \
  "buffer_load_dwordx4 v252, s[48:51], s53 offen offset:0 lds\n\t" \
  "v_mfma_f32_16x16x32_bf16 v[116:119], v[208:211], v[192:195], v[116:119]\n\t" \
  "v_mfma_f32_16x16x32_bf16 v[116:119], v[212:215], v[196:199], v[116:119]\n\t" \
  "s_add_u32 s55, s72, 40960\n\t" \
  "buffer_load_dwordx4 a[28:31], v4, s[64:67], s55 offen offset:2048 sc1\n\t" \
  "s_waitcnt vmcnt(21) lgkmcnt(0)\n\t" \
  "s_barrier\n\t" \
  "v_add_u32 v255, 53248, v255\n\t" \
  "v_mfma_f32_16x16x32_bf16 v[120:123], v[216:219], v[168:171], v[120:123]\n\t" \
  "ds_read_b64_tr_b16 v[200:201], v255 offset:0\n\t" \
  "v_mfma_f32_16x16x32_bf16 v[120:123], v[220:223], v[172:175], v[120:123]\n\t" \
  "ds_read_b64_tr_b16 v[202:203], v255 offset:1280\n\t" \
  "v_mfma_f32_16x16x32_bf16 v[124:127], v[216:219], v[176:179], v[124:127]\n\t" \
  "ds_read_b64_tr_b16 v[204:205], v255 offset:5120\n\t" \
  "v_mfma_f32_16x16x32_bf16 v[124:127], v[220:223], v[180:183], v[124:127]\n\t" \
  "ds_read_b64_tr_b16 v[206:207], v255 offset:6400\n\t" \
  "v_mfma_f32_16x16x32_bf16 v[128:131], v[216:219], v[184:187], v[128:131]\n\t" \
  "v_mfma_f32_16x16x32_bf16 v[128:131], v[220:223], v[188:191], v[128:131]\n\t" \
  "buffer_load_dwordx4 a[32:35], v4, s[64:67], s55 offen offset:3072 sc1\n\t" \
  "v_mfma_f32_16x16x32_bf16 v[132:135], v[216:219], v[192:195], v[132:135]\n\t" \
  "v_mfma_f32_16x16x32_bf16 v[132:135], v[220:223], v[196:199], v[132:135]\n\t" \
  "buffer_load_dwordx4 a[36:39], v3, s[64:67], s55 offen offset:0 sc1\n\t" \
  "v_mfma_f32_16x16x32_bf16 v[136:139], v[224:227], v[168:171], v[136:139]\n\t" \
  "ds_read_b64_tr_b16 v[208:209], v255 offset:10240\n\t" \
  "v_mfma_f32_16x16x32_bf16 v[136:139], v[228:231], v[172:175], v[136:139]\n\t" \
  "ds_read_b64_tr_b16 v[210:211], v255 offset:11520\n\t" \
  "s_add_u32 s54, s54, 1\n\t" \
  "s_cmp_eq_u32 s54, 10\n\t" \
  "s_cselect_b32 s54, 2, s54\n\t" \
  "v_mfma_f32_16x16x32_bf16 v[140:143], v[224:227], v[176:179], v[140:143]\n\t" \
  "ds_read_b64_tr_b16 v[212:213], v255 offset:15360\n\t" \
  "v_mfma_f32_16x16x32_bf16 v[140:143], v[228:231], v[180:183], v[140:143]\n\t" \
  "ds_read_b64_tr_b16 v[214:215], v255 offset:16640\n\t" \
  "s_lshl_b32 s52, s54, 7\n\t" \
  "s_add_u32 s52, s52, s42\n\t" \
  "v_mfma_f32_16x16x32_bf16 v[144:147], v[224:227], v[184:187], v[144:147]\n\t" \
  "s_mul_i32 s53, s54, 221184\n\t" \
  "s_add_u32 s53, s53, s43\n\t" \
  "v_mfma_f32_16x16x32_bf16 v[144:147], v[228:231], v[188:191], v[144:147]\n\t" \
  "s_add_u32 s55, s72, 81920\n\t" \
  "buffer_load_dwordx4 a[40:43], v4, s[64:67], s55 offen offset:0 sc1\n\t" \
  "v_mfma_f32_16x16x32_bf16 v[148:151], v[224:227], v[192:195], v[148:151]\n\t" \
  "v_add_u32 v253, 53248, v253\n\t" \
  "v_add_u32 v254, 53248, v254\n\t" \
  "v_mfma_f32_16x16x32_bf16 v[148:151], v[228:231], v[196:199], v[148:151]\n\t" \
  "buffer_load_dwordx4 a[44:47], v4, s[64:67], s55 offen offset:1024 sc1\n\t" \
  "v_mfma_f32_16x16x32_bf16 v[152:155], v[232:235], v[168:171], v[152:155]\n\t" \
  "ds_read_b64_tr_b16 v[216:217], v255 offset:32\n\t" \
  "v_mfma_f32_16x16x32_bf16 v[152:155], v[236:239], v[172:175], v[152:155]\n\t" \
  "ds_read_b64_tr_b16 v[218:219], v255 offset:1312\n\t" \
  "ds_read_b128 v[168:171], v253 offset:0\n\t" \
  "ds_read_b128 v[172:175], v254 offset:0\n\t" \
  "v_mfma_f32_16x16x32_bf16 v[156:159], v[232:235], v[176:179], v[156:159]\n\t" \
  "ds_read_b64_tr_b16 v[220:221], v255 offset:5152\n\t" \
  "v_mfma_f32_16x16x32_bf16 v[156:159], v[236:239], v[180:183], v[156:159]\n\t" \
  "ds_read_b64_tr_b16 v[222:223], v255 offset:6432\n\t" \
  "ds_read_b128 v[176:179], v253 offset:2048\n\t" \
  "ds_read_b128 v[180:183], v254 offset:2048\n\t" \
  "v_mfma_f32_16x16x32_bf16 v[160:163], v[232:235], v[184:187], v[160:163]\n\t" \
  "v_mfma_f32_16x16x32_bf16 v[160:163], v[236:239], v[188:191], v[160:163]\n\t" \
  "buffer_load_dwordx4 a[48:51], v4, s[64:67], s55 offen offset:2048 sc1\n\t" \
  "ds_read_b128 v[184:187], v253 offset:4096\n\t" \
  "ds_read_b128 v[188:191], v254 offset:4096\n\t" \
  "v_mfma_f32_16x16x32_bf16 v[164:167], v[232:235], v[192:195], v[164:167]\n\t" \
  "v_mfma_f32_16x16x32_bf16 v[164:167], v[236:239], v[196:199], v[164:167]\n\t" \
  "buffer_load_dwordx4 a[52:55], v4, s[64:67], s55 offen offset:3072 sc1\n\t" \
  "ds_read_b128 v[192:195], v253 offset:6144\n\t" \
  "ds_read_b128 v[196:199], v254 offset:6144\n\t" \
  "s_waitcnt lgkmcnt(9)\n\t" \
  "v_mfma_f32_16x16x32_bf16 v[8:11], v[200:203], v[168:171], v[8:11]\n\t" \
  "ds_read_b64_tr_b16 v[224:225], v255 offset:10272\n\t" \
  "s_waitcnt lgkmcnt(9)\n\t" \
  "v_mfma_f32_16x16x32_bf16 v[8:11], v[204:207], v[172:175], v[8:11]\n\t" \
  "ds_read_b64_tr_b16 v[226:227], v255 offset:11552\n\t" \
  "s_waitcnt lgkmcnt(7)\n\t" \
  "v_mfma_f32_16x16x32_bf16 v[12:15], v[200:203], v[176:179], v[12:15]\n\t" \
  "ds_read_b64_tr_b16 v[228:229], v255 offset:15392\n\t" \
  "s_waitcnt lgkmcnt(7)\n\t" \
  "v_mfma_f32_16x16x32_bf16 v[12:15], v[204:207], v[180:183], v[12:15]\n\t" \
  "ds_read_b64_tr_b16 v[230:231], v255 offset:16672\n\t" \
  "s_waitcnt lgkmcnt(7)\n\t" \
  "v_mfma_f32_16x16x32_bf16 v[16:19], v[200:203], v[184:187], v[16:19]\n\t" \
  "s_add_u32 m0, s88, 0\n\t" \
  "s_waitcnt lgkmcnt(6)\n\t" \
  "v_mfma_f32_16x16x32_bf16 v[16:19], v[204:207], v[188:191], v[16:19]\n\t" \
  "buffer_load_dwordx4 v240, s[44:47], s52 offen offset:0 lds\n\t" \
  "s_waitcnt lgkmcnt(5)\n\t" \
  "v_mfma_f32_16x16x32_bf16 v[20:23], v[200:203], v[192:195], v[20:23]\n\t" \
  "s_waitcnt lgkmcnt(4)\n\t" \
  "v_mfma_f32_16x16x32_bf16 v[20:23], v[204:207], v[196:199], v[20:23]\n\t" \
  "buffer_load_dwordx4 v241, s[44:47], s52 offen offset:1024 lds\n\t" \
  "v_mfma_f32_16x16x32_bf16 v[24:27], v[208:211], v[168:171], v[24:27]\n\t" \
  "ds_read_b64_tr_b16 v[232:233], v255 offset:64\n\t" \
  "v_mfma_f32_16x16x32_bf16 v[24:27], v[212:215], v[172:175], v[24:27]\n\t" \
  "ds_read_b64_tr_b16 v[234:235], v255 offset:1344\n\t" \
  "v_mfma_f32_16x16x32_bf16 v[28:31], v[208:211], v[176:179], v[28:31]\n\t" \
  "ds_read_b64_tr_b16 v[236:237], v255 offset:5184\n\t" \
  "v_mfma_f32_16x16x32_bf16 v[28:31], v[212:215], v[180:183], v[28:31]\n\t" \
  "ds_read_b64_tr_b16 v[238:239], v255 offset:6464\n\t" \
  "v_mfma_f32_16x16x32_bf16 v[32:35], v[208:211], v[184:187], v[32:35]\n\t" \
  "v_mfma_f32_16x16x32_bf16 v[32:35], v[212:215], v[188:191], v[32:35]\n\t" \
  "buffer_load_dwordx4 v242, s[44:47], s52 offen offset:2048 lds\n\t" \
  "v_mfma_f32_16x16x32_bf16 v[36:39], v[208:211], v[192:195], v[36:39]\n\t" \
  "v_mfma_f32_16x16x32_bf16 v[36:39], v[212:215], v[196:199], v[36:39]\n\t" \
  "buffer_load_dwordx4 v243, s[44:47], s52 offen offset:3072 lds\n\t" \
  "v_mfma_f32_16x16x32_bf16 v[40:43], v[216:219], v[168:171], v[40:43]\n\t" \
  "ds_read_b64_tr_b16 v[200:201], v255 offset:10304\n\t" \
  "v_mfma_f32_16x16x32_bf16 v[40:43], v[220:223], v[172:175], v[40:43]\n\t" \
  "ds_read_b64_tr_b16 v[202:203], v255 offset:11584\n\t" \
  "v_mfma_f32_16x16x32_bf16 v[44:47], v[216:219], v[176:179], v[44:47]\n\t" \
  "ds_read_b64_tr_b16 v[204:205], v255 offset:15424\n\t" \
  "v_mfma_f32_16x16x32_bf16 v[44:47], v[220:223], v[180:183], v[44:47]\n\t" \
  "ds_read_b64_tr_b16 v[206:207], v255 offset:16704\n\t" \
  "v_mfma_f32_16x16x32_bf16 v[48:51], v[216:219], v[184:187], v[48:51]\n\t" \
  "s_add_u32 m0, s88, 4096\n\t" \
  "v_mfma_f32_16x16x32_bf16 v[48:51], v[220:223], v[188:191], v[48:51]\n\t" \
  "buffer_load_dwordx4 v244, s[44:47], s52 offen offset:0 lds\n\t" \
  "v_mfma_f32_16x16x32_bf16 v[52:55], v[216:219], v[192:195], v[52:55]\n\t" \
  "v_mfma_f32_16x16x32_bf16 v[52:55], v[220:223], v[196:199], v[52:55]\n\t" \
  "buffer_load_dwordx4 v245, s[44:47], s52 offen offset:1024 lds\n\t" \
  "s_waitcnt lgkmcnt(10)\n\t" \
  "v_mfma_f32_16x16x32_bf16 v[56:59], v[224:227], v[168:171], v[56:59]\n\t" \
  "ds_read_b64_tr_b16 v[208:209], v255 offset:96\n\t" \
  "s_waitcnt lgkmcnt(9)\n\t" \
  "v_mfma_f32_16x16x32_bf16 v[56:59], v[228:231], v[172:175], v[56:59]\n\t" \
  "ds_read_b64_tr_b16 v[210:211], v255 offset:1376\n\t" \
  "v_mfma_f32_16x16x32_bf16 v[60:63], v[224:227], v[176:179], v[60:63]\n\t" \
  "ds_read_b64_tr_b16 v[212:213], v255 offset:5216\n\t" \
  "v_mfma_f32_16x16x32_bf16 v[60:63], v[228:231], v[180:183], v[60:63]\n\t" \
  "ds_read_b64_tr_b16 v[214:215], v255 offset:6496\n\t" \
  "v_mfma_f32_16x16x32_bf16 v[64:67], v[224:227], v[184:187], v[64:67]\n\t" \
  "v_mfma_f32_16x16x32_bf16 v[64:67], v[228:231], v[188:191], v[64:67]\n\t" \
  "buffer_load_dwordx4 v246, s[44:47], s52 offen offset:2048 lds\n\t" \
  "v_mfma_f32_16x16x32_bf16 v[68:71], v[224:227], v[192:195], v[68:71]\n\t" \
  "v_mfma_f32_16x16x32_bf16 v[68:71], v[228:231], v[196:199], v[68:71]\n\t" \
  "buffer_load_dwordx4 v247, s[44:47], s52 offen offset:3072 lds\n\t" \
  "s_waitcnt lgkmcnt(10)\n\t" \
  "v_mfma_f32_16x16x32_bf16 v[72:75], v[232:235], v[168:171], v[72:75]\n\t" \
  "ds_read_b64_tr_b16 v[216:217], v255 offset:10336\n\t" \
  "s_waitcnt lgkmcnt(9)\n\t" \
  "v_mfma_f32_16x16x32_bf16 v[72:75], v[236:239], v[172:175], v[72:75]\n\t" \
  "ds_read_b64_tr_b16 v[218:219], v255 offset:11616\n\t" \
  "v_mfma_f32_16x16x32_bf16 v[76:79], v[232:235], v[176:179], v[76:79]\n\t" \
  "ds_read_b64_tr_b16 v[220:221], v255 offset:15456\n\t" \
  "v_mfma_f32_16x16x32_bf16 v[76:79], v[236:239], v[180:183], v[76:79]\n\t" \
  "ds_read_b64_tr_b16 v[222:223], v255 offset:16736\n\t" \
  "v_mfma_f32_16x16x32_bf16 v[80:83], v[232:235], v[184:187], v[80:83]\n\t" \
  "s_add_u32 m0, s89, 0\n\t" \
  "v_mfma_f32_16x16x32_bf16 v[80:83], v[236:239], v[188:191], v[80:83]\n\t" \
  "buffer_load_dwordx4 v248, s[48:51], s53 offen offset:0 lds\n\t" \
  "v_mfma_f32_16x16x32_bf16 v[84:87], v[232:235], v[192:195], v[84:87]\n\t" \
  "v_mfma_f32_16x16x32_bf16 v[84:87], v[236:239], v[196:199], v[84:87]\n\t" \
  "buffer_load_dwordx4 v249, s[48:51], s53 offen offset:1024 lds\n\t" \
  "s_waitcnt lgkmcnt(10)\n\t" \
  "v_mfma_f32_16x16x32_bf16 v[88:91], v[200:203], v[168:171], v[88:91]\n\t" \
  "ds_read_b64_tr_b16 v[224:225], v255 offset:128\n\t" \
  "s_waitcnt lgkmcnt(9)\n\t" \
  "v_mfma_f32_16x16x32_bf16 v[88:91], v[204:207], v[172:175], v[88:91]\n\t" \
  "ds_read_b64_tr_b16 v[226:227], v255 offset:1408\n\t" \
  "v_mfma_f32_16x16x32_bf16 v[92:95], v[200:203], v[176:179], v[92:95]\n\t" \
  "ds_read_b64_tr_b16 v[228:229], v255 offset:5248\n\t" \
  "v_mfma_f32_16x16x32_bf16 v[92:95], v[204:207], v[180:183], v[92:95]\n\t" \
  "ds_read_b64_tr_b16 v[230:231], v255 offset:6528\n\t" \
  "v_mfma_f32_16x16x32_bf16 v[96:99], v[200:203], v[184:187], v[96:99]\n\t" \
  "v_mfma_f32_16x16x32_bf16 v[96:99], v[204:207], v[188:191], v[96:99]\n\t" \
  "buffer_load_dwordx4 v250, s[48:51], s53 offen offset:2048 lds\n\t" \
  "v_mfma_f32_16x16x32_bf16 v[100:103], v[200:203], v[192:195], v[100:103]\n\t" \
  "v_mfma_f32_16x16x32_bf16 v[100:103], v[204:207], v[196:199], v[100:103]\n\t" \
  "buffer_load_dwordx4 v251, s[48:51], s53 offen offset:3072 lds\n\t" \
  "s_waitcnt lgkmcnt(10)\n\t" \
  "v_mfma_f32_16x16x32_bf16 v[104:107], v[208:211], v[168:171], v[104:107]\n\t" \
  "ds_read_b64_tr_b16 v[232:233], v255 offset:10368\n\t" \
  "s_waitcnt lgkmcnt(9)\n\t" \
  "v_mfma_f32_16x16x32_bf16 v[104:107], v[212:215], v[172:175], v[104:107]\n\t" \
  "ds_read_b64_tr_b16 v[234:235], v255 offset:11648\n\t" \
  "v_mfma_f32_16x16x32_bf16 v[108:111], v[208:211], v[176:179], v[108:111]\n\t" \
  "ds_read_b64_tr_b16 v[236:237], v255 offset:15488\n\t" \
  "v_mfma_f32_16x16x32_bf16 v[108:111], v[212:215], v[180:183], v[108:111]\n\t" \
  "ds_read_b64_tr_b16 v[238:239], v255 offset:16768\n\t" \
  "v_mfma_f32_16x16x32_bf16 v[112:115], v[208:211], v[184:187], v[112:115]\n\t" \
  "s_add_u32 m0, s89, 4096\n\t" \
  "v_mfma_f32_16x16x32_bf16 v[112:115], v[212:215], v[188:191], v[112:115]\n\t" \
  "buffer_load_dwordx4 v252, s[48:51], s53 offen offset:0 lds\n\t" \
  "v_mfma_f32_16x16x32_bf16 v[116:119], v[208:211], v[192:195], v[116:119]\n\t" \
  "v_mfma_f32_16x16x32_bf16 v[116:119], v[212:215], v[196:199], v[116:119]\n\t" \
  "s_add_u32 s55, s72, 81920\n\t" \
  "buffer_load_dwordx4 a[56:59], v3, s[64:67], s55 offen offset:0 sc1\n\t" \
  "s_waitcnt vmcnt(21) lgkmcnt(0)\n\t" \
  "s_barrier\n\t" \
  "v_add_u32 v255, 53248, v255\n\t" \
  "v_mfma_f32_16x16x32_bf16 v[120:123], v[216:219], v[168:171], v[120:123]\n\t" \
  "ds_read_b64_tr_b16 v[200:201], v255 offset:0\n\t" \
  "v_mfma_f32_16x16x32_bf16 v[120:123], v[220:223], v[172:175], v[120:123]\n\t" \
  "ds_read_b64_tr_b16 v[202:203], v255 offset:1280\n\t" \
  "v_mfma_f32_16x16x32_bf16 v[124:127], v[216:219], v[176:179], v[124:127]\n\t" \
  "ds_read_b64_tr_b16 v[204:205], v255 offset:5120\n\t" \
  "v_mfma_f32_16x16x32_bf16 v[124:127], v[220:223], v[180:183], v[124:127]\n\t" \
  "ds_read_b64_tr_b16 v[206:207], v255 offset:6400\n\t" \
  "v_mfma_f32_16x16x32_bf16 v[128:131], v[216:219], v[184:187], v[128:131]\n\t" \
  "v_mfma_f32_16x16x32_bf16 v[128:131], v[220:223], v[188:191], v[128:131]\n\t" \
  "s_add_u32 s55, s72, 122880\n\t" \
  "buffer_load_dwordx4 a[60:63], v4, s[64:67], s55 offen offset:0 sc1\n\t" \
  "v_mfma_f32_16x16x32_bf16 v[132:135], v[216:219], v[192:195], v[132:135]\n\t" \
  "v_mfma_f32_16x16x32_bf16 v[132:135], v[220:223], v[196:199], v[132:135]\n\t" \
  "buffer_load_dwordx4 a[64:67], v4, s[64:67], s55 offen offset:1024 sc1\n\t" \
  "v_mfma_f32_16x16x32_bf16 v[136:139], v[224:227], v[168:171], v[136:139]\n\t" \
  "ds_read_b64_tr_b16 v[208:209], v255 offset:10240\n\t" \
  "v_mfma_f32_16x16x32_bf16 v[136:139], v[228:231], v[172:175], v[136:139]\n\t" \
  "ds_read_b64_tr_b16 v[210:211], v255 offset:11520\n\t" \
  "v_mfma_f32_16x16x32_bf16 v[140:143], v[224:227], v[176:179], v[140:143]\n\t" \
  "ds_read_b64_tr_b16 v[212:213], v255 offset:15360\n\t" \
  "v_mfma_f32_16x16x32_bf16 v[140:143], v[228:231], v[180:183], v[140:143]\n\t" \
  "ds_read_b64_tr_b16 v[214:215], v255 offset:16640\n\t" \
  "v_mfma_f32_16x16x32_bf16 v[144:147], v[224:227], v[184:187], v[144:147]\n\t" \
  "v_mfma_f32_16x16x32_bf16 v[144:147], v[228:231], v[188:191], v[144:147]\n\t" \
  "buffer_load_dwordx4 a[68:71], v4, s[64:67], s55 offen offset:2048 sc1\n\t" \
  "v_mfma_f32_16x16x32_bf16 v[148:151], v[224:227], v[192:195], v[148:151]\n\t" \
  "v_add_u32 v253, 53248, v253\n\t" \
  "v_add_u32 v254, 53248, v254\n\t" \
  "v_mfma_f32_16x16x32_bf16 v[148:151], v[228:231], v[196:199], v[148:151]\n\t" \
  "buffer_load_dwordx4 a[72:75], v4, s[64:67], s55 offen offset:3072 sc1\n\t" \
  "v_mfma_f32_16x16x32_bf16 v[152:155], v[232:235], v[168:171], v[152:155]\n\t" \
  "ds_read_b64_tr_b16 v[216:217], v255 offset:32\n\t" \
  "v_mfma_f32_16x16x32_bf16 v[152:155], v[236:239], v[172:175], v[152:155]\n\t" \
  "ds_read_b64_tr_b16 v[218:219], v255 offset:1312\n\t" \
  "ds_read_b128 v[168:171], v253 offset:0\n\t" \
  "ds_read_b128 v[172:175], v254 offset:0\n\t" \
  "v_mfma_f32_16x16x32_bf16 v[156:159], v[232:235], v[176:179], v[156:159]\n\t" \
  "ds_read_b64_tr_b16 v[220:221], v255 offset:5152\n\t" \
  "v_mfma_f32_16x16x32_bf16 v[156:159], v[236:239], v[180:183], v[156:159]\n\t" \
  "ds_read_b64_tr_b16 v[222:223], v255 offset:6432\n\t" \
  "ds_read_b128 v[176:179], v253 offset:2048\n\t" \
  "ds_read_b128 v[180:183], v254 offset:2048\n\t" \
  "v_mfma_f32_16x16x32_bf16 v[160:163], v[232:235], v[184:187], v[160:163]\n\t" \
  "v_mfma_f32_16x16x32_bf16 v[160:163], v[236:239], v[188:191], v[160:163]\n\t" \
  "buffer_load_dwordx4 a[76:79], v3, s[64:67], s55 offen offset:0 sc1\n\t" \
  "ds_read_b128 v[184:187], v253 offset:4096\n\t" \
  "ds_read_b128 v[188:191], v254 offset:4096\n\t" \
  "v_mfma_f32_16x16x32_bf16 v[164:167], v[232:235], v[192:195], v[164:167]\n\t" \
  "v_mfma_f32_16x16x32_bf16 v[164:167], v[236:239], v[196:199], v[164:167]\n\t" \
  "s_add_u32 s55, s72, 163840\n\t" \
  "buffer_load_dwordx4 a[80:83], v4, s[64:67], s55 offen offset:0 sc1\n\t" \
  "ds_read_b128 v[192:195], v253 offset:6144\n\t" \
  "ds_read_b128 v[196:199], v254 offset:6144\n\t" \
  "s_waitcnt lgkmcnt(9)\n\t" \
  "v_mfma_f32_16x16x32_bf16 v[8:11], v[200:203], v[168:171], v[8:11]\n\t" \
  "ds_read_b64_tr_b16 v[224:225], v255 offset:10272\n\t" \
  "s_waitcnt lgkmcnt(9)\n\t" \
  "v_mfma_f32_16x16x32_bf16 v[8:11], v[204:207], v[172:175], v[8:11]\n\t" \
  "ds_read_b64_tr_b16 v[226:227], v255 offset:11552\n\t" \
  "s_waitcnt lgkmcnt(7)\n\t" \
  "v_mfma_f32_16x16x32_bf16 v[12:15], v[200:203], v[176:179], v[12:15]\n\t" \
  "ds_read_b64_tr_b16 v[228:229], v255 offset:15392\n\t" \
  "s_waitcnt lgkmcnt(7)\n\t" \
  "v_mfma_f32_16x16x32_bf16 v[12:15], v[204:207], v[180:183], v[12:15]\n\t" \
  "ds_read_b64_tr_b16 v[230:231], v255 offset:16672\n\t" \
  "s_waitcnt lgkmcnt(7)\n\t" \
  "v_mfma_f32_16x16x32_bf16 v[16:19], v[200:203], v[184:187], v[16:19]\n\t" \
  "v_lshlrev_b32 v5, 3, v4\n\t" \
  "v_min_u32 v5, 1152, v5\n\t" \
  "global_load_dword a241, v5, s[92:93]\n\t" \
  "s_waitcnt lgkmcnt(6)\n\t" \
  "v_mfma_f32_16x16x32_bf16 v[16:19], v[204:207], v[188:191], v[16:19]\n\t" \
  "s_waitcnt lgkmcnt(5)\n\t" \
  "v_mfma_f32_16x16x32_bf16 v[20:23], v[200:203], v[192:195], v[20:23]\n\t" \
  "global_load_dword a242, v5, s[94:95]\n\t" \
  "s_waitcnt lgkmcnt(4)\n\t" \
  "v_mfma_f32_16x16x32_bf16 v[20:23], v[204:207], v[196:199], v[20:23]\n\t" \
  "s_add_u32 s55, s72, 163840\n\t" \
  "buffer_load_dwordx4 a[84:87], v4, s[64:67], s55 offen offset:1024 sc1\n\t" \
  "v_mfma_f32_16x16x32_bf16 v[24:27], v[208:211], v[168:171], v[24:27]\n\t" \
  "ds_read_b64_tr_b16 v[232:233], v255 offset:64\n\t" \
  "v_mfma_f32_16x16x32_bf16 v[24:27], v[212:215], v[172:175], v[24:27]\n\t" \
  "ds_read_b64_tr_b16 v[234:235], v255 offset:1344\n\t" \
  "v_mfma_f32_16x16x32_bf16 v[28:31], v[208:211], v[176:179], v[28:31]\n\t" \
  "ds_read_b64_tr_b16 v[236:237], v255 offset:5184\n\t" \
  "v_mfma_f32_16x16x32_bf16 v[28:31], v[212:215], v[180:183], v[28:31]\n\t" \
  "ds_read_b64_tr_b16 v[238:239], v255 offset:6464\n\t" \
  "v_mfma_f32_16x16x32_bf16 v[32:35], v[208:211], v[184:187], v[32:35]\n\t" \
  "buffer_load_dwordx4 a[88:91], v4, s[64:67], s55 offen offset:2048 sc1\n\t" \
  "v_mfma_f32_16x16x32_bf16 v[32:35], v[212:215], v[188:191], v[32:35]\n\t" \
  "v_mfma_f32_16x16x32_bf16 v[36:39], v[208:211], v[192:195], v[36:39]\n\t" \
  "buffer_load_dwordx4 a[92:95], v4, s[64:67], s55 offen offset:3072 sc1\n\t" \
  "v_mfma_f32_16x16x32_bf16 v[36:39], v[212:215], v[196:199], v[36:39]\n\t" \
  "buffer_load_dwordx4 a[96:99], v3, s[64:67], s55 offen offset:0 sc1\n\t" \
  "v_mfma_f32_16x16x32_bf16 v[40:43], v[216:219], v[168:171], v[40:43]\n\t" \
  "ds_read_b64_tr_b16 v[200:201], v255 offset:10304\n\t" \
  "v_mfma_f32_16x16x32_bf16 v[40:43], v[220:223], v[172:175], v[40:43]\n\t" \
  "ds_read_b64_tr_b16 v[202:203], v255 offset:11584\n\t" \
  "v_mfma_f32_16x16x32_bf16 v[44:47], v[216:219], v[176:179], v[44:47]\n\t" \
  "ds_read_b64_tr_b16 v[204:205], v255 offset:15424\n\t" \
  "v_mfma_f32_16x16x32_bf16 v[44:47], v[220:223], v[180:183], v[44:47]\n\t" \
  "ds_read_b64_tr_b16 v[206:207], v255 offset:16704\n\t" \
  "v_mfma_f32_16x16x32_bf16 v[48:51], v[216:219], v[184:187], v[48:51]\n\t" \
  "s_add_u32 s55, s72, 204800\n\t" \
  "buffer_load_dwordx4 a[100:103], v4, s[64:67], s55 offen offset:0 sc1\n\t" \
  "v_mfma_f32_16x16x32_bf16 v[48:51], v[220:223], v[188:191], v[48:51]\n\t" \
  "v_mfma_f32_16x16x32_bf16 v[52:55], v[216:219], v[192:195], v[52:55]\n\t" \
  "buffer_load_dwordx4 a[104:107], v4, s[64:67], s55 offen offset:1024 sc1\n\t" \
  "v_mfma_f32_16x16x32_bf16 v[52:55], v[220:223], v[196:199], v[52:55]\n\t" \
  "buffer_load_dwordx4 a[108:111], v4, s[64:67], s55 offen offset:2048 sc1\n\t" \
  "s_waitcnt lgkmcnt(10)\n\t" \
  "v_mfma_f32_16x16x32_bf16 v[56:59], v[224:227], v[168:171], v[56:59]\n\t" \
  "ds_read_b64_tr_b16 v[208:209], v255 offset:96\n\t" \
  "s_waitcnt lgkmcnt(9)\n\t" \
  "v_mfma_f32_16x16x32_bf16 v[56:59], v[228:231], v[172:175], v[56:59]\n\t" \
  "ds_read_b64_tr_b16 v[210:211], v255 offset:1376\n\t" \
  "v_mfma_f32_16x16x32_bf16 v[60:63], v[224:227], v[176:179], v[60:63]\n\t" \
  "ds_read_b64_tr_b16 v[212:213], v255 offset:5216\n\t" \
  "v_mfma_f32_16x16x32_bf16 v[60:63], v[228:231], v[180:183], v[60:63]\n\t" \
  "ds_read_b64_tr_b16 v[214:215], v255 offset:6496\n\t" \
  "v_mfma_f32_16x16x32_bf16 v[64:67], v[224:227], v[184:187], v[64:67]\n\t" \
  "buffer_load_dwordx4 a[112:115], v4, s[64:67], s55 offen offset:3072 sc1\n\t" \
  "v_mfma_f32_16x16x32_bf16 v[64:67], v[228:231], v[188:191], v[64:67]\n\t" \
  "v_mfma_f32_16x16x32_bf16 v[68:71], v[224:227], v[192:195], v[68:71]\n\t" \
  "buffer_load_dwordx4 a[116:119], v3, s[64:67], s55 offen offset:0 sc1\n\t" \
  "v_mfma_f32_16x16x32_bf16 v[68:71], v[228:231], v[196:199], v[68:71]\n\t" \
  "s_add_u32 s55, s72, 245760\n\t" \
  "buffer_load_dwordx4 a[120:123], v4, s[64:67], s55 offen offset:0 sc1\n\t" \
  "s_waitcnt lgkmcnt(10)\n\t" \
  "v_mfma_f32_16x16x32_bf16 v[72:75], v[232:235], v[168:171], v[72:75]\n\t" \
  "ds_read_b64_tr_b16 v[216:217], v255 offset:10336\n\t" \
  "s_waitcnt lgkmcnt(9)\n\t" \
  "v_mfma_f32_16x16x32_bf16 v[72:75], v[236:239], v[172:175], v[72:75]\n\t" \
  "ds_read_b64_tr_b16 v[218:219], v255 offset:11616\n\t" \
  "v_mfma_f32_16x16x32_bf16 v[76:79], v[232:235], v[176:179], v[76:79]\n\t" \
  "ds_read_b64_tr_b16 v[220:221], v255 offset:15456\n\t" \
  "v_mfma_f32_16x16x32_bf16 v[76:79], v[236:239], v[180:183], v[76:79]\n\t" \
  "ds_read_b64_tr_b16 v[222:223], v255 offset:16736\n\t" \
  "v_mfma_f32_16x16x32_bf16 v[80:83], v[232:235], v[184:187], v[80:83]\n\t" \
  "buffer_load_dwordx4 a[124:127], v4, s[64:67], s55 offen offset:1024 sc1\n\t" \
  "v_mfma_f32_16x16x32_bf16 v[80:83], v[236:239], v[188:191], v[80:83]\n\t" \
  "v_mfma_f32_16x16x32_bf16 v[84:87], v[232:235], v[192:195], v[84:87]\n\t" \
  "buffer_load_dwordx4 a[128:131], v4, s[64:67], s55 offen offset:2048 sc1\n\t" \
  "v_mfma_f32_16x16x32_bf16 v[84:87], v[236:239], v[196:199], v[84:87]\n\t" \
  "buffer_load_dwordx4 a[132:135], v4, s[64:67], s55 offen offset:3072 sc1\n\t" \
  "s_waitcnt lgkmcnt(10)\n\t" \
  "v_mfma_f32_16x16x32_bf16 v[88:91], v[200:203], v[168:171], v[88:91]\n\t" \
  "ds_read_b64_tr_b16 v[224:225], v255 offset:128\n\t" \
  "s_waitcnt lgkmcnt(9)\n\t" \
  "v_mfma_f32_16x16x32_bf16 v[88:91], v[204:207], v[172:175], v[88:91]\n\t" \
  "ds_read_b64_tr_b16 v[226:227], v255 offset:1408\n\t" \
  "v_mfma_f32_16x16x32_bf16 v[92:95], v[200:203], v[176:179], v[92:95]\n\t" \
  "ds_read_b64_tr_b16 v[228:229], v255 offset:5248\n\t" \
  "v_mfma_f32_16x16x32_bf16 v[92:95], v[204:207], v[180:183], v[92:95]\n\t" \
  "ds_read_b64_tr_b16 v[230:231], v255 offset:6528\n\t" \
  "v_mfma_f32_16x16x32_bf16 v[96:99], v[200:203], v[184:187], v[96:99]\n\t" \
  "buffer_load_dwordx4 a[136:139], v3, s[64:67], s55 offen offset:0 sc1\n\t" \
  "v_mfma_f32_16x16x32_bf16 v[96:99], v[204:207], v[188:191], v[96:99]\n\t" \
  "v_mfma_f32_16x16x32_bf16 v[100:103], v[200:203], v[192:195], v[100:103]\n\t" \
  "s_mov_b32 s55, s96\n\t" \
  "buffer_load_dwordx4 a[140:143], v4, s[64:67], s55 offen offset:0 sc1\n\t" \
  "v_mfma_f32_16x16x32_bf16 v[100:103], v[204:207], v[196:199], v[100:103]\n\t" \
  "buffer_load_dwordx4 a[144:147], v4, s[64:67], s55 offen offset:1024 sc1\n\t" \
  "s_waitcnt lgkmcnt(10)\n\t" \
  "v_mfma_f32_16x16x32_bf16 v[104:107], v[208:211], v[168:171], v[104:107]\n\t" \
  "ds_read_b64_tr_b16 v[232:233], v255 offset:10368\n\t" \
  "s_waitcnt lgkmcnt(9)\n\t" \
  "v_mfma_f32_16x16x32_bf16 v[104:107], v[212:215], v[172:175], v[104:107]\n\t" \
  "ds_read_b64_tr_b16 v[234:235], v255 offset:11648\n\t" \
  "v_mfma_f32_16x16x32_bf16 v[108:111], v[208:211], v[176:179], v[108:111]\n\t" \
  "ds_read_b64_tr_b16 v[236:237], v255 offset:15488\n\t" \
  "v_mfma_f32_16x16x32_bf16 v[108:111], v[212:215], v[180:183], v[108:111]\n\t" \
  "ds_read_b64_tr_b16 v[238:239], v255 offset:16768\n\t" \
  "v_mfma_f32_16x16x32_bf16 v[112:115], v[208:211], v[184:187], v[112:115]\n\t" \
  "buffer_load_dwordx4 a[148:151], v4, s[64:67], s55 offen offset:2048 sc1\n\t" \
  "v_mfma_f32_16x16x32_bf16 v[112:115], v[212:215], v[188:191], v[112:115]\n\t" \
  "v_mfma_f32_16x16x32_bf16 v[116:119], v[208:211], v[192:195], v[116:119]\n\t" \
  "buffer_load_dwordx4 a[152:155], v4, s[64:67], s55 offen offset:3072 sc1\n\t" \
  "v_mfma_f32_16x16x32_bf16 v[116:119], v[212:215], v[196:199], v[116:119]\n\t" \
  "buffer_load_dwordx4 a[156:159], v3, s[64:67], s55 offen offset:0 sc1\n\t" \
  "s_waitcnt vmcnt(28) lgkmcnt(0)\n\t" \
  "s_barrier\n\t" \
  "v_add_u32 v255, 4294860800, v255\n\t" \
  "v_mfma_f32_16x16x32_bf16 v[120:123], v[216:219], v[168:171], v[120:123]\n\t" \
  "ds_read_b64_tr_b16 v[200:201], v255 offset:0\n\t" \
  "v_mfma_f32_16x16x32_bf16 v[120:123], v[220:223], v[172:175], v[120:123]\n\t" \
  "ds_read_b64_tr_b16 v[202:203], v255 offset:1280\n\t" \
  "v_mfma_f32_16x16x32_bf16 v[124:127], v[216:219], v[176:179], v[124:127]\n\t" \
  "ds_read_b64_tr_b16 v[204:205], v255 offset:5120\n\t" \
  "v_mfma_f32_16x16x32_bf16 v[124:127], v[220:223], v[180:183], v[124:127]\n\t" \
  "ds_read_b64_tr_b16 v[206:207], v255 offset:6400\n\t" \
  "v_mfma_f32_16x16x32_bf16 v[128:131], v[216:219], v[184:187], v[128:131]\n\t" \
  "s_add_u32 s55, s73, 0\n\t" \
  "buffer_load_dwordx4 a[160:163], v4, s[64:67], s55 offen offset:0 sc1\n\t" \
  "v_mfma_f32_16x16x32_bf16 v[128:131], v[220:223], v[188:191], v[128:131]\n\t" \
  "v_mfma_f32_16x16x32_bf16 v[132:135], v[216:219], v[192:195], v[132:135]\n\t" \
  "buffer_load_dwordx4 a[164:167], v4, s[64:67], s55 offen offset:1024 sc1\n\t" \
  "v_mfma_f32_16x16x32_bf16 v[132:135], v[220:223], v[196:199], v[132:135]\n\t" \
  "buffer_load_dwordx4 a[168:171], v4, s[64:67], s55 offen offset:2048 sc1\n\t" \
  "v_mfma_f32_16x16x32_bf16 v[136:139], v[224:227], v[168:171], v[136:139]\n\t" \
  "ds_read_b64_tr_b16 v[208:209], v255 offset:10240\n\t" \
  "v_mfma_f32_16x16x32_bf16 v[136:139], v[228:231], v[172:175], v[136:139]\n\t" \
  "ds_read_b64_tr_b16 v[210:211], v255 offset:11520\n\t" \
  "v_mfma_f32_16x16x32_bf16 v[140:143], v[224:227], v[176:179], v[140:143]\n\t" \
  "ds_read_b64_tr_b16 v[212:213], v255 offset:15360\n\t" \
  "v_mfma_f32_16x16x32_bf16 v[140:143], v[228:231], v[180:183], v[140:143]\n\t" \
  "ds_read_b64_tr_b16 v[214:215], v255 offset:16640\n\t" \
  "v_mfma_f32_16x16x32_bf16 v[144:147], v[224:227], v[184:187], v[144:147]\n\t" \
  "buffer_load_dwordx4 a[172:175], v4, s[64:67], s55 offen offset:3072 sc1\n\t" \
  "v_mfma_f32_16x16x32_bf16 v[144:147], v[228:231], v[188:191], v[144:147]\n\t" \
  "v_mfma_f32_16x16x32_bf16 v[148:151], v[224:227], v[192:195], v[148:151]\n\t" \
  "v_add_u32 v253, 4294860800, v253\n\t" \
  "v_add_u32 v254, 4294860800, v254\n\t" \
  "buffer_load_dwordx4 a[176:179], v3, s[64:67], s55 offen offset:0 sc1\n\t" \
  "v_mfma_f32_16x16x32_bf16 v[148:151], v[228:231], v[196:199], v[148:151]\n\t" \
  "s_add_u32 s55, s73, 40960\n\t" \
  "buffer_load_dwordx4 a[180:183], v4, s[64:67], s55 offen offset:0 sc1\n\t" \
  "v_mfma_f32_16x16x32_bf16 v[152:155], v[232:235], v[168:171], v[152:155]\n\t" \
  "ds_read_b64_tr_b16 v[216:217], v255 offset:32\n\t" \
  "v_mfma_f32_16x16x32_bf16 v[152:155], v[236:239], v[172:175], v[152:155]\n\t" \
  "ds_read_b64_tr_b16 v[218:219], v255 offset:1312\n\t" \
  "ds_read_b128 v[168:171], v253 offset:0\n\t" \
  "ds_read_b128 v[172:175], v254 offset:0\n\t" \
  "v_mfma_f32_16x16x32_bf16 v[156:159], v[232:235], v[176:179], v[156:159]\n\t" \
  "ds_read_b64_tr_b16 v[220:221], v255 offset:5152\n\t" \
  "v_mfma_f32_16x16x32_bf16 v[156:159], v[236:239], v[180:183], v[156:159]\n\t" \
  "ds_read_b64_tr_b16 v[222:223], v255 offset:6432\n\t" \
  "ds_read_b128 v[176:179], v253 offset:2048\n\t" \
  "ds_read_b128 v[180:183], v254 offset:2048\n\t" \
  "v_mfma_f32_16x16x32_bf16 v[160:163], v[232:235], v[184:187], v[160:163]\n\t" \
  "buffer_load_dwordx4 a[184:187], v4, s[64:67], s55 offen offset:1024 sc1\n\t" \
  "v_mfma_f32_16x16x32_bf16 v[160:163], v[236:239], v[188:191], v[160:163]\n\t" \
  "ds_read_b128 v[184:187], v253 offset:4096\n\t" \
  "ds_read_b128 v[188:191], v254 offset:4096\n\t" \
  "v_mfma_f32_16x16x32_bf16 v[164:167], v[232:235], v[192:195], v[164:167]\n\t" \
  "buffer_load_dwordx4 a[188:191], v4, s[64:67], s55 offen offset:2048 sc1\n\t" \
  "v_mfma_f32_16x16x32_bf16 v[164:167], v[236:239], v[196:199], v[164:167]\n\t" \
  "ds_read_b128 v[192:195], v253 offset:6144\n\t" \
  "ds_read_b128 v[196:199], v254 offset:6144\n\t" \
  "s_waitcnt lgkmcnt(9)\n\t" \
  "v_mfma_f32_16x16x32_bf16 v[8:11], v[200:203], v[168:171], v[8:11]\n\t" \
  "ds_read_b64_tr_b16 v[224:225], v255 offset:10272\n\t" \
  "s_waitcnt lgkmcnt(9)\n\t" \
  "v_mfma_f32_16x16x32_bf16 v[8:11], v[204:207], v[172:175], v[8:11]\n\t" \
  "ds_read_b64_tr_b16 v[226:227], v255 offset:11552\n\t" \
  "s_waitcnt lgkmcnt(7)\n\t" \
  "v_mfma_f32_16x16x32_bf16 v[12:15], v[200:203], v[176:179], v[12:15]\n\t" \
  "ds_read_b64_tr_b16 v[228:229], v255 offset:15392\n\t" \
  "s_waitcnt lgkmcnt(7)\n\t" \
  "v_mfma_f32_16x16x32_bf16 v[12:15], v[204:207], v[180:183], v[12:15]\n\t" \
  "ds_read_b64_tr_b16 v[230:231], v255 offset:16672\n\t" \
  "s_add_u32 m0, s91, 0\n\t" \
  "s_add_u32 s55, s72, 4294803456\n\t" \
  "s_waitcnt lgkmcnt(7)\n\t" \
  "v_mfma_f32_16x16x32_bf16 v[16:19], v[200:203], v[184:187], v[16:19]\n\t" \
  "buffer_load_dwordx4 v4, s[64:67], s55 offen offset:0 sc1 lds\n\t" \
  "s_mov_b64 s[48:49], s[40:41]\n\t" \
  "s_cmp_eq_u32 s97, 206\n\t" \
  "s_cselect_b32 s50, 0, 16531712\n\t" \
  "s_waitcnt lgkmcnt(6)\n\t" \
  "v_mfma_f32_16x16x32_bf16 v[16:19], v[204:207], v[188:191], v[16:19]\n\t" \
  "v_and_b32 v243, 0xff, v4\n\t" \
  "v_and_b32 v244, 0x100, v4\n\t" \
  "v_lshl_add_u32 v243, v244, 2, v243\n\t" \
  "s_waitcnt lgkmcnt(5)\n\t" \
  "v_mfma_f32_16x16x32_bf16 v[20:23], v[200:203], v[192:195], v[20:23]\n\t" \
  "buffer_load_dwordx4 v4, s[64:67], s55 offen offset:1024 sc1 lds\n\t" \
  "v_and_b32 v244, 0x200, v4\n\t" \
  "v_lshrrev_b32 v244, 1, v244\n\t" \
  "v_add_u32 v243, v243, v244\n\t" \
  "s_waitcnt lgkmcnt(4)\n\t" \
  "v_mfma_f32_16x16x32_bf16 v[20:23], v[204:207], v[196:199], v[20:23]\n\t" \
  "buffer_load_dwordx4 v4, s[64:67], s55 offen offset:2048 sc1 lds\n\t" \
  "v_mfma_f32_16x16x32_bf16 v[24:27], v[208:211], v[168:171], v[24:27]\n\t" \
  "ds_read_b64_tr_b16 v[232:233], v255 offset:64\n\t" \
  "v_mfma_f32_16x16x32_bf16 v[24:27], v[212:215], v[172:175], v[24:27]\n\t" \
  "ds_read_b64_tr_b16 v[234:235], v255 offset:1344\n\t" \
  "v_mfma_f32_16x16x32_bf16 v[28:31], v[208:211], v[176:179], v[28:31]\n\t" \
  "ds_read_b64_tr_b16 v[236:237], v255 offset:5184\n\t" \
  "v_readlane_b32 s76, v2, 8\n\t" \
  "v_mfma_f32_16x16x32_bf16 v[28:31], v[212:215], v[180:183], v[28:31]\n\t" \
  "ds_read_b64_tr_b16 v[238:239], v255 offset:6464\n\t" \
  "v_readlane_b32 s77, v2, 23\n\t" \
  "v_mfma_f32_16x16x32_bf16 v[32:35], v[208:211], v[184:187], v[32:35]\n\t" \
  "buffer_load_dwordx4 v4, s[64:67], s55 offen offset:3072 sc1 lds\n\t" \
  "v_readlane_b32 s78, v2, 24\n\t" \
  "v_mfma_f32_16x16x32_bf16 v[32:35], v[212:215], v[188:191], v[32:35]\n\t" \
  "s_add_u32 m0, s91, 4096\n\t" \
  "s_add_u32 s55, s72, 4294807552\n\t" \
  "v_readlane_b32 s79, v2, 22\n\t" \
  "v_mfma_f32_16x16x32_bf16 v[36:39], v[208:211], v[192:195], v[36:39]\n\t" \
  "buffer_load_dwordx4 v4, s[64:67], s55 offen offset:0 sc1 lds\n\t" \
  "s_add_u32 m0, s91, 5120\n\t" \
  "s_add_u32 s55, s72, 4294844416\n\t" \
  "v_mfma_f32_16x16x32_bf16 v[36:39], v[212:215], v[196:199], v[36:39]\n\t" \
  "buffer_load_dwordx4 v4, s[64:67], s55 offen offset:0 sc1 lds\n\t" \
  "v_mfma_f32_16x16x32_bf16 v[40:43], v[216:219], v[168:171], v[40:43]\n\t" \
  "ds_read_b64_tr_b16 v[200:201], v255 offset:10304\n\t" \
  "v_mfma_f32_16x16x32_bf16 v[40:43], v[220:223], v[172:175], v[40:43]\n\t" \
  "ds_read_b64_tr_b16 v[202:203], v255 offset:11584\n\t" \
  "v_mfma_f32_16x16x32_bf16 v[44:47], v[216:219], v[176:179], v[44:47]\n\t" \
  "ds_read_b64_tr_b16 v[204:205], v255 offset:15424\n\t" \
  "s_add_u32 s57, s97, 0\n\t" \
  "s_lshr_b32 s58, s57, 2\n\t" \
  "s_lshl_b32 s58, s58, 12\n\t" \
  "s_and_b32 s57, s57, 2\n\t" \
  "s_lshl_b32 s57, s57, 8\n\t" \
  "s_add_u32 s58, s58, s57\n\t" \
  "v_mfma_f32_16x16x32_bf16 v[44:47], v[220:223], v[180:183], v[44:47]\n\t" \
  "ds_read_b64_tr_b16 v[206:207], v255 offset:16704\n\t" \
  "s_add_u32 s58, s58, s35\n\t" \
  "v_mul_f32 v24, v8, v24\n\t" \
  "v_mul_f32 v25, v9, v25\n\t" \
  "v_mul_f32 v26, v10, v26\n\t" \
  "v_mul_f32 v27, v11, v27\n\t" \
  "v_mul_f32 v28, v12, v28\n\t" \
  "v_mfma_f32_16x16x32_bf16 v[48:51], v[216:219], v[184:187], v[48:51]\n\t" \
  "buffer_load_dwordx4 v4, s[64:67], s55 offen offset:1024 sc1 lds\n\t" \
  "v_mul_f32 v29, v13, v29\n\t" \
  "v_mul_f32 v30, v14, v30\n\t" \
  "v_mul_f32 v31, v15, v31\n\t" \
  "v_mul_f32 v8, 0xbfb8aa3b, v8\n\t" \
  "v_mul_f32 v9, 0xbfb8aa3b, v9\n\t" \
  "v_mul_f32 v10, 0xbfb8aa3b, v10\n\t" \
  "v_mfma_f32_16x16x32_bf16 v[48:51], v[220:223], v[188:191], v[48:51]\n\t" \
  "v_mul_f32 v11, 0xbfb8aa3b, v11\n\t" \
  "v_mul_f32 v12, 0xbfb8aa3b, v12\n\t" \
  "v_mul_f32 v13, 0xbfb8aa3b, v13\n\t" \
  "v_mul_f32 v14, 0xbfb8aa3b, v14\n\t" \
  "v_mul_f32 v15, 0xbfb8aa3b, v15\n\t" \
  "v_exp_f32 v8, v8\n\t" \
  "v_mfma_f32_16x16x32_bf16 v[52:55], v[216:219], v[192:195], v[52:55]\n\t" \
  "buffer_load_dwordx4 v4, s[64:67], s55 offen offset:2048 sc1 lds\n\t" \
  "v_exp_f32 v9, v9\n\t" \
  "v_exp_f32 v10, v10\n\t" \
  "v_exp_f32 v11, v11\n\t" \
  "v_exp_f32 v12, v12\n\t" \
  "v_exp_f32 v13, v13\n\t" \
  "v_mfma_f32_16x16x32_bf16 v[52:55], v[220:223], v[196:199], v[52:55]\n\t" \
  "buffer_load_dwordx4 v4, s[64:67], s55 offen offset:3072 sc1 lds\n\t" \
  "v_exp_f32 v14, v14\n\t" \
  "v_exp_f32 v15, v15\n\t" \
  "v_add_f32 v8, 1.0, v8\n\t" \
  "v_add_f32 v9, 1.0, v9\n\t" \
  "v_add_f32 v10, 1.0, v10\n\t" \
  "v_add_f32 v11, 1.0, v11\n\t" \
  "s_waitcnt lgkmcnt(10)\n\t" \
  "v_mfma_f32_16x16x32_bf16 v[56:59], v[224:227], v[168:171], v[56:59]\n\t" \
  "ds_read_b64_tr_b16 v[208:209], v255 offset:96\n\t" \
  "v_add_f32 v12, 1.0, v12\n\t" \
  "v_add_f32 v13, 1.0, v13\n\t" \
  "v_add_f32 v14, 1.0, v14\n\t" \
  "v_add_f32 v15, 1.0, v15\n\t" \
  "v_rcp_f32 v8, v8\n\t" \
  "v_rcp_f32 v9, v9\n\t" \
  "s_waitcnt lgkmcnt(9)\n\t" \
  "v_mfma_f32_16x16x32_bf16 v[56:59], v[228:231], v[172:175], v[56:59]\n\t" \
  "ds_read_b64_tr_b16 v[210:211], v255 offset:1376\n\t" \
  "v_rcp_f32 v10, v10\n\t" \
  "v_rcp_f32 v11, v11\n\t" \
  "v_rcp_f32 v12, v12\n\t" \
  "v_rcp_f32 v13, v13\n\t" \
  "v_rcp_f32 v14, v14\n\t" \
  "v_rcp_f32 v15, v15\n\t" \
  "v_mfma_f32_16x16x32_bf16 v[60:63], v[224:227], v[176:179], v[60:63]\n\t" \
  "ds_read_b64_tr_b16 v[212:213], v255 offset:5216\n\t" \
  "v_mul_f32 v24, v24, v8\n\t" \
  "v_mul_f32 v25, v25, v9\n\t" \
  "v_mul_f32 v26, v26, v10\n\t" \
  "v_mul_f32 v27, v27, v11\n\t" \
  "v_mul_f32 v28, v28, v12\n\t" \
  "v_mfma_f32_16x16x32_bf16 v[60:63], v[228:231], v[180:183], v[60:63]\n\t" \
  "ds_read_b64_tr_b16 v[214:215], v255 offset:6496\n\t" \
  "s_add_u32 m0, s91, 9216\n\t" \
  "s_add_u32 s55, s72, 4294848512\n\t" \
  "v_mul_f32 v29, v29, v13\n\t" \
  "v_mul_f32 v30, v30, v14\n\t" \
  "v_mul_f32 v31, v31, v15\n\t" \
  "v_cvt_pk_bf16_f32 v8, v24, v25\n\t" \
  "v_cvt_pk_bf16_f32 v9, v26, v27\n\t" \
  "v_cvt_pk_bf16_f32 v10, v28, v29\n\t" \
  "v_mfma_f32_16x16x32_bf16 v[64:67], v[224:227], v[184:187], v[64:67]\n\t" \
  "buffer_load_dwordx4 v4, s[64:67], s55 offen offset:0 sc1 lds\n\t" \
  "v_cvt_pk_bf16_f32 v11, v30, v31\n\t" \
  "s_nop 1\n\t" \
  "v_permlane16_swap_b32 v8, v10\n\t" \
  "v_permlane16_swap_b32 v9, v11\n\t" \
  "buffer_store_dwordx4 v[8:11], v243, s[48:51], s58 offen offset:0 sc1\n\t" \
  "v_mul_f32 v32, v16, v32\n\t" \
  "v_mfma_f32_16x16x32_bf16 v[64:67], v[228:231], v[188:191], v[64:67]\n\t" \
  "s_add_u32 m0, s91, 10240\n\t" \
  "s_add_u32 s55, s72, 4294885376\n\t" \
  "v_mul_f32 v33, v17, v33\n\t" \
  "v_mul_f32 v34, v18, v34\n\t" \
  "v_mul_f32 v35, v19, v35\n\t" \
  "v_mul_f32 v36, v20, v36\n\t" \
  "v_mul_f32 v37, v21, v37\n\t" \
  "v_mul_f32 v38, v22, v38\n\t" \
  "v_mfma_f32_16x16x32_bf16 v[68:71], v[224:227], v[192:195], v[68:71]\n\t" \
  "buffer_load_dwordx4 v4, s[64:67], s55 offen offset:0 sc1 lds\n\t" \
  "v_mul_f32 v39, v23, v39\n\t" \
  "v_mul_f32 v16, 0xbfb8aa3b, v16\n\t" \
  "v_mul_f32 v17, 0xbfb8aa3b, v17\n\t" \
  "v_mul_f32 v18, 0xbfb8aa3b, v18\n\t" \
  "v_mul_f32 v19, 0xbfb8aa3b, v19\n\t" \
  "v_mul_f32 v20, 0xbfb8aa3b, v20\n\t" \
  "v_mfma_f32_16x16x32_bf16 v[68:71], v[228:231], v[196:199], v[68:71]\n\t" \
  "buffer_load_dwordx4 v4, s[64:67], s55 offen offset:1024 sc1 lds\n\t" \
  "v_mul_f32 v21, 0xbfb8aa3b, v21\n\t" \
  "v_mul_f32 v22, 0xbfb8aa3b, v22\n\t" \
  "v_mul_f32 v23, 0xbfb8aa3b, v23\n\t" \
  "v_exp_f32 v16, v16\n\t" \
  "v_exp_f32 v17, v17\n\t" \
  "s_waitcnt lgkmcnt(10)\n\t" \
  "v_mfma_f32_16x16x32_bf16 v[72:75], v[232:235], v[168:171], v[72:75]\n\t" \
  "ds_read_b64_tr_b16 v[216:217], v255 offset:10336\n\t" \
  "v_exp_f32 v18, v18\n\t" \
  "v_exp_f32 v19, v19\n\t" \
  "v_exp_f32 v20, v20\n\t" \
  "v_exp_f32 v21, v21\n\t" \
  "v_exp_f32 v22, v22\n\t" \
  "v_exp_f32 v23, v23\n\t" \
  "s_waitcnt lgkmcnt(9)\n\t" \
  "v_mfma_f32_16x16x32_bf16 v[72:75], v[236:239], v[172:175], v[72:75]\n\t" \
  "ds_read_b64_tr_b16 v[218:219], v255 offset:11616\n\t" \
  "v_add_f32 v16, 1.0, v16\n\t" \
  "v_add_f32 v17, 1.0, v17\n\t" \
  "v_add_f32 v18, 1.0, v18\n\t" \
  "v_add_f32 v19, 1.0, v19\n\t" \
  "v_add_f32 v20, 1.0, v20\n\t" \
  "v_add_f32 v21, 1.0, v21\n\t" \
  "v_mfma_f32_16x16x32_bf16 v[76:79], v[232:235], v[176:179], v[76:79]\n\t" \
  "ds_read_b64_tr_b16 v[220:221], v255 offset:15456\n\t" \
  "v_add_f32 v22, 1.0, v22\n\t" \
  "v_add_f32 v23, 1.0, v23\n\t" \
  "v_rcp_f32 v16, v16\n\t" \
  "v_rcp_f32 v17, v17\n\t" \
  "v_rcp_f32 v18, v18\n\t" \
  "v_rcp_f32 v19, v19\n\t" \
  "v_mfma_f32_16x16x32_bf16 v[76:79], v[236:239], v[180:183], v[76:79]\n\t" \
  "ds_read_b64_tr_b16 v[222:223], v255 offset:16736\n\t" \
  "v_rcp_f32 v20, v20\n\t" \
  "v_rcp_f32 v21, v21\n\t" \
  "v_rcp_f32 v22, v22\n\t" \
  "v_rcp_f32 v23, v23\n\t" \
  "v_mul_f32 v32, v32, v16\n\t" \
  "v_mfma_f32_16x16x32_bf16 v[80:83], v[232:235], v[184:187], v[80:83]\n\t" \
  "buffer_load_dwordx4 v4, s[64:67], s55 offen offset:2048 sc1 lds\n\t" \
  "v_mul_f32 v33, v33, v17\n\t" \
  "v_mul_f32 v34, v34, v18\n\t" \
  "v_mul_f32 v35, v35, v19\n\t" \
  "v_mul_f32 v36, v36, v20\n\t" \
  "v_mul_f32 v37, v37, v21\n\t" \
  "v_mul_f32 v38, v38, v22\n\t" \
  "v_mfma_f32_16x16x32_bf16 v[80:83], v[236:239], v[188:191], v[80:83]\n\t" \
  "v_mul_f32 v39, v39, v23\n\t" \
  "v_cvt_pk_bf16_f32 v16, v32, v33\n\t" \
  "v_cvt_pk_bf16_f32 v17, v34, v35\n\t" \
  "v_cvt_pk_bf16_f32 v18, v36, v37\n\t" \
  "v_cvt_pk_bf16_f32 v19, v38, v39\n\t" \
  "s_nop 1\n\t" \
  "v_mfma_f32_16x16x32_bf16 v[84:87], v[232:235], v[192:195], v[84:87]\n\t" \
  "buffer_load_dwordx4 v4, s[64:67], s55 offen offset:3072 sc1 lds\n\t" \
  "s_add_u32 m0, s91, 14336\n\t" \
  "s_add_u32 s55, s72, 4294889472\n\t" \
  "v_permlane16_swap_b32 v16, v18\n\t" \
  "v_permlane16_swap_b32 v17, v19\n\t" \
  "buffer_store_dwordx4 v[16:19], v243, s[48:51], s58 offen offset:2048 sc1\n\t" \
  "s_add_u32 s57, s97, 2\n\t" \
  "s_lshr_b32 s58, s57, 2\n\t" \
  "s_lshl_b32 s58, s58, 12\n\t" \
  "v_mfma_f32_16x16x32_bf16 v[84:87], v[236:239], v[196:199], v[84:87]\n\t" \
  "buffer_load_dwordx4 v4, s[64:67], s55 offen offset:0 sc1 lds\n\t" \
  "s_and_b32 s57, s57, 2\n\t" \
  "s_lshl_b32 s57, s57, 8\n\t" \
  "s_add_u32 s58, s58, s57\n\t" \
  "s_add_u32 s58, s58, s35\n\t" \
  "v_mul_f32 v56, v40, v56\n\t" \
  "v_mul_f32 v57, v41, v57\n\t" \
  "s_waitcnt lgkmcnt(10)\n\t" \
  "v_mfma_f32_16x16x32_bf16 v[88:91], v[200:203], v[168:171], v[88:91]\n\t" \
  "ds_read_b64_tr_b16 v[224:225], v255 offset:128\n\t" \
  "v_mul_f32 v58, v42, v58\n\t" \
  "v_mul_f32 v59, v43, v59\n\t" \
  "v_mul_f32 v60, v44, v60\n\t" \
  "v_mul_f32 v61, v45, v61\n\t" \
  "v_mul_f32 v62, v46, v62\n\t" \
  "s_waitcnt lgkmcnt(9)\n\t" \
  "v_mfma_f32_16x16x32_bf16 v[88:91], v[204:207], v[172:175], v[88:91]\n\t" \
  "ds_read_b64_tr_b16 v[226:227], v255 offset:1408\n\t" \
  "v_mul_f32 v63, v47, v63\n\t" \
  "v_mul_f32 v40, 0xbfb8aa3b, v40\n\t" \
  "v_mul_f32 v41, 0xbfb8aa3b, v41\n\t" \
  "v_mul_f32 v42, 0xbfb8aa3b, v42\n\t" \
  "v_mul_f32 v43, 0xbfb8aa3b, v43\n\t" \
  "v_mul_f32 v44, 0xbfb8aa3b, v44\n\t" \
  "v_mfma_f32_16x16x32_bf16 v[92:95], v[200:203], v[176:179], v[92:95]\n\t" \
  "ds_read_b64_tr_b16 v[228:229], v255 offset:5248\n\t" \
  "v_mul_f32 v45, 0xbfb8aa3b, v45\n\t" \
  "v_mul_f32 v46, 0xbfb8aa3b, v46\n\t" \
  "v_mul_f32 v47, 0xbfb8aa3b, v47\n\t" \
  "v_exp_f32 v40, v40\n\t" \
  "v_exp_f32 v41, v41\n\t" \
  "v_exp_f32 v42, v42\n\t" \
  "v_mfma_f32_16x16x32_bf16 v[92:95], v[204:207], v[180:183], v[92:95]\n\t" \
  "ds_read_b64_tr_b16 v[230:231], v255 offset:6528\n\t" \
  "s_add_u32 m0, s91, 15360\n\t" \
  "s_add_u32 s55, s72, 4294926336\n\t" \
  "v_exp_f32 v43, v43\n\t" \
  "v_exp_f32 v44, v44\n\t" \
  "v_exp_f32 v45, v45\n\t" \
  "v_exp_f32 v46, v46\n\t" \
  "v_exp_f32 v47, v47\n\t" \
  "v_add_f32 v40, 1.0, v40\n\t" \
  "v_mfma_f32_16x16x32_bf16 v[96:99], v[200:203], v[184:187], v[96:99]\n\t" \
  "buffer_load_dwordx4 v4, s[64:67], s55 offen offset:0 sc1 lds\n\t" \
  "v_add_f32 v41, 1.0, v41\n\t" \
  "v_add_f32 v42, 1.0, v42\n\t" \
  "v_add_f32 v43, 1.0, v43\n\t" \
  "v_add_f32 v44, 1.0, v44\n\t" \
  "v_add_f32 v45, 1.0, v45\n\t" \
  "v_mfma_f32_16x16x32_bf16 v[96:99], v[204:207], v[188:191], v[96:99]\n\t" \
  "v_add_f32 v46, 1.0, v46\n\t" \
  "v_add_f32 v47, 1.0, v47\n\t" \
  "v_rcp_f32 v40, v40\n\t" \
  "v_rcp_f32 v41, v41\n\t" \
  "v_rcp_f32 v42, v42\n\t" \
  "v_rcp_f32 v43, v43\n\t" \
  "v_mfma_f32_16x16x32_bf16 v[100:103], v[200:203], v[192:195], v[100:103]\n\t" \
  "buffer_load_dwordx4 v4, s[64:67], s55 offen offset:1024 sc1 lds\n\t" \
  "v_rcp_f32 v44, v44\n\t" \
  "v_rcp_f32 v45, v45\n\t" \
  "v_rcp_f32 v46, v46\n\t" \
  "v_rcp_f32 v47, v47\n\t" \
  "v_mul_f32 v56, v56, v40\n\t" \
  "v_mul_f32 v57, v57, v41\n\t" \
  "v_mfma_f32_16x16x32_bf16 v[100:103], v[204:207], v[196:199], v[100:103]\n\t" \
  "buffer_load_dwordx4 v4, s[64:67], s55 offen offset:2048 sc1 lds\n\t" \
  "v_mul_f32 v58, v58, v42\n\t" \
  "v_mul_f32 v59, v59, v43\n\t" \
  "v_mul_f32 v60, v60, v44\n\t" \
  "v_mul_f32 v61, v61, v45\n\t" \
  "v_mul_f32 v62, v62, v46\n\t" \
  "v_mul_f32 v63, v63, v47\n\t" \
  "s_waitcnt lgkmcnt(10)\n\t" \
  "v_mfma_f32_16x16x32_bf16 v[104:107], v[208:211], v[168:171], v[104:107]\n\t" \
  "ds_read_b64_tr_b16 v[232:233], v255 offset:10368\n\t" \
  "v_cvt_pk_bf16_f32 v40, v56, v57\n\t" \
  "v_cvt_pk_bf16_f32 v41, v58, v59\n\t" \
  "v_cvt_pk_bf16_f32 v42, v60, v61\n\t" \
  "v_cvt_pk_bf16_f32 v43, v62, v63\n\t" \
  "s_nop 1\n\t" \
  "s_waitcnt lgkmcnt(9)\n\t" \
  "v_mfma_f32_16x16x32_bf16 v[104:107], v[212:215], v[172:175], v[104:107]\n\t" \
  "ds_read_b64_tr_b16 v[234:235], v255 offset:11648\n\t" \
  "v_permlane16_swap_b32 v40, v42\n\t" \
  "v_permlane16_swap_b32 v41, v43\n\t" \
  "buffer_store_dwordx4 v[40:43], v243, s[48:51], s58 offen offset:0 sc1\n\t" \
  "v_mul_f32 v64, v48, v64\n\t" \
  "v_mul_f32 v65, v49, v65\n\t" \
  "v_mul_f32 v66, v50, v66\n\t" \
  "v_mfma_f32_16x16x32_bf16 v[108:111], v[208:211], v[176:179], v[108:111]\n\t" \
  "ds_read_b64_tr_b16 v[236:237], v255 offset:15488\n\t" \
  "v_mul_f32 v67, v51, v67\n\t" \
  "v_mul_f32 v68, v52, v68\n\t" \
  "v_mul_f32 v69, v53, v69\n\t" \
  "v_mul_f32 v70, v54, v70\n\t" \
  "v_mul_f32 v71, v55, v71\n\t" \
  "v_mul_f32 v48, 0xbfb8aa3b, v48\n\t" \
  "v_mfma_f32_16x16x32_bf16 v[108:111], v[212:215], v[180:183], v[108:111]\n\t" \
  "ds_read_b64_tr_b16 v[238:239], v255 offset:16768\n\t" \
  "v_mul_f32 v49, 0xbfb8aa3b, v49\n\t" \
  "v_mul_f32 v50, 0xbfb8aa3b, v50\n\t" \
  "v_mul_f32 v51, 0xbfb8aa3b, v51\n\t" \
  "v_mul_f32 v52, 0xbfb8aa3b, v52\n\t" \
  "v_mul_f32 v53, 0xbfb8aa3b, v53\n\t" \
  "v_mul_f32 v54, 0xbfb8aa3b, v54\n\t" \
  "v_mfma_f32_16x16x32_bf16 v[112:115], v[208:211], v[184:187], v[112:115]\n\t" \
  "buffer_load_dwordx4 v4, s[64:67], s55 offen offset:3072 sc1 lds\n\t" \
  "v_mul_f32 v55, 0xbfb8aa3b, v55\n\t" \
  "v_exp_f32 v48, v48\n\t" \
  "v_exp_f32 v49, v49\n\t" \
  "v_exp_f32 v50, v50\n\t" \
  "v_exp_f32 v51, v51\n\t" \
  "v_exp_f32 v52, v52\n\t" \
  "v_mfma_f32_16x16x32_bf16 v[112:115], v[212:215], v[188:191], v[112:115]\n\t" \
  "s_add_u32 m0, s91, 19456\n\t" \
  "s_add_u32 s55, s72, 4294930432\n\t" \
  "v_exp_f32 v53, v53\n\t" \
  "v_exp_f32 v54, v54\n\t" \
  "v_exp_f32 v55, v55\n\t" \
  "v_add_f32 v48, 1.0, v48\n\t" \
  "v_add_f32 v49, 1.0, v49\n\t" \
  "v_mfma_f32_16x16x32_bf16 v[116:119], v[208:211], v[192:195], v[116:119]\n\t" \
  "buffer_load_dwordx4 v4, s[64:67], s55 offen offset:0 sc1 lds\n\t" \
  "v_add_f32 v50, 1.0, v50\n\t" \
  "v_add_f32 v51, 1.0, v51\n\t" \
  "v_add_f32 v52, 1.0, v52\n\t" \
  "v_add_f32 v53, 1.0, v53\n\t" \
  "v_add_f32 v54, 1.0, v54\n\t" \
  "v_add_f32 v55, 1.0, v55\n\t" \
  "v_mfma_f32_16x16x32_bf16 v[116:119], v[212:215], v[196:199], v[116:119]\n\t" \
  "v_lshrrev_b32 v242, 8, v4\n\t" \
  "v_lshlrev_b32 v242, 3, v242\n\t" \
  "v_and_b32 v241, 0xf0, v4\n\t" \
  "v_mul_u32_u24 v241, 80, v241\n\t" \
  "v_add_u32 v241, v241, v242\n\t" \
  "global_load_dwordx2 a[244:245], v241, s[86:87] offset:0\n\t" \
  "v_rcp_f32 v48, v48\n\t" \
  "v_rcp_f32 v49, v49\n\t" \
  "v_rcp_f32 v50, v50\n\t" \
  "v_rcp_f32 v51, v51\n\t" \
  "v_rcp_f32 v52, v52\n\t" \
  "v_rcp_f32 v53, v53\n\t" \
  "s_waitcnt lgkmcnt(10)\n\t" \
  "v_mfma_f32_16x16x32_bf16 v[120:123], v[216:219], v[168:171], v[120:123]\n\t" \
  "v_rcp_f32 v54, v54\n\t" \
  "v_rcp_f32 v55, v55\n\t" \
  "v_mul_f32 v64, v64, v48\n\t" \
  "v_mul_f32 v65, v65, v49\n\t" \
  "v_mul_f32 v66, v66, v50\n\t" \
  "v_mul_f32 v67, v67, v51\n\t" \
  "s_waitcnt lgkmcnt(8)\n\t" \
  "v_mfma_f32_16x16x32_bf16 v[120:123], v[220:223], v[172:175], v[120:123]\n\t" \
  "v_mul_f32 v68, v68, v52\n\t" \
  "v_mul_f32 v69, v69, v53\n\t" \
  "v_mul_f32 v70, v70, v54\n\t" \
  "v_mul_f32 v71, v71, v55\n\t" \
  "v_cvt_pk_bf16_f32 v48, v64, v65\n\t" \
  "v_mfma_f32_16x16x32_bf16 v[124:127], v[216:219], v[176:179], v[124:127]\n\t" \
  "v_cvt_pk_bf16_f32 v49, v66, v67\n\t" \
  "v_cvt_pk_bf16_f32 v50, v68, v69\n\t" \
  "v_cvt_pk_bf16_f32 v51, v70, v71\n\t" \
  "s_nop 1\n\t" \
  "v_permlane16_swap_b32 v48, v50\n\t" \
  "v_permlane16_swap_b32 v49, v51\n\t" \
  "v_mfma_f32_16x16x32_bf16 v[124:127], v[220:223], v[180:183], v[124:127]\n\t" \
  "buffer_store_dwordx4 v[48:51], v243, s[48:51], s58 offen offset:2048 sc1\n\t" \
  "s_add_u32 s57, s97, 4\n\t" \
  "s_lshr_b32 s58, s57, 2\n\t" \
  "s_lshl_b32 s58, s58, 12\n\t" \
  "s_and_b32 s57, s57, 2\n\t" \
  "s_lshl_b32 s57, s57, 8\n\t" \
  "v_mfma_f32_16x16x32_bf16 v[128:131], v[216:219], v[184:187], v[128:131]\n\t" \
  "global_load_dwordx2 a[246:247], v241, s[86:87] offset:32\n\t" \
  "s_add_u32 s58, s58, s57\n\t" \
  "s_add_u32 s58, s58, s35\n\t" \
  "s_mov_b32 s50, 16531712\n\t" \
  "v_mul_f32 v88, v72, v88\n\t" \
  "v_mul_f32 v89, v73, v89\n\t" \
  "v_mul_f32 v90, v74, v90\n\t" \
  "v_mfma_f32_16x16x32_bf16 v[128:131], v[220:223], v[188:191], v[128:131]\n\t" \
  "v_mul_f32 v91, v75, v91\n\t" \
  "v_mul_f32 v92, v76, v92\n\t" \
  "v_mul_f32 v93, v77, v93\n\t" \
  "v_mul_f32 v94, v78, v94\n\t" \
  "v_mul_f32 v95, v79, v95\n\t" \
  "v_mul_f32 v72, 0xbfb8aa3b, v72\n\t" \
  "v_mfma_f32_16x16x32_bf16 v[132:135], v[216:219], v[192:195], v[132:135]\n\t" \
  "global_load_dwordx2 a[248:249], v241, s[86:87] offset:64\n\t" \
  "v_mul_f32 v73, 0xbfb8aa3b, v73\n\t" \
  "v_mul_f32 v74, 0xbfb8aa3b, v74\n\t" \
  "v_mul_f32 v75, 0xbfb8aa3b, v75\n\t" \
  "v_mul_f32 v76, 0xbfb8aa3b, v76\n\t" \
  "v_mul_f32 v77, 0xbfb8aa3b, v77\n\t" \
  "v_mfma_f32_16x16x32_bf16 v[132:135], v[220:223], v[196:199], v[132:135]\n\t" \
  "global_load_dwordx2 a[250:251], v241, s[86:87] offset:96\n\t" \
  "v_mul_f32 v78, 0xbfb8aa3b, v78\n\t" \
  "v_mul_f32 v79, 0xbfb8aa3b, v79\n\t" \
  "v_exp_f32 v72, v72\n\t" \
  "v_exp_f32 v73, v73\n\t" \
  "v_exp_f32 v74, v74\n\t" \
  "v_exp_f32 v75, v75\n\t" \
  "s_waitcnt lgkmcnt(6)\n\t" \
  "v_mfma_f32_16x16x32_bf16 v[136:139], v[224:227], v[168:171], v[136:139]\n\t" \
  "v_exp_f32 v76, v76\n\t" \
  "v_exp_f32 v77, v77\n\t" \
  "v_exp_f32 v78, v78\n\t" \
  "v_exp_f32 v79, v79\n\t" \
  "v_add_f32 v72, 1.0, v72\n\t" \
  "v_add_f32 v73, 1.0, v73\n\t" \
  "s_waitcnt lgkmcnt(4)\n\t" \
  "v_mfma_f32_16x16x32_bf16 v[136:139], v[228:231], v[172:175], v[136:139]\n\t" \
  "v_add_f32 v74, 1.0, v74\n\t" \
  "v_add_f32 v75, 1.0, v75\n\t" \
  "v_add_f32 v76, 1.0, v76\n\t" \
  "v_add_f32 v77, 1.0, v77\n\t" \
  "v_add_f32 v78, 1.0, v78\n\t" \
  "v_add_f32 v79, 1.0, v79\n\t" \
  "v_mfma_f32_16x16x32_bf16 v[140:143], v[224:227], v[176:179], v[140:143]\n\t" \
  "v_rcp_f32 v72, v72\n\t" \
  "v_rcp_f32 v73, v73\n\t" \
  "v_rcp_f32 v74, v74\n\t" \
  "v_rcp_f32 v75, v75\n\t" \
  "v_rcp_f32 v76, v76\n\t" \
  "v_mfma_f32_16x16x32_bf16 v[140:143], v[228:231], v[180:183], v[140:143]\n\t" \
  "v_rcp_f32 v77, v77\n\t" \
  "v_rcp_f32 v78, v78\n\t" \
  "v_rcp_f32 v79, v79\n\t" \
  "v_mul_f32 v88, v88, v72\n\t" \
  "v_mul_f32 v89, v89, v73\n\t" \
  "v_mul_f32 v90, v90, v74\n\t" \
  "v_mfma_f32_16x16x32_bf16 v[144:147], v[224:227], v[184:187], v[144:147]\n\t" \
  "global_load_dwordx2 a[252:253], v241, s[86:87] offset:128\n\t" \
  "v_mul_f32 v91, v91, v75\n\t" \
  "v_mul_f32 v92, v92, v76\n\t" \
  "v_mul_f32 v93, v93, v77\n\t" \
  "v_mul_f32 v94, v94, v78\n\t" \
  "v_mul_f32 v95, v95, v79\n\t" \
  "v_cvt_pk_bf16_f32 v72, v88, v89\n\t" \
  "v_mfma_f32_16x16x32_bf16 v[144:147], v[228:231], v[188:191], v[144:147]\n\t" \
  "v_cvt_pk_bf16_f32 v73, v90, v91\n\t" \
  "v_cvt_pk_bf16_f32 v74, v92, v93\n\t" \
  "v_cvt_pk_bf16_f32 v75, v94, v95\n\t" \
  "s_nop 1\n\t" \
  "v_permlane16_swap_b32 v72, v74\n\t" \
  "v_permlane16_swap_b32 v73, v75\n\t" \
  "v_mfma_f32_16x16x32_bf16 v[148:151], v[224:227], v[192:195], v[148:151]\n\t" \
  "buffer_store_dwordx4 v[72:75], v243, s[48:51], s58 offen offset:0 sc1\n\t" \
  "v_mul_f32 v96, v80, v96\n\t" \
  "v_mul_f32 v97, v81, v97\n\t" \
  "v_mul_f32 v98, v82, v98\n\t" \
  "v_mul_f32 v99, v83, v99\n\t" \
  "v_mul_f32 v100, v84, v100\n\t" \
  "v_mfma_f32_16x16x32_bf16 v[148:151], v[228:231], v[196:199], v[148:151]\n\t" \
  "v_mul_f32 v101, v85, v101\n\t" \
  "v_mul_f32 v102, v86, v102\n\t" \
  "v_mul_f32 v103, v87, v103\n\t" \
  "v_mul_f32 v80, 0xbfb8aa3b, v80\n\t" \
  "v_mul_f32 v81, 0xbfb8aa3b, v81\n\t" \
  "s_waitcnt lgkmcnt(2)\n\t" \
  "v_mfma_f32_16x16x32_bf16 v[152:155], v[232:235], v[168:171], v[152:155]\n\t" \
  "v_mul_f32 v82, 0xbfb8aa3b, v82\n\t" \
  "v_mul_f32 v83, 0xbfb8aa3b, v83\n\t" \
  "v_mul_f32 v84, 0xbfb8aa3b, v84\n\t" \
  "v_mul_f32 v85, 0xbfb8aa3b, v85\n\t" \
  "v_mul_f32 v86, 0xbfb8aa3b, v86\n\t" \
  "v_mul_f32 v87, 0xbfb8aa3b, v87\n\t" \
  "s_waitcnt lgkmcnt(0)\n\t" \
  "v_mfma_f32_16x16x32_bf16 v[152:155], v[236:239], v[172:175], v[152:155]\n\t" \
  "v_exp_f32 v80, v80\n\t" \
  "v_exp_f32 v81, v81\n\t" \
  "v_exp_f32 v82, v82\n\t" \
  "v_exp_f32 v83, v83\n\t" \
  "v_exp_f32 v84, v84\n\t" \
  "v_exp_f32 v85, v85\n\t" \
  "v_mfma_f32_16x16x32_bf16 v[156:159], v[232:235], v[176:179], v[156:159]\n\t" \
  "v_exp_f32 v86, v86\n\t" \
  "v_exp_f32 v87, v87\n\t" \
  "v_add_f32 v80, 1.0, v80\n\t" \
  "v_add_f32 v81, 1.0, v81\n\t" \
  "v_add_f32 v82, 1.0, v82\n\t" \
  "v_add_f32 v83, 1.0, v83\n\t" \
  "v_mfma_f32_16x16x32_bf16 v[156:159], v[236:239], v[180:183], v[156:159]\n\t" \
  "v_add_f32 v84, 1.0, v84\n\t" \
  "v_add_f32 v85, 1.0, v85\n\t" \
  "v_add_f32 v86, 1.0, v86\n\t" \
  "v_add_f32 v87, 1.0, v87\n\t" \
  "v_rcp_f32 v80, v80\n\t" \
  "v_mfma_f32_16x16x32_bf16 v[160:163], v[232:235], v[184:187], v[160:163]\n\t" \
  "v_rcp_f32 v81, v81\n\t" \
  "v_rcp_f32 v82, v82\n\t" \
  "v_rcp_f32 v83, v83\n\t" \
  "v_rcp_f32 v84, v84\n\t" \
  "v_rcp_f32 v85, v85\n\t" \
  "v_rcp_f32 v86, v86\n\t" \
  "v_mfma_f32_16x16x32_bf16 v[160:163], v[236:239], v[188:191], v[160:163]\n\t" \
  "v_rcp_f32 v87, v87\n\t" \
  "v_mul_f32 v96, v96, v80\n\t" \
  "v_mul_f32 v97, v97, v81\n\t" \
  "v_mul_f32 v98, v98, v82\n\t" \
  "v_mul_f32 v99, v99, v83\n\t" \
  "v_mul_f32 v100, v100, v84\n\t" \
  "v_mfma_f32_16x16x32_bf16 v[164:167], v[232:235], v[192:195], v[164:167]\n\t" \
  "v_mul_f32 v101, v101, v85\n\t" \
  "v_mul_f32 v102, v102, v86\n\t" \
  "v_mul_f32 v103, v103, v87\n\t" \
  "v_cvt_pk_bf16_f32 v80, v96, v97\n\t" \
  "v_cvt_pk_bf16_f32 v81, v98, v99\n\t" \
  "v_cvt_pk_bf16_f32 v82, v100, v101\n\t" \
  "v_mfma_f32_16x16x32_bf16 v[164:167], v[236:239], v[196:199], v[164:167]\n\t" \
  "v_cvt_pk_bf16_f32 v83, v102, v103\n\t" \
  "s_nop 1\n\t" \
  "v_permlane16_swap_b32 v80, v82\n\t" \
  "v_permlane16_swap_b32 v81, v83\n\t" \
  "buffer_store_dwordx4 v[80:83], v243, s[48:51], s58 offen offset:2048 sc1\n\t" \
  "s_waitcnt lgkmcnt(0)\n\t" \
  "s_barrier\n\t" \
  "s_nop 7\n\t" \
  "s_mov_b64 s[44:45], s[40:41]\n\t" \
  "s_mov_b32 s46, 16531712\n\t" \
  "s_mov_b32 s47, 0x20000\n\t" \
  "s_mov_b32 s52, 0xbfb8aa3b\n\t" \
  "s_mov_b32 s53, 0xbfb8aa3b\n\t" \
  "v_and_b32 v192, 0xff, v4\n\t" \
  "v_and_b32 v193, 0x100, v4\n\t" \
  "v_lshl_add_u32 v192, v193, 2, v192\n\t" \
  "v_and_b32 v193, 0x200, v4\n\t" \
  "v_lshrrev_b32 v193, 1, v193\n\t" \
  "v_add_u32 v192, v192, v193\n\t" \
  "v_add_u32 v193, 4096, v4\n\t" \
  "s_mov_b64 s[48:49], s[40:41]\n\t" \
  "s_mov_b32 s50, 16531712\n\t" \
  "s_mov_b32 s51, 0x20000\n\t" \
  "s_cmp_eq_u32 s97, 206\n\t" \
  "s_cselect_b32 s50, 0, s50\n\t" \
  "v_and_b32 v237, 0xf0, v4\n\t" \
  "v_lshrrev_b32 v236, 8, v4\n\t" \
  "v_mul_u32_u24 v236, 1280, v236\n\t" \
  "v_add_u32 v236, v236, v237\n\t" \
  "v_add_u32 v236, s79, v236\n\t" \
  "v_pk_mul_f32 v[168:169], v[104:105], s[52:53]\n\t" \
  "v_pk_mul_f32 v[170:171], v[106:107], s[52:53]\n\t" \
  "v_pk_mul_f32 v[172:173], v[108:109], s[52:53]\n\t" \
  "v_pk_mul_f32 v[174:175], v[110:111], s[52:53]\n\t" \
  "s_mov_b32 s50, 16531712\n\t" \
  "s_add_u32 s54, s97, 6\n\t" \
  "s_lshr_b32 s55, s54, 2\n\t" \
  "s_lshl_b32 s55, s55, 12\n\t" \
  "s_and_b32 s54, s54, 2\n\t" \
  "s_lshl_b32 s54, s54, 8\n\t" \
  "s_add_u32 s55, s55, s54\n\t" \
  "s_add_u32 s55, s55, s35\n\t" \
  "v_exp_f32 v168, v168\n\t" \
  "v_exp_f32 v169, v169\n\t" \
  "v_exp_f32 v170, v170\n\t" \
  "v_exp_f32 v171, v171\n\t" \
  "global_load_dwordx4 v[196:199], v236, s[36:37] offset:0\n\t" \
  "v_exp_f32 v172, v172\n\t" \
  "v_exp_f32 v173, v173\n\t" \
  "v_exp_f32 v174, v174\n\t" \
  "v_exp_f32 v175, v175\n\t" \
  "v_pk_mul_f32 v[120:121], v[104:105], v[120:121]\n\t" \
  "v_pk_mul_f32 v[122:123], v[106:107], v[122:123]\n\t" \
  "v_pk_mul_f32 v[124:125], v[108:109], v[124:125]\n\t" \
  "v_pk_mul_f32 v[126:127], v[110:111], v[126:127]\n\t" \
  "global_load_dwordx4 v[200:203], v236, s[36:37] offset:256\n\t" \
  "v_pk_add_f32 v[168:169], v[168:169], 1.0 op_sel_hi:[1,0]\n\t" \
  "v_pk_add_f32 v[170:171], v[170:171], 1.0 op_sel_hi:[1,0]\n\t" \
  "v_pk_add_f32 v[172:173], v[172:173], 1.0 op_sel_hi:[1,0]\n\t" \
  "v_pk_add_f32 v[174:175], v[174:175], 1.0 op_sel_hi:[1,0]\n\t" \
  "v_rcp_f32 v168, v168\n\t" \
  "v_rcp_f32 v169, v169\n\t" \
  "v_rcp_f32 v170, v170\n\t" \
  "v_rcp_f32 v171, v171\n\t" \
  "global_load_dwordx4 v[204:207], v236, s[36:37] offset:512\n\t" \
  "v_rcp_f32 v172, v172\n\t" \
  "v_rcp_f32 v173, v173\n\t" \
  "v_rcp_f32 v174, v174\n\t" \
  "v_rcp_f32 v175, v175\n\t" \
  "v_pk_mul_f32 v[120:121], v[120:121], v[168:169]\n\t" \
  "v_pk_mul_f32 v[122:123], v[122:123], v[170:171]\n\t" \
  "v_pk_mul_f32 v[124:125], v[124:125], v[172:173]\n\t" \
  "v_pk_mul_f32 v[126:127], v[126:127], v[174:175]\n\t" \
  "global_load_dwordx4 v[208:211], v236, s[36:37] offset:768\n\t" \
  "v_cvt_pk_bf16_f32 v184, v120, v121\n\t" \
  "v_cvt_pk_bf16_f32 v185, v122, v123\n\t" \
  "v_cvt_pk_bf16_f32 v186, v124, v125\n\t" \
  "v_cvt_pk_bf16_f32 v187, v126, v127\n\t" \
  "v_pk_mul_f32 v[168:169], v[112:113], s[52:53]\n\t" \
  "v_pk_mul_f32 v[170:171], v[114:115], s[52:53]\n\t" \
  "v_pk_mul_f32 v[172:173], v[116:117], s[52:53]\n\t" \
  "v_pk_mul_f32 v[174:175], v[118:119], s[52:53]\n\t" \
  "v_permlane16_swap_b32 v184, v186\n\t" \
  "v_permlane16_swap_b32 v185, v187\n\t" \
  "buffer_store_dwordx4 v[184:187], v192, s[48:51], s55 offen offset:0 sc1\n\t" \
  "v_exp_f32 v168, v168\n\t" \
  "v_exp_f32 v169, v169\n\t" \
  "v_exp_f32 v170, v170\n\t" \
  "v_exp_f32 v171, v171\n\t" \
  "global_load_dwordx4 v[212:215], v236, s[36:37] offset:1024\n\t" \
  "v_exp_f32 v172, v172\n\t" \
  "v_exp_f32 v173, v173\n\t" \
  "v_exp_f32 v174, v174\n\t" \
  "v_exp_f32 v175, v175\n\t" \
  "v_pk_mul_f32 v[128:129], v[112:113], v[128:129]\n\t" \
  "v_pk_mul_f32 v[130:131], v[114:115], v[130:131]\n\t" \
  "v_pk_mul_f32 v[132:133], v[116:117], v[132:133]\n\t" \
  "v_pk_mul_f32 v[134:135], v[118:119], v[134:135]\n\t" \
  "global_load_dwordx4 v[216:219], v237, s[92:93] offset:0\n\t" \
  "v_pk_add_f32 v[168:169], v[168:169], 1.0 op_sel_hi:[1,0]\n\t" \
  "v_pk_add_f32 v[170:171], v[170:171], 1.0 op_sel_hi:[1,0]\n\t" \
  "v_pk_add_f32 v[172:173], v[172:173], 1.0 op_sel_hi:[1,0]\n\t" \
  "v_pk_add_f32 v[174:175], v[174:175], 1.0 op_sel_hi:[1,0]\n\t" \
  "v_rcp_f32 v168, v168\n\t" \
  "v_rcp_f32 v169, v169\n\t" \
  "v_rcp_f32 v170, v170\n\t" \
  "v_rcp_f32 v171, v171\n\t" \
  "global_load_dwordx4 v[220:223], v237, s[92:93] offset:256\n\t" \
  "v_rcp_f32 v172, v172\n\t" \
  "v_rcp_f32 v173, v173\n\t" \
  "v_rcp_f32 v174, v174\n\t" \
  "v_rcp_f32 v175, v175\n\t" \
  "v_pk_mul_f32 v[128:129], v[128:129], v[168:169]\n\t" \
  "v_pk_mul_f32 v[130:131], v[130:131], v[170:171]\n\t" \
  "v_pk_mul_f32 v[132:133], v[132:133], v[172:173]\n\t" \
  "v_pk_mul_f32 v[134:135], v[134:135], v[174:175]\n\t" \
  "global_load_dwordx4 v[224:227], v237, s[92:93] offset:512\n\t" \
  "v_cvt_pk_bf16_f32 v188, v128, v129\n\t" \
  "v_cvt_pk_bf16_f32 v189, v130, v131\n\t" \
  "v_cvt_pk_bf16_f32 v190, v132, v133\n\t" \
  "v_cvt_pk_bf16_f32 v191, v134, v135\n\t" \
  "v_pk_mul_f32 v[168:169], v[136:137], s[52:53]\n\t" \
  "v_pk_mul_f32 v[170:171], v[138:139], s[52:53]\n\t" \
  "v_pk_mul_f32 v[172:173], v[140:141], s[52:53]\n\t" \
  "v_pk_mul_f32 v[174:175], v[142:143], s[52:53]\n\t" \
  "v_permlane16_swap_b32 v188, v190\n\t" \
  "v_permlane16_swap_b32 v189, v191\n\t" \
  "buffer_store_dwordx4 v[188:191], v192, s[48:51], s55 offen offset:2048 sc1\n\t" \
  "s_add_u32 s54, s97, 8\n\t" \
  "s_lshr_b32 s55, s54, 2\n\t" \
  "s_lshl_b32 s55, s55, 12\n\t" \
  "s_and_b32 s54, s54, 2\n\t" \
  "s_lshl_b32 s54, s54, 8\n\t" \
  "s_add_u32 s55, s55, s54\n\t" \
  "s_add_u32 s55, s55, s35\n\t" \
  "v_exp_f32 v168, v168\n\t" \
  "v_exp_f32 v169, v169\n\t" \
  "v_exp_f32 v170, v170\n\t" \
  "v_exp_f32 v171, v171\n\t" \
  "global_load_dwordx4 v[228:231], v237, s[92:93] offset:768\n\t" \
  "v_exp_f32 v172, v172\n\t" \
  "v_exp_f32 v173, v173\n\t" \
  "v_exp_f32 v174, v174\n\t" \
  "v_exp_f32 v175, v175\n\t" \
  "v_pk_mul_f32 v[152:153], v[136:137], v[152:153]\n\t" \
  "v_pk_mul_f32 v[154:155], v[138:139], v[154:155]\n\t" \
  "v_pk_mul_f32 v[156:157], v[140:141], v[156:157]\n\t" \
  "v_pk_mul_f32 v[158:159], v[142:143], v[158:159]\n\t" \
  "global_load_dwordx4 v[232:235], v237, s[92:93] offset:1024\n\t" \
  "v_pk_add_f32 v[168:169], v[168:169], 1.0 op_sel_hi:[1,0]\n\t" \
  "v_pk_add_f32 v[170:171], v[170:171], 1.0 op_sel_hi:[1,0]\n\t" \
  "v_pk_add_f32 v[172:173], v[172:173], 1.0 op_sel_hi:[1,0]\n\t" \
  "v_pk_add_f32 v[174:175], v[174:175], 1.0 op_sel_hi:[1,0]\n\t" \
  "v_rcp_f32 v168, v168\n\t" \
  "v_rcp_f32 v169, v169\n\t" \
  "v_rcp_f32 v170, v170\n\t" \
  "v_rcp_f32 v171, v171\n\t" \
  "v_rcp_f32 v172, v172\n\t" \
  "v_rcp_f32 v173, v173\n\t" \
  "v_rcp_f32 v174, v174\n\t" \
  "v_rcp_f32 v175, v175\n\t" \
  "v_pk_mul_f32 v[152:153], v[152:153], v[168:169]\n\t" \
  "v_pk_mul_f32 v[154:155], v[154:155], v[170:171]\n\t" \
  "v_pk_mul_f32 v[156:157], v[156:157], v[172:173]\n\t" \
  "v_pk_mul_f32 v[158:159], v[158:159], v[174:175]\n\t" \
  "v_cvt_pk_bf16_f32 v184, v152, v153\n\t" \
  "v_cvt_pk_bf16_f32 v185, v154, v155\n\t" \
  "v_cvt_pk_bf16_f32 v186, v156, v157\n\t" \
  "v_cvt_pk_bf16_f32 v187, v158, v159\n\t" \
  "v_pk_mul_f32 v[168:169], v[144:145], s[52:53]\n\t" \
  "v_pk_mul_f32 v[170:171], v[146:147], s[52:53]\n\t" \
  "v_pk_mul_f32 v[172:173], v[148:149], s[52:53]\n\t" \
  "v_pk_mul_f32 v[174:175], v[150:151], s[52:53]\n\t" \
  "v_permlane16_swap_b32 v184, v186\n\t" \
  "v_permlane16_swap_b32 v185, v187\n\t" \
  "buffer_store_dwordx4 v[184:187], v192, s[48:51], s55 offen offset:0 sc1\n\t" \
  "v_exp_f32 v168, v168\n\t" \
  "v_exp_f32 v169, v169\n\t" \
  "v_exp_f32 v170, v170\n\t" \
  "v_exp_f32 v171, v171\n\t" \
  "v_exp_f32 v172, v172\n\t" \
  "v_exp_f32 v173, v173\n\t" \
  "v_exp_f32 v174, v174\n\t" \
  "v_exp_f32 v175, v175\n\t" \
  "v_pk_mul_f32 v[160:161], v[144:145], v[160:161]\n\t" \
  "v_pk_mul_f32 v[162:163], v[146:147], v[162:163]\n\t" \
  "v_pk_mul_f32 v[164:165], v[148:149], v[164:165]\n\t" \
  "v_pk_mul_f32 v[166:167], v[150:151], v[166:167]\n\t" \
  "v_pk_add_f32 v[168:169], v[168:169], 1.0 op_sel_hi:[1,0]\n\t" \
  "v_pk_add_f32 v[170:171], v[170:171], 1.0 op_sel_hi:[1,0]\n\t" \
  "v_pk_add_f32 v[172:173], v[172:173], 1.0 op_sel_hi:[1,0]\n\t" \
  "v_pk_add_f32 v[174:175], v[174:175], 1.0 op_sel_hi:[1,0]\n\t" \
  "v_rcp_f32 v168, v168\n\t" \
  "v_rcp_f32 v169, v169\n\t" \
  "v_rcp_f32 v170, v170\n\t" \
  "v_rcp_f32 v171, v171\n\t" \
  "v_rcp_f32 v172, v172\n\t" \
  "v_rcp_f32 v173, v173\n\t" \
  "v_rcp_f32 v174, v174\n\t" \
  "v_rcp_f32 v175, v175\n\t" \
  "v_pk_mul_f32 v[160:161], v[160:161], v[168:169]\n\t" \
  "v_pk_mul_f32 v[162:163], v[162:163], v[170:171]\n\t" \
  "v_pk_mul_f32 v[164:165], v[164:165], v[172:173]\n\t" \
  "v_pk_mul_f32 v[166:167], v[166:167], v[174:175]\n\t" \
  "v_cvt_pk_bf16_f32 v188, v160, v161\n\t" \
  "v_cvt_pk_bf16_f32 v189, v162, v163\n\t" \
  "v_cvt_pk_bf16_f32 v190, v164, v165\n\t" \
  "v_cvt_pk_bf16_f32 v191, v166, v167\n\t" \
  "s_nop 1\n\t" \
  "v_permlane16_swap_b32 v188, v190\n\t" \
  "v_permlane16_swap_b32 v189, v191\n\t" \
  "buffer_store_dwordx4 v[188:191], v192, s[48:51], s55 offen offset:2048 sc1\n\t" \
  "s_waitcnt vmcnt(2)\n\t" \
  "v_mov_b32 v176, 0\n\t" \
  "v_mov_b32 v177, 0\n\t" \
  "v_dot2c_f32_bf16 v176, v196, v216\n\t" \
  "v_dot2c_f32_bf16 v177, v197, v217\n\t" \
  "v_dot2c_f32_bf16 v176, v198, v218\n\t" \
  "v_dot2c_f32_bf16 v177, v199, v219\n\t" \
  "v_dot2c_f32_bf16 v176, v200, v220\n\t" \
  "v_dot2c_f32_bf16 v177, v201, v221\n\t" \
  "v_dot2c_f32_bf16 v176, v202, v222\n\t" \
  "v_dot2c_f32_bf16 v177, v203, v223\n\t" \
  "v_dot2c_f32_bf16 v176, v204, v224\n\t" \
  "v_dot2c_f32_bf16 v177, v205, v225\n\t" \
  "v_dot2c_f32_bf16 v176, v206, v226\n\t" \
  "v_dot2c_f32_bf16 v177, v207, v227\n\t" \
  "v_dot2c_f32_bf16 v176, v208, v228\n\t" \
  "v_dot2c_f32_bf16 v177, v209, v229\n\t" \
  "v_dot2c_f32_bf16 v176, v210, v230\n\t" \
  "v_dot2c_f32_bf16 v177, v211, v231\n\t" \
  "v_dot2c_f32_bf16 v176, v212, v232\n\t" \
  "v_dot2c_f32_bf16 v177, v213, v233\n\t" \
  "v_dot2c_f32_bf16 v176, v214, v234\n\t" \
  "v_dot2c_f32_bf16 v177, v215, v235\n\t" \
  "v_mov_b32 v180, s74\n\t" \
  "v_mov_b32 v181, s74\n\t" \
  "v_mov_b32 v182, s74\n\t" \
  "v_mov_b32 v183, s74\n\t" \
  "v_add_f32 v176, v176, v177\n\t" \
  "s_nop 1\n\t" \
  "v_add_f32_dpp v176, v176, v176 row_ror:8 row_mask:0xf bank_mask:0xf\n\t" \
  "s_nop 1\n\t" \
  "v_add_f32_dpp v176, v176, v176 row_ror:4 row_mask:0xf bank_mask:0xf\n\t" \
  "s_nop 1\n\t" \
  "v_add_f32_dpp v176, v176, v176 row_ror:2 row_mask:0xf bank_mask:0xf\n\t" \
  "s_nop 1\n\t" \
  "v_add_f32_dpp v176, v176, v176 row_ror:1 row_mask:0xf bank_mask:0xf\n\t" \
  "v_cvt_pk_bf16_f32 v176, v176, v176\n\t" \
  "v_lshlrev_b32 v176, 16, v176\n\t" \
  "v_mul_f32 v176, 0xbfb8aa3b, v176\n\t" \
  "v_exp_f32 v176, v176\n\t" \
  "s_nop 0\n\t" \
  "v_add_f32 v176, 1.0, v176\n\t" \
  "v_rcp_f32 v176, v176\n\t" \
  "s_nop 0\n\t" \
  "v_cvt_pk_bf16_f32 v176, v176, v176\n\t" \
  "v_lshlrev_b32 v176, 16, v176\n\t" \
  "v_mov_b32 v177, s74\n\t" \
  "v_lshrrev_b32 v178, 5, v4\n\t" \
  "s_waitcnt vmcnt(0)\n\t" \
  "s_mov_b64 exec, 0xff\n\t" \
  "buffer_store_dwordx4 v[180:183], v4, s[44:47], s76 offen sc1\n\t" \
  "s_cmp_lg_u32 s77, 0\n\t" \
  "s_cbranch_scc1 EPI_NG_%=\n\t" \
  "s_mov_b32 exec_lo, 0x00010001\n\t" \
  "s_mov_b32 exec_hi, 0x00010001\n\t" \
  "buffer_store_dwordx2 v[176:177], v178, s[44:47], s78 offen sc1\n\t" \
  "EPI_NG_%=:\n\t" \
  "s_mov_b64 exec, -1\n\t"
#define P1_SSM \
  "s_nop 4\n\t" \
  "s_waitcnt vmcnt(12)\n\t" \
  "s_barrier\n\t" \
  "ds_read_b128 v[200:203], v252 offset:0\n\t" \
  "ds_read_b128 v[204:207], v253 offset:0\n\t" \
  "ds_read_b128 v[208:211], v252 offset:8192\n\t" \
  "ds_read_b128 v[212:215], v253 offset:8192\n\t" \
  "ds_read_b128 v[168:171], v254 offset:0\n\t" \
  "ds_read_b128 v[172:175], v255 offset:0\n\t" \
  "ds_read_b128 v[176:179], v254 offset:2048\n\t" \
  "ds_read_b128 v[180:183], v255 offset:2048\n\t" \
  "ds_read_b128 v[184:187], v254 offset:4096\n\t" \
  "ds_read_b128 v[188:191], v255 offset:4096\n\t" \
  "ds_read_b128 v[192:195], v254 offset:6144\n\t" \
  "ds_read_b128 v[196:199], v255 offset:6144\n\t" \
  "s_waitcnt lgkmcnt(7)\n\t" \
  "v_mfma_f32_16x16x32_bf16 v[8:11], v[168:171], v[200:203], 0\n\t" \
  "ds_read_b128 v[216:219], v252 offset:2048\n\t" \
  "s_waitcnt lgkmcnt(7)\n\t" \
  "v_mfma_f32_16x16x32_bf16 v[8:11], v[172:175], v[204:207], v[8:11]\n\t" \
  "ds_read_b128 v[220:223], v253 offset:2048\n\t" \
  "s_waitcnt lgkmcnt(7)\n\t" \
  "v_mfma_f32_16x16x32_bf16 v[12:15], v[176:179], v[200:203], 0\n\t" \
  "v_add_u32 v5, 106496, v5\n\t" \
  "s_add_u32 m0, s88, 57344\n\t" \
  "s_waitcnt lgkmcnt(6)\n\t" \
  "v_mfma_f32_16x16x32_bf16 v[12:15], v[180:183], v[204:207], v[12:15]\n\t" \
  "buffer_load_dwordx4 v246, s[44:47], s52 offen offset:2048 lds\n\t" \
  "s_waitcnt lgkmcnt(5)\n\t" \
  "v_mfma_f32_16x16x32_bf16 v[16:19], v[184:187], v[200:203], 0\n\t" \
  "s_waitcnt lgkmcnt(4)\n\t" \
  "v_mfma_f32_16x16x32_bf16 v[16:19], v[188:191], v[204:207], v[16:19]\n\t" \
  "buffer_load_dwordx4 v247, s[44:47], s52 offen offset:3072 lds\n\t" \
  "s_waitcnt lgkmcnt(3)\n\t" \
  "v_mfma_f32_16x16x32_bf16 v[20:23], v[192:195], v[200:203], 0\n\t" \
  "s_add_u32 m0, s89, 53248\n\t" \
  "s_waitcnt lgkmcnt(2)\n\t" \
  "v_mfma_f32_16x16x32_bf16 v[20:23], v[196:199], v[204:207], v[20:23]\n\t" \
  "buffer_load_dwordx4 v248, s[48:51], s53 offen offset:0 lds\n\t" \
  "v_mfma_f32_16x16x32_bf16 v[24:27], v[168:171], v[208:211], 0\n\t" \
  "ds_read_b128 v[224:227], v252 offset:10240\n\t" \
  "v_mfma_f32_16x16x32_bf16 v[24:27], v[172:175], v[212:215], v[24:27]\n\t" \
  "ds_read_b128 v[228:231], v253 offset:10240\n\t" \
  "v_mfma_f32_16x16x32_bf16 v[28:31], v[176:179], v[208:211], 0\n\t" \
  "s_add_u32 m0, s89, 54272\n\t" \
  "v_mfma_f32_16x16x32_bf16 v[28:31], v[180:183], v[212:215], v[28:31]\n\t" \
  "buffer_load_dwordx4 v249, s[48:51], s53 offen offset:0 lds\n\t" \
  "v_mfma_f32_16x16x32_bf16 v[32:35], v[184:187], v[208:211], 0\n\t" \
  "s_add_u32 m0, s89, 55296\n\t" \
  "v_mfma_f32_16x16x32_bf16 v[32:35], v[188:191], v[212:215], v[32:35]\n\t" \
  "buffer_load_dwordx4 v250, s[48:51], s53 offen offset:0 lds\n\t" \
  "v_mfma_f32_16x16x32_bf16 v[36:39], v[192:195], v[208:211], 0\n\t" \
  "s_add_u32 m0, s89, 56320\n\t" \
  "v_mfma_f32_16x16x32_bf16 v[36:39], v[196:199], v[212:215], v[36:39]\n\t" \
  "buffer_load_dwordx4 v251, s[48:51], s53 offen offset:0 lds\n\t" \
  "s_waitcnt lgkmcnt(3)\n\t" \
  "v_mfma_f32_16x16x32_bf16 v[40:43], v[168:171], v[216:219], 0\n\t" \
  "ds_read_b128 v[200:203], v252 offset:4096\n\t" \
  "s_waitcnt vmcnt(6)\n\t" \
  "s_cmp_eq_u32 s70, 0\n\t" \
  "s_cbranch_scc0 P1ssm_RS_%=\n\t" \
  "ds_read_b64_tr_b16 a[0:1], v5 offset:0\n\t" \
  "ds_read_b64_tr_b16 a[2:3], v5 offset:512\n\t" \
  "ds_read_b64_tr_b16 a[4:5], v5 offset:32\n\t" \
  "ds_read_b64_tr_b16 a[6:7], v5 offset:544\n\t" \
  "ds_read_b64_tr_b16 a[8:9], v5 offset:64\n\t" \
  "ds_read_b64_tr_b16 a[10:11], v5 offset:576\n\t" \
  "ds_read_b64_tr_b16 a[12:13], v5 offset:96\n\t" \
  "ds_read_b64_tr_b16 a[14:15], v5 offset:608\n\t" \
  "s_branch P1ssm_RE_%=\n\t" \
  "P1ssm_RS_%=:\n\t" \
  "ds_read_b64 a[0:1], v5 offset:0\n\t" \
  "ds_read_b64 a[2:3], v5 offset:2048\n\t" \
  "ds_read_b64 a[4:5], v5 offset:32\n\t" \
  "ds_read_b64 a[6:7], v5 offset:2080\n\t" \
  "ds_read_b64 a[8:9], v5 offset:64\n\t" \
  "ds_read_b64 a[10:11], v5 offset:2112\n\t" \
  "ds_read_b64 a[12:13], v5 offset:96\n\t" \
  "ds_read_b64 a[14:15], v5 offset:2144\n\t" \
  "P1ssm_RE_%=:\n\t" \
  "s_waitcnt lgkmcnt(11)\n\t" \
  "v_mfma_f32_16x16x32_bf16 v[40:43], v[172:175], v[220:223], v[40:43]\n\t" \
  "ds_read_b128 v[204:207], v253 offset:4096\n\t" \
  "v_mfma_f32_16x16x32_bf16 v[44:47], v[176:179], v[216:219], 0\n\t" \
  "s_add_u32 s54, s90, 2\n\t" \
  "s_lshl_b32 s52, s54, 7\n\t" \
  "s_add_u32 s52, s52, s42\n\t" \
  "s_lshl_b32 s53, s54, 7\n\t" \
  "s_add_u32 s53, s53, s43\n\t" \
  "s_add_u32 m0, s89, 106496\n\t" \
  "v_mfma_f32_16x16x32_bf16 v[44:47], v[180:183], v[220:223], v[44:47]\n\t" \
  "buffer_load_dwordx4 v248, s[48:51], s53 offen offset:0 lds\n\t" \
  "v_mfma_f32_16x16x32_bf16 v[48:51], v[184:187], v[216:219], 0\n\t" \
  "s_add_u32 m0, s89, 107520\n\t" \
  "v_mfma_f32_16x16x32_bf16 v[48:51], v[188:191], v[220:223], v[48:51]\n\t" \
  "buffer_load_dwordx4 v249, s[48:51], s53 offen offset:0 lds\n\t" \
  "v_mfma_f32_16x16x32_bf16 v[52:55], v[192:195], v[216:219], 0\n\t" \
  "s_add_u32 m0, s89, 108544\n\t" \
  "v_mfma_f32_16x16x32_bf16 v[52:55], v[196:199], v[220:223], v[52:55]\n\t" \
  "buffer_load_dwordx4 v250, s[48:51], s53 offen offset:0 lds\n\t" \
  "s_waitcnt lgkmcnt(11)\n\t" \
  "v_mfma_f32_16x16x32_bf16 v[56:59], v[168:171], v[224:227], 0\n\t" \
  "ds_read_b128 v[208:211], v252 offset:12288\n\t" \
  "s_waitcnt lgkmcnt(11)\n\t" \
  "v_mfma_f32_16x16x32_bf16 v[56:59], v[172:175], v[228:231], v[56:59]\n\t" \
  "ds_read_b128 v[212:215], v253 offset:12288\n\t" \
  "v_mfma_f32_16x16x32_bf16 v[60:63], v[176:179], v[224:227], 0\n\t" \
  "s_add_u32 m0, s89, 109568\n\t" \
  "v_mfma_f32_16x16x32_bf16 v[60:63], v[180:183], v[228:231], v[60:63]\n\t" \
  "buffer_load_dwordx4 v251, s[48:51], s53 offen offset:0 lds\n\t" \
  "v_mfma_f32_16x16x32_bf16 v[64:67], v[184:187], v[224:227], 0\n\t" \
  "s_add_u32 m0, s88, 106496\n\t" \
  "v_mfma_f32_16x16x32_bf16 v[64:67], v[188:191], v[228:231], v[64:67]\n\t" \
  "s_waitcnt lgkmcnt(3)\n\t" \
  "buffer_load_dwordx4 v240, s[44:47], s52 offen offset:0 lds\n\t" \
  "v_mfma_f32_16x16x32_bf16 v[68:71], v[192:195], v[224:227], 0\n\t" \
  "v_mfma_f32_16x16x32_bf16 v[68:71], v[196:199], v[228:231], v[68:71]\n\t" \
  "buffer_load_dwordx4 v241, s[44:47], s52 offen offset:1024 lds\n\t" \
  "v_mfma_f32_16x16x32_bf16 v[72:75], v[168:171], v[200:203], 0\n\t" \
  "ds_read_b128 v[216:219], v252 offset:6144\n\t" \
  "s_waitcnt lgkmcnt(3)\n\t" \
  "v_mfma_f32_16x16x32_bf16 v[72:75], v[172:175], v[204:207], v[72:75]\n\t" \
  "ds_read_b128 v[220:223], v253 offset:6144\n\t" \
  "v_mfma_f32_16x16x32_bf16 v[76:79], v[176:179], v[200:203], 0\n\t" \
  "v_mfma_f32_16x16x32_bf16 v[76:79], v[180:183], v[204:207], v[76:79]\n\t" \
  "buffer_load_dwordx4 v242, s[44:47], s52 offen offset:2048 lds\n\t" \
  "v_mfma_f32_16x16x32_bf16 v[80:83], v[184:187], v[200:203], 0\n\t" \
  "v_mfma_f32_16x16x32_bf16 v[80:83], v[188:191], v[204:207], v[80:83]\n\t" \
  "buffer_load_dwordx4 v243, s[44:47], s52 offen offset:3072 lds\n\t" \
  "v_mfma_f32_16x16x32_bf16 v[84:87], v[192:195], v[200:203], 0\n\t" \
  "s_add_u32 m0, s88, 110592\n\t" \
  "v_mfma_f32_16x16x32_bf16 v[84:87], v[196:199], v[204:207], v[84:87]\n\t" \
  "buffer_load_dwordx4 v244, s[44:47], s52 offen offset:0 lds\n\t" \
  "s_waitcnt lgkmcnt(3)\n\t" \
  "v_mfma_f32_16x16x32_bf16 v[88:91], v[168:171], v[208:211], 0\n\t" \
  "ds_read_b128 v[224:227], v252 offset:14336\n\t" \
  "s_waitcnt lgkmcnt(3)\n\t" \
  "v_mfma_f32_16x16x32_bf16 v[88:91], v[172:175], v[212:215], v[88:91]\n\t" \
  "ds_read_b128 v[228:231], v253 offset:14336\n\t" \
  "v_mfma_f32_16x16x32_bf16 v[92:95], v[176:179], v[208:211], 0\n\t" \
  "v_mfma_f32_16x16x32_bf16 v[92:95], v[180:183], v[212:215], v[92:95]\n\t" \
  "buffer_load_dwordx4 v245, s[44:47], s52 offen offset:1024 lds\n\t" \
  "v_mfma_f32_16x16x32_bf16 v[96:99], v[184:187], v[208:211], 0\n\t" \
  "v_mfma_f32_16x16x32_bf16 v[96:99], v[188:191], v[212:215], v[96:99]\n\t" \
  "buffer_load_dwordx4 v246, s[44:47], s52 offen offset:2048 lds\n\t" \
  "v_mfma_f32_16x16x32_bf16 v[100:103], v[192:195], v[208:211], 0\n\t" \
  "v_mfma_f32_16x16x32_bf16 v[100:103], v[196:199], v[212:215], v[100:103]\n\t" \
  "buffer_load_dwordx4 v247, s[44:47], s52 offen offset:3072 lds\n\t" \
  "s_waitcnt vmcnt(12) lgkmcnt(0)\n\t" \
  "s_barrier\n\t" \
  "v_add_u32 v252, 53248, v252\n\t" \
  "v_add_u32 v253, 53248, v253\n\t" \
  "v_mfma_f32_16x16x32_bf16 v[104:107], v[168:171], v[216:219], 0\n\t" \
  "ds_read_b128 v[200:203], v252 offset:0\n\t" \
  "v_mfma_f32_16x16x32_bf16 v[104:107], v[172:175], v[220:223], v[104:107]\n\t" \
  "ds_read_b128 v[204:207], v253 offset:0\n\t" \
  "s_add_u32 s54, s54, 1\n\t" \
  "s_cmp_eq_u32 s54, 10\n\t" \
  "s_cselect_b32 s54, 2, s54\n\t" \
  "v_mfma_f32_16x16x32_bf16 v[108:111], v[176:179], v[216:219], 0\n\t" \
  "v_mfma_f32_16x16x32_bf16 v[108:111], v[180:183], v[220:223], v[108:111]\n\t" \
  "s_mov_b32 s55, 16531456\n\t" \
  "buffer_load_dword a240, v7, s[64:67], s55 offen sc1\n\t" \
  "s_lshl_b32 s52, s54, 7\n\t" \
  "s_add_u32 s52, s52, s42\n\t" \
  "v_mfma_f32_16x16x32_bf16 v[112:115], v[184:187], v[216:219], 0\n\t" \
  "s_lshl_b32 s53, s54, 7\n\t" \
  "s_add_u32 s53, s53, s43\n\t" \
  "v_mfma_f32_16x16x32_bf16 v[112:115], v[188:191], v[220:223], v[112:115]\n\t" \
  "s_mov_b32 s55, s68\n\t" \
  "buffer_store_dwordx4 a[0:3], v4, s[64:67], s55 offen sc1\n\t" \
  "v_mfma_f32_16x16x32_bf16 v[116:119], v[192:195], v[216:219], 0\n\t" \
  "v_add_u32 v254, 53248, v254\n\t" \
  "v_add_u32 v255, 53248, v255\n\t" \
  "v_mfma_f32_16x16x32_bf16 v[116:119], v[196:199], v[220:223], v[116:119]\n\t" \
  "s_add_u32 s55, s55, s69\n\t" \
  "buffer_store_dwordx4 a[4:7], v4, s[64:67], s55 offen sc1\n\t" \
  "v_mfma_f32_16x16x32_bf16 v[120:123], v[168:171], v[224:227], 0\n\t" \
  "ds_read_b128 v[208:211], v252 offset:8192\n\t" \
  "v_mfma_f32_16x16x32_bf16 v[120:123], v[172:175], v[228:231], v[120:123]\n\t" \
  "ds_read_b128 v[212:215], v253 offset:8192\n\t" \
  "ds_read_b128 v[168:171], v254 offset:0\n\t" \
  "ds_read_b128 v[172:175], v255 offset:0\n\t" \
  "v_mfma_f32_16x16x32_bf16 v[124:127], v[176:179], v[224:227], 0\n\t" \
  "v_mfma_f32_16x16x32_bf16 v[124:127], v[180:183], v[228:231], v[124:127]\n\t" \
  "s_add_u32 s55, s55, s69\n\t" \
  "buffer_store_dwordx4 a[8:11], v4, s[64:67], s55 offen sc1\n\t" \
  "ds_read_b128 v[176:179], v254 offset:2048\n\t" \
  "ds_read_b128 v[180:183], v255 offset:2048\n\t" \
  "v_mfma_f32_16x16x32_bf16 v[128:131], v[184:187], v[224:227], 0\n\t" \
  "v_mfma_f32_16x16x32_bf16 v[128:131], v[188:191], v[228:231], v[128:131]\n\t" \
  "s_add_u32 s55, s55, s69\n\t" \
  "buffer_store_dwordx4 a[12:15], v4, s[64:67], s55 offen sc1\n\t" \
  "ds_read_b128 v[184:187], v254 offset:4096\n\t" \
  "ds_read_b128 v[188:191], v255 offset:4096\n\t" \
  "v_mfma_f32_16x16x32_bf16 v[132:135], v[192:195], v[224:227], 0\n\t" \
  "v_mfma_f32_16x16x32_bf16 v[132:135], v[196:199], v[228:231], v[132:135]\n\t" \
  "ds_read_b128 v[192:195], v254 offset:6144\n\t" \
  "ds_read_b128 v[196:199], v255 offset:6144\n\t" \
  "s_waitcnt lgkmcnt(7)\n\t" \
  "v_mfma_f32_16x16x32_bf16 v[8:11], v[168:171], v[200:203], v[8:11]\n\t" \
  "ds_read_b128 v[216:219], v252 offset:2048\n\t" \
  "s_waitcnt lgkmcnt(7)\n\t" \
  "v_mfma_f32_16x16x32_bf16 v[8:11], v[172:175], v[204:207], v[8:11]\n\t" \
  "ds_read_b128 v[220:223], v253 offset:2048\n\t" \
  "s_waitcnt lgkmcnt(7)\n\t" \
  "v_mfma_f32_16x16x32_bf16 v[12:15], v[176:179], v[200:203], v[12:15]\n\t" \
  "s_waitcnt lgkmcnt(6)\n\t" \
  "v_mfma_f32_16x16x32_bf16 v[12:15], v[180:183], v[204:207], v[12:15]\n\t" \
  "s_add_u32 m0, s88, 0\n\t" \
  "s_waitcnt lgkmcnt(5)\n\t" \
  "v_mfma_f32_16x16x32_bf16 v[16:19], v[184:187], v[200:203], v[16:19]\n\t" \
  "buffer_load_dwordx4 v240, s[44:47], s52 offen offset:0 lds\n\t" \
  "s_waitcnt lgkmcnt(4)\n\t" \
  "v_mfma_f32_16x16x32_bf16 v[16:19], v[188:191], v[204:207], v[16:19]\n\t" \
  "s_waitcnt lgkmcnt(3)\n\t" \
  "v_mfma_f32_16x16x32_bf16 v[20:23], v[192:195], v[200:203], v[20:23]\n\t" \
  "buffer_load_dwordx4 v241, s[44:47], s52 offen offset:1024 lds\n\t" \
  "s_waitcnt lgkmcnt(2)\n\t" \
  "v_mfma_f32_16x16x32_bf16 v[20:23], v[196:199], v[204:207], v[20:23]\n\t" \
  "v_mfma_f32_16x16x32_bf16 v[24:27], v[168:171], v[208:211], v[24:27]\n\t" \
  "ds_read_b128 v[224:227], v252 offset:10240\n\t" \
  "v_mfma_f32_16x16x32_bf16 v[24:27], v[172:175], v[212:215], v[24:27]\n\t" \
  "ds_read_b128 v[228:231], v253 offset:10240\n\t" \
  "v_mfma_f32_16x16x32_bf16 v[28:31], v[176:179], v[208:211], v[28:31]\n\t" \
  "v_mfma_f32_16x16x32_bf16 v[28:31], v[180:183], v[212:215], v[28:31]\n\t" \
  "v_mfma_f32_16x16x32_bf16 v[32:35], v[184:187], v[208:211], v[32:35]\n\t" \
  "buffer_load_dwordx4 v242, s[44:47], s52 offen offset:2048 lds\n\t" \
  "v_mfma_f32_16x16x32_bf16 v[32:35], v[188:191], v[212:215], v[32:35]\n\t" \
  "v_mfma_f32_16x16x32_bf16 v[36:39], v[192:195], v[208:211], v[36:39]\n\t" \
  "buffer_load_dwordx4 v243, s[44:47], s52 offen offset:3072 lds\n\t" \
  "v_mfma_f32_16x16x32_bf16 v[36:39], v[196:199], v[212:215], v[36:39]\n\t" \
  "s_waitcnt lgkmcnt(3)\n\t" \
  "v_mfma_f32_16x16x32_bf16 v[40:43], v[168:171], v[216:219], v[40:43]\n\t" \
  "ds_read_b128 v[200:203], v252 offset:4096\n\t" \
  "s_waitcnt lgkmcnt(3)\n\t" \
  "v_mfma_f32_16x16x32_bf16 v[40:43], v[172:175], v[220:223], v[40:43]\n\t" \
  "ds_read_b128 v[204:207], v253 offset:4096\n\t" \
  "v_mfma_f32_16x16x32_bf16 v[44:47], v[176:179], v[216:219], v[44:47]\n\t" \
  "v_mfma_f32_16x16x32_bf16 v[44:47], v[180:183], v[220:223], v[44:47]\n\t" \
  "s_add_u32 m0, s88, 4096\n\t" \
  "v_mfma_f32_16x16x32_bf16 v[48:51], v[184:187], v[216:219], v[48:51]\n\t" \
  "buffer_load_dwordx4 v244, s[44:47], s52 offen offset:0 lds\n\t" \
  "v_mfma_f32_16x16x32_bf16 v[48:51], v[188:191], v[220:223], v[48:51]\n\t" \
  "v_mfma_f32_16x16x32_bf16 v[52:55], v[192:195], v[216:219], v[52:55]\n\t" \
  "buffer_load_dwordx4 v245, s[44:47], s52 offen offset:1024 lds\n\t" \
  "v_mfma_f32_16x16x32_bf16 v[52:55], v[196:199], v[220:223], v[52:55]\n\t" \
  "s_waitcnt lgkmcnt(3)\n\t" \
  "v_mfma_f32_16x16x32_bf16 v[56:59], v[168:171], v[224:227], v[56:59]\n\t" \
  "ds_read_b128 v[208:211], v252 offset:12288\n\t" \
  "s_waitcnt lgkmcnt(3)\n\t" \
  "v_mfma_f32_16x16x32_bf16 v[56:59], v[172:175], v[228:231], v[56:59]\n\t" \
  "ds_read_b128 v[212:215], v253 offset:12288\n\t" \
  "v_mfma_f32_16x16x32_bf16 v[60:63], v[176:179], v[224:227], v[60:63]\n\t" \
  "v_mfma_f32_16x16x32_bf16 v[60:63], v[180:183], v[228:231], v[60:63]\n\t" \
  "v_mfma_f32_16x16x32_bf16 v[64:67], v[184:187], v[224:227], v[64:67]\n\t" \
  "buffer_load_dwordx4 v246, s[44:47], s52 offen offset:2048 lds\n\t" \
  "v_mfma_f32_16x16x32_bf16 v[64:67], v[188:191], v[228:231], v[64:67]\n\t" \
  "v_mfma_f32_16x16x32_bf16 v[68:71], v[192:195], v[224:227], v[68:71]\n\t" \
  "buffer_load_dwordx4 v247, s[44:47], s52 offen offset:3072 lds\n\t" \
  "v_mfma_f32_16x16x32_bf16 v[68:71], v[196:199], v[228:231], v[68:71]\n\t" \
  "s_waitcnt lgkmcnt(3)\n\t" \
  "v_mfma_f32_16x16x32_bf16 v[72:75], v[168:171], v[200:203], v[72:75]\n\t" \
  "ds_read_b128 v[216:219], v252 offset:6144\n\t" \
  "s_waitcnt lgkmcnt(3)\n\t" \
  "v_mfma_f32_16x16x32_bf16 v[72:75], v[172:175], v[204:207], v[72:75]\n\t" \
  "ds_read_b128 v[220:223], v253 offset:6144\n\t" \
  "v_mfma_f32_16x16x32_bf16 v[76:79], v[176:179], v[200:203], v[76:79]\n\t" \
  "v_mfma_f32_16x16x32_bf16 v[76:79], v[180:183], v[204:207], v[76:79]\n\t" \
  "s_add_u32 m0, s89, 0\n\t" \
  "v_mfma_f32_16x16x32_bf16 v[80:83], v[184:187], v[200:203], v[80:83]\n\t" \
  "buffer_load_dwordx4 v248, s[48:51], s53 offen offset:0 lds\n\t" \
  "v_mfma_f32_16x16x32_bf16 v[80:83], v[188:191], v[204:207], v[80:83]\n\t" \
  "s_add_u32 m0, s89, 1024\n\t" \
  "v_mfma_f32_16x16x32_bf16 v[84:87], v[192:195], v[200:203], v[84:87]\n\t" \
  "buffer_load_dwordx4 v249, s[48:51], s53 offen offset:0 lds\n\t" \
  "v_mfma_f32_16x16x32_bf16 v[84:87], v[196:199], v[204:207], v[84:87]\n\t" \
  "s_waitcnt lgkmcnt(3)\n\t" \
  "v_mfma_f32_16x16x32_bf16 v[88:91], v[168:171], v[208:211], v[88:91]\n\t" \
  "ds_read_b128 v[224:227], v252 offset:14336\n\t" \
  "s_waitcnt lgkmcnt(3)\n\t" \
  "v_mfma_f32_16x16x32_bf16 v[88:91], v[172:175], v[212:215], v[88:91]\n\t" \
  "ds_read_b128 v[228:231], v253 offset:14336\n\t" \
  "v_mfma_f32_16x16x32_bf16 v[92:95], v[176:179], v[208:211], v[92:95]\n\t" \
  "v_mfma_f32_16x16x32_bf16 v[92:95], v[180:183], v[212:215], v[92:95]\n\t" \
  "s_add_u32 m0, s89, 2048\n\t" \
  "v_mfma_f32_16x16x32_bf16 v[96:99], v[184:187], v[208:211], v[96:99]\n\t" \
  "buffer_load_dwordx4 v250, s[48:51], s53 offen offset:0 lds\n\t" \
  "v_mfma_f32_16x16x32_bf16 v[96:99], v[188:191], v[212:215], v[96:99]\n\t" \
  "s_add_u32 m0, s89, 3072\n\t" \
  "v_mfma_f32_16x16x32_bf16 v[100:103], v[192:195], v[208:211], v[100:103]\n\t" \
  "buffer_load_dwordx4 v251, s[48:51], s53 offen offset:0 lds\n\t" \
  "v_mfma_f32_16x16x32_bf16 v[100:103], v[196:199], v[212:215], v[100:103]\n\t" \
  "s_waitcnt vmcnt(17) lgkmcnt(0)\n\t" \
  "s_barrier\n\t" \
  "v_add_u32 v252, 53248, v252\n\t" \
  "v_add_u32 v253, 53248, v253\n\t" \
  "v_mfma_f32_16x16x32_bf16 v[104:107], v[168:171], v[216:219], v[104:107]\n\t" \
  "ds_read_b128 v[200:203], v252 offset:0\n\t" \
  "v_mfma_f32_16x16x32_bf16 v[104:107], v[172:175], v[220:223], v[104:107]\n\t" \
  "ds_read_b128 v[204:207], v253 offset:0\n\t" \
  "s_add_u32 s54, s54, 1\n\t" \
  "s_cmp_eq_u32 s54, 10\n\t" \
  "s_cselect_b32 s54, 2, s54\n\t" \
  "v_mfma_f32_16x16x32_bf16 v[108:111], v[176:179], v[216:219], v[108:111]\n\t" \
  "v_mfma_f32_16x16x32_bf16 v[108:111], v[180:183], v[220:223], v[108:111]\n\t" \
  "s_lshl_b32 s52, s54, 7\n\t" \
  "s_add_u32 s52, s52, s42\n\t" \
  "v_mfma_f32_16x16x32_bf16 v[112:115], v[184:187], v[216:219], v[112:115]\n\t" \
  "s_lshl_b32 s53, s54, 7\n\t" \
  "s_add_u32 s53, s53, s43\n\t" \
  "v_mfma_f32_16x16x32_bf16 v[112:115], v[188:191], v[220:223], v[112:115]\n\t" \
  "v_mfma_f32_16x16x32_bf16 v[116:119], v[192:195], v[216:219], v[116:119]\n\t" \
  "v_add_u32 v254, 53248, v254\n\t" \
  "v_add_u32 v255, 53248, v255\n\t" \
  "v_mfma_f32_16x16x32_bf16 v[116:119], v[196:199], v[220:223], v[116:119]\n\t" \
  "v_mfma_f32_16x16x32_bf16 v[120:123], v[168:171], v[224:227], v[120:123]\n\t" \
  "ds_read_b128 v[208:211], v252 offset:8192\n\t" \
  "v_mfma_f32_16x16x32_bf16 v[120:123], v[172:175], v[228:231], v[120:123]\n\t" \
  "ds_read_b128 v[212:215], v253 offset:8192\n\t" \
  "ds_read_b128 v[168:171], v254 offset:0\n\t" \
  "ds_read_b128 v[172:175], v255 offset:0\n\t" \
  "v_mfma_f32_16x16x32_bf16 v[124:127], v[176:179], v[224:227], v[124:127]\n\t" \
  "v_mfma_f32_16x16x32_bf16 v[124:127], v[180:183], v[228:231], v[124:127]\n\t" \
  "ds_read_b128 v[176:179], v254 offset:2048\n\t" \
  "ds_read_b128 v[180:183], v255 offset:2048\n\t" \
  "v_mfma_f32_16x16x32_bf16 v[128:131], v[184:187], v[224:227], v[128:131]\n\t" \
  "v_mfma_f32_16x16x32_bf16 v[128:131], v[188:191], v[228:231], v[128:131]\n\t" \
  "ds_read_b128 v[184:187], v254 offset:4096\n\t" \
  "ds_read_b128 v[188:191], v255 offset:4096\n\t" \
  "v_mfma_f32_16x16x32_bf16 v[132:135], v[192:195], v[224:227], v[132:135]\n\t" \
  "v_mfma_f32_16x16x32_bf16 v[132:135], v[196:199], v[228:231], v[132:135]\n\t" \
  "ds_read_b128 v[192:195], v254 offset:6144\n\t" \
  "ds_read_b128 v[196:199], v255 offset:6144\n\t" \
  "s_waitcnt lgkmcnt(7)\n\t" \
  "v_mfma_f32_16x16x32_bf16 v[8:11], v[168:171], v[200:203], v[8:11]\n\t" \
  "ds_read_b128 v[216:219], v252 offset:2048\n\t" \
  "s_waitcnt lgkmcnt(7)\n\t" \
  "v_mfma_f32_16x16x32_bf16 v[8:11], v[172:175], v[204:207], v[8:11]\n\t" \
  "ds_read_b128 v[220:223], v253 offset:2048\n\t" \
  "s_waitcnt lgkmcnt(7)\n\t" \
  "v_mfma_f32_16x16x32_bf16 v[12:15], v[176:179], v[200:203], v[12:15]\n\t" \
  "s_waitcnt lgkmcnt(6)\n\t" \
  "v_mfma_f32_16x16x32_bf16 v[12:15], v[180:183], v[204:207], v[12:15]\n\t" \
  "s_add_u32 m0, s88, 53248\n\t" \
  "s_waitcnt lgkmcnt(5)\n\t" \
  "v_mfma_f32_16x16x32_bf16 v[16:19], v[184:187], v[200:203], v[16:19]\n\t" \
  "buffer_load_dwordx4 v240, s[44:47], s52 offen offset:0 lds\n\t" \
  "s_waitcnt lgkmcnt(4)\n\t" \
  "v_mfma_f32_16x16x32_bf16 v[16:19], v[188:191], v[204:207], v[16:19]\n\t" \
  "s_waitcnt lgkmcnt(3)\n\t" \
  "v_mfma_f32_16x16x32_bf16 v[20:23], v[192:195], v[200:203], v[20:23]\n\t" \
  "buffer_load_dwordx4 v241, s[44:47], s52 offen offset:1024 lds\n\t" \
  "s_waitcnt lgkmcnt(2)\n\t" \
  "v_mfma_f32_16x16x32_bf16 v[20:23], v[196:199], v[204:207], v[20:23]\n\t" \
  "v_mfma_f32_16x16x32_bf16 v[24:27], v[168:171], v[208:211], v[24:27]\n\t" \
  "ds_read_b128 v[224:227], v252 offset:10240\n\t" \
  "v_mfma_f32_16x16x32_bf16 v[24:27], v[172:175], v[212:215], v[24:27]\n\t" \
  "ds_read_b128 v[228:231], v253 offset:10240\n\t" \
  "v_mfma_f32_16x16x32_bf16 v[28:31], v[176:179], v[208:211], v[28:31]\n\t" \
  "v_mfma_f32_16x16x32_bf16 v[28:31], v[180:183], v[212:215], v[28:31]\n\t" \
  "v_mfma_f32_16x16x32_bf16 v[32:35], v[184:187], v[208:211], v[32:35]\n\t" \
  "buffer_load_dwordx4 v242, s[44:47], s52 offen offset:2048 lds\n\t" \
  "v_mfma_f32_16x16x32_bf16 v[32:35], v[188:191], v[212:215], v[32:35]\n\t" \
  "v_mfma_f32_16x16x32_bf16 v[36:39], v[192:195], v[208:211], v[36:39]\n\t" \
  "buffer_load_dwordx4 v243, s[44:47], s52 offen offset:3072 lds\n\t" \
  "v_mfma_f32_16x16x32_bf16 v[36:39], v[196:199], v[212:215], v[36:39]\n\t" \
  "s_waitcnt lgkmcnt(3)\n\t" \
  "v_mfma_f32_16x16x32_bf16 v[40:43], v[168:171], v[216:219], v[40:43]\n\t" \
  "ds_read_b128 v[200:203], v252 offset:4096\n\t" \
  "s_waitcnt lgkmcnt(3)\n\t" \
  "v_mfma_f32_16x16x32_bf16 v[40:43], v[172:175], v[220:223], v[40:43]\n\t" \
  "ds_read_b128 v[204:207], v253 offset:4096\n\t" \
  "v_mfma_f32_16x16x32_bf16 v[44:47], v[176:179], v[216:219], v[44:47]\n\t" \
  "v_mfma_f32_16x16x32_bf16 v[44:47], v[180:183], v[220:223], v[44:47]\n\t" \
  "s_add_u32 m0, s88, 57344\n\t" \
  "v_mfma_f32_16x16x32_bf16 v[48:51], v[184:187], v[216:219], v[48:51]\n\t" \
  "buffer_load_dwordx4 v244, s[44:47], s52 offen offset:0 lds\n\t" \
  "v_mfma_f32_16x16x32_bf16 v[48:51], v[188:191], v[220:223], v[48:51]\n\t" \
  "v_mfma_f32_16x16x32_bf16 v[52:55], v[192:195], v[216:219], v[52:55]\n\t" \
  "buffer_load_dwordx4 v245, s[44:47], s52 offen offset:1024 lds\n\t" \
  "v_mfma_f32_16x16x32_bf16 v[52:55], v[196:199], v[220:223], v[52:55]\n\t" \
  "s_waitcnt lgkmcnt(3)\n\t" \
  "v_mfma_f32_16x16x32_bf16 v[56:59], v[168:171], v[224:227], v[56:59]\n\t" \
  "ds_read_b128 v[208:211], v252 offset:12288\n\t" \
  "s_waitcnt lgkmcnt(3)\n\t" \
  "v_mfma_f32_16x16x32_bf16 v[56:59], v[172:175], v[228:231], v[56:59]\n\t" \
  "ds_read_b128 v[212:215], v253 offset:12288\n\t" \
  "v_mfma_f32_16x16x32_bf16 v[60:63], v[176:179], v[224:227], v[60:63]\n\t" \
  "v_mfma_f32_16x16x32_bf16 v[60:63], v[180:183], v[228:231], v[60:63]\n\t" \
  "v_mfma_f32_16x16x32_bf16 v[64:67], v[184:187], v[224:227], v[64:67]\n\t" \
  "buffer_load_dwordx4 v246, s[44:47], s52 offen offset:2048 lds\n\t" \
  "v_mfma_f32_16x16x32_bf16 v[64:67], v[188:191], v[228:231], v[64:67]\n\t" \
  "v_mfma_f32_16x16x32_bf16 v[68:71], v[192:195], v[224:227], v[68:71]\n\t" \
  "buffer_load_dwordx4 v247, s[44:47], s52 offen offset:3072 lds\n\t" \
  "v_mfma_f32_16x16x32_bf16 v[68:71], v[196:199], v[228:231], v[68:71]\n\t" \
  "s_waitcnt lgkmcnt(3)\n\t" \
  "v_mfma_f32_16x16x32_bf16 v[72:75], v[168:171], v[200:203], v[72:75]\n\t" \
  "ds_read_b128 v[216:219], v252 offset:6144\n\t" \
  "s_waitcnt lgkmcnt(3)\n\t" \
  "v_mfma_f32_16x16x32_bf16 v[72:75], v[172:175], v[204:207], v[72:75]\n\t" \
  "ds_read_b128 v[220:223], v253 offset:6144\n\t" \
  "v_mfma_f32_16x16x32_bf16 v[76:79], v[176:179], v[200:203], v[76:79]\n\t" \
  "v_mfma_f32_16x16x32_bf16 v[76:79], v[180:183], v[204:207], v[76:79]\n\t" \
  "s_add_u32 m0, s89, 53248\n\t" \
  "v_mfma_f32_16x16x32_bf16 v[80:83], v[184:187], v[200:203], v[80:83]\n\t" \
  "buffer_load_dwordx4 v248, s[48:51], s53 offen offset:0 lds\n\t" \
  "v_mfma_f32_16x16x32_bf16 v[80:83], v[188:191], v[204:207], v[80:83]\n\t" \
  "s_add_u32 m0, s89, 54272\n\t" \
  "v_mfma_f32_16x16x32_bf16 v[84:87], v[192:195], v[200:203], v[84:87]\n\t" \
  "buffer_load_dwordx4 v249, s[48:51], s53 offen offset:0 lds\n\t" \
  "v_mfma_f32_16x16x32_bf16 v[84:87], v[196:199], v[204:207], v[84:87]\n\t" \
  "s_waitcnt lgkmcnt(3)\n\t" \
  "v_mfma_f32_16x16x32_bf16 v[88:91], v[168:171], v[208:211], v[88:91]\n\t" \
  "ds_read_b128 v[224:227], v252 offset:14336\n\t" \
  "s_waitcnt lgkmcnt(3)\n\t" \
  "v_mfma_f32_16x16x32_bf16 v[88:91], v[172:175], v[212:215], v[88:91]\n\t" \
  "ds_read_b128 v[228:231], v253 offset:14336\n\t" \
  "v_mfma_f32_16x16x32_bf16 v[92:95], v[176:179], v[208:211], v[92:95]\n\t" \
  "v_mfma_f32_16x16x32_bf16 v[92:95], v[180:183], v[212:215], v[92:95]\n\t" \
  "s_add_u32 m0, s89, 55296\n\t" \
  "v_mfma_f32_16x16x32_bf16 v[96:99], v[184:187], v[208:211], v[96:99]\n\t" \
  "buffer_load_dwordx4 v250, s[48:51], s53 offen offset:0 lds\n\t" \
  "v_mfma_f32_16x16x32_bf16 v[96:99], v[188:191], v[212:215], v[96:99]\n\t" \
  "s_add_u32 m0, s89, 56320\n\t" \
  "v_mfma_f32_16x16x32_bf16 v[100:103], v[192:195], v[208:211], v[100:103]\n\t" \
  "buffer_load_dwordx4 v251, s[48:51], s53 offen offset:0 lds\n\t" \
  "v_mfma_f32_16x16x32_bf16 v[100:103], v[196:199], v[212:215], v[100:103]\n\t" \
  "s_waitcnt vmcnt(12) lgkmcnt(0)\n\t" \
  "s_barrier\n\t" \
  "v_add_u32 v252, 4294860800, v252\n\t" \
  "v_add_u32 v253, 4294860800, v253\n\t" \
  "v_mfma_f32_16x16x32_bf16 v[104:107], v[168:171], v[216:219], v[104:107]\n\t" \
  "ds_read_b128 v[200:203], v252 offset:0\n\t" \
  "v_mfma_f32_16x16x32_bf16 v[104:107], v[172:175], v[220:223], v[104:107]\n\t" \
  "ds_read_b128 v[204:207], v253 offset:0\n\t" \
  "s_add_u32 s54, s54, 1\n\t" \
  "s_cmp_eq_u32 s54, 10\n\t" \
  "s_cselect_b32 s54, 2, s54\n\t" \
  "v_accvgpr_read_b32 v7, a240\n\t" \
  "s_nop 0\n\t" \
  "v_readfirstlane_b32 s74, v7\n\t" \
  "v_mfma_f32_16x16x32_bf16 v[108:111], v[176:179], v[216:219], v[108:111]\n\t" \
  "v_mfma_f32_16x16x32_bf16 v[108:111], v[180:183], v[220:223], v[108:111]\n\t" \
  "s_lshl_b32 s52, s54, 7\n\t" \
  "s_add_u32 s52, s52, s42\n\t" \
  "s_mul_i32 s74, s74, 1664525\n\t" \
  "s_add_u32 s74, s74, 1013904223\n\t" \
  "s_not_b32 s55, s74\n\t" \
  "v_mfma_f32_16x16x32_bf16 v[112:115], v[184:187], v[216:219], v[112:115]\n\t" \
  "s_lshl_b32 s53, s54, 7\n\t" \
  "s_add_u32 s53, s53, s43\n\t" \
  "v_accvgpr_write_b32 a0, s74\n\t" \
  "v_accvgpr_write_b32 a1, s74\n\t" \
  "v_mfma_f32_16x16x32_bf16 v[112:115], v[188:191], v[220:223], v[112:115]\n\t" \
  "v_accvgpr_write_b32 a2, s74\n\t" \
  "v_accvgpr_write_b32 a3, s74\n\t" \
  "v_mfma_f32_16x16x32_bf16 v[116:119], v[192:195], v[216:219], v[116:119]\n\t" \
  "v_add_u32 v254, 4294860800, v254\n\t" \
  "v_add_u32 v255, 4294860800, v255\n\t" \
  "v_mov_b32 v7, s55\n\t" \
  "s_mov_b64 exec, 0xff\n\t" \
  "buffer_store_dwordx4 a[0:3], v4, s[64:67], s71 offen sc1\n\t" \
  "s_mov_b64 exec, -1\n\t" \
  "v_mfma_f32_16x16x32_bf16 v[116:119], v[196:199], v[220:223], v[116:119]\n\t" \
  "v_mfma_f32_16x16x32_bf16 v[120:123], v[168:171], v[224:227], v[120:123]\n\t" \
  "ds_read_b128 v[208:211], v252 offset:8192\n\t" \
  "v_mfma_f32_16x16x32_bf16 v[120:123], v[172:175], v[228:231], v[120:123]\n\t" \
  "ds_read_b128 v[212:215], v253 offset:8192\n\t" \
  "ds_read_b128 v[168:171], v254 offset:0\n\t" \
  "ds_read_b128 v[172:175], v255 offset:0\n\t" \
  "v_mfma_f32_16x16x32_bf16 v[124:127], v[176:179], v[224:227], v[124:127]\n\t" \
  "v_mfma_f32_16x16x32_bf16 v[124:127], v[180:183], v[228:231], v[124:127]\n\t" \
  "ds_read_b128 v[176:179], v254 offset:2048\n\t" \
  "ds_read_b128 v[180:183], v255 offset:2048\n\t" \
  "v_mfma_f32_16x16x32_bf16 v[128:131], v[184:187], v[224:227], v[128:131]\n\t" \
  "v_mfma_f32_16x16x32_bf16 v[128:131], v[188:191], v[228:231], v[128:131]\n\t" \
  "ds_read_b128 v[184:187], v254 offset:4096\n\t" \
  "ds_read_b128 v[188:191], v255 offset:4096\n\t" \
  "v_mfma_f32_16x16x32_bf16 v[132:135], v[192:195], v[224:227], v[132:135]\n\t" \
  "v_mfma_f32_16x16x32_bf16 v[132:135], v[196:199], v[228:231], v[132:135]\n\t" \
  "ds_read_b128 v[192:195], v254 offset:6144\n\t" \
  "ds_read_b128 v[196:199], v255 offset:6144\n\t" \
  "s_waitcnt lgkmcnt(7)\n\t" \
  "v_mfma_f32_16x16x32_bf16 v[8:11], v[168:171], v[200:203], v[8:11]\n\t" \
  "ds_read_b128 v[216:219], v252 offset:2048\n\t" \
  "s_waitcnt lgkmcnt(7)\n\t" \
  "v_mfma_f32_16x16x32_bf16 v[8:11], v[172:175], v[204:207], v[8:11]\n\t" \
  "ds_read_b128 v[220:223], v253 offset:2048\n\t" \
  "s_waitcnt lgkmcnt(7)\n\t" \
  "v_mfma_f32_16x16x32_bf16 v[12:15], v[176:179], v[200:203], v[12:15]\n\t" \
  "s_waitcnt lgkmcnt(6)\n\t" \
  "v_mfma_f32_16x16x32_bf16 v[12:15], v[180:183], v[204:207], v[12:15]\n\t" \
  "s_add_u32 m0, s88, 106496\n\t" \
  "s_waitcnt lgkmcnt(5)\n\t" \
  "v_mfma_f32_16x16x32_bf16 v[16:19], v[184:187], v[200:203], v[16:19]\n\t" \
  "buffer_load_dwordx4 v240, s[44:47], s52 offen offset:0 lds\n\t" \
  "s_waitcnt lgkmcnt(4)\n\t" \
  "v_mfma_f32_16x16x32_bf16 v[16:19], v[188:191], v[204:207], v[16:19]\n\t" \
  "s_waitcnt lgkmcnt(3)\n\t" \
  "v_mfma_f32_16x16x32_bf16 v[20:23], v[192:195], v[200:203], v[20:23]\n\t" \
  "buffer_load_dwordx4 v241, s[44:47], s52 offen offset:1024 lds\n\t" \
  "s_waitcnt lgkmcnt(2)\n\t" \
  "v_mfma_f32_16x16x32_bf16 v[20:23], v[196:199], v[204:207], v[20:23]\n\t" \
  "v_mfma_f32_16x16x32_bf16 v[24:27], v[168:171], v[208:211], v[24:27]\n\t" \
  "ds_read_b128 v[224:227], v252 offset:10240\n\t" \
  "v_mfma_f32_16x16x32_bf16 v[24:27], v[172:175], v[212:215], v[24:27]\n\t" \
  "ds_read_b128 v[228:231], v253 offset:10240\n\t" \
  "v_mfma_f32_16x16x32_bf16 v[28:31], v[176:179], v[208:211], v[28:31]\n\t" \
  "v_mfma_f32_16x16x32_bf16 v[28:31], v[180:183], v[212:215], v[28:31]\n\t" \
  "v_mfma_f32_16x16x32_bf16 v[32:35], v[184:187], v[208:211], v[32:35]\n\t" \
  "buffer_load_dwordx4 v242, s[44:47], s52 offen offset:2048 lds\n\t" \
  "v_mfma_f32_16x16x32_bf16 v[32:35], v[188:191], v[212:215], v[32:35]\n\t" \
  "v_mfma_f32_16x16x32_bf16 v[36:39], v[192:195], v[208:211], v[36:39]\n\t" \
  "buffer_load_dwordx4 v243, s[44:47], s52 offen offset:3072 lds\n\t" \
  "v_mfma_f32_16x16x32_bf16 v[36:39], v[196:199], v[212:215], v[36:39]\n\t" \
  "s_waitcnt lgkmcnt(3)\n\t" \
  "v_mfma_f32_16x16x32_bf16 v[40:43], v[168:171], v[216:219], v[40:43]\n\t" \
  "ds_read_b128 v[200:203], v252 offset:4096\n\t" \
  "s_waitcnt lgkmcnt(3)\n\t" \
  "v_mfma_f32_16x16x32_bf16 v[40:43], v[172:175], v[220:223], v[40:43]\n\t" \
  "ds_read_b128 v[204:207], v253 offset:4096\n\t" \
  "v_mfma_f32_16x16x32_bf16 v[44:47], v[176:179], v[216:219], v[44:47]\n\t" \
  "v_mfma_f32_16x16x32_bf16 v[44:47], v[180:183], v[220:223], v[44:47]\n\t" \
  "s_add_u32 m0, s88, 110592\n\t" \
  "v_mfma_f32_16x16x32_bf16 v[48:51], v[184:187], v[216:219], v[48:51]\n\t" \
  "buffer_load_dwordx4 v244, s[44:47], s52 offen offset:0 lds\n\t" \
  "v_mfma_f32_16x16x32_bf16 v[48:51], v[188:191], v[220:223], v[48:51]\n\t" \
  "v_mfma_f32_16x16x32_bf16 v[52:55], v[192:195], v[216:219], v[52:55]\n\t" \
  "buffer_load_dwordx4 v245, s[44:47], s52 offen offset:1024 lds\n\t" \
  "v_mfma_f32_16x16x32_bf16 v[52:55], v[196:199], v[220:223], v[52:55]\n\t" \
  "s_waitcnt lgkmcnt(3)\n\t" \
  "v_mfma_f32_16x16x32_bf16 v[56:59], v[168:171], v[224:227], v[56:59]\n\t" \
  "ds_read_b128 v[208:211], v252 offset:12288\n\t" \
  "s_waitcnt lgkmcnt(3)\n\t" \
  "v_mfma_f32_16x16x32_bf16 v[56:59], v[172:175], v[228:231], v[56:59]\n\t" \
  "ds_read_b128 v[212:215], v253 offset:12288\n\t" \
  "v_mfma_f32_16x16x32_bf16 v[60:63], v[176:179], v[224:227], v[60:63]\n\t" \
  "v_mfma_f32_16x16x32_bf16 v[60:63], v[180:183], v[228:231], v[60:63]\n\t" \
  "v_mfma_f32_16x16x32_bf16 v[64:67], v[184:187], v[224:227], v[64:67]\n\t" \
  "buffer_load_dwordx4 v246, s[44:47], s52 offen offset:2048 lds\n\t" \
  "v_mfma_f32_16x16x32_bf16 v[64:67], v[188:191], v[228:231], v[64:67]\n\t" \
  "v_mfma_f32_16x16x32_bf16 v[68:71], v[192:195], v[224:227], v[68:71]\n\t" \
  "buffer_load_dwordx4 v247, s[44:47], s52 offen offset:3072 lds\n\t" \
  "v_mfma_f32_16x16x32_bf16 v[68:71], v[196:199], v[228:231], v[68:71]\n\t" \
  "s_waitcnt lgkmcnt(3)\n\t" \
  "v_mfma_f32_16x16x32_bf16 v[72:75], v[168:171], v[200:203], v[72:75]\n\t" \
  "ds_read_b128 v[216:219], v252 offset:6144\n\t" \
  "s_waitcnt lgkmcnt(3)\n\t" \
  "v_mfma_f32_16x16x32_bf16 v[72:75], v[172:175], v[204:207], v[72:75]\n\t" \
  "ds_read_b128 v[220:223], v253 offset:6144\n\t" \
  "v_mfma_f32_16x16x32_bf16 v[76:79], v[176:179], v[200:203], v[76:79]\n\t" \
  "v_mfma_f32_16x16x32_bf16 v[76:79], v[180:183], v[204:207], v[76:79]\n\t" \
  "s_add_u32 m0, s89, 106496\n\t" \
  "v_mfma_f32_16x16x32_bf16 v[80:83], v[184:187], v[200:203], v[80:83]\n\t" \
  "buffer_load_dwordx4 v248, s[48:51], s53 offen offset:0 lds\n\t" \
  "v_mfma_f32_16x16x32_bf16 v[80:83], v[188:191], v[204:207], v[80:83]\n\t" \
  "s_add_u32 m0, s89, 107520\n\t" \
  "v_mfma_f32_16x16x32_bf16 v[84:87], v[192:195], v[200:203], v[84:87]\n\t" \
  "buffer_load_dwordx4 v249, s[48:51], s53 offen offset:0 lds\n\t" \
  "v_mfma_f32_16x16x32_bf16 v[84:87], v[196:199], v[204:207], v[84:87]\n\t" \
  "s_waitcnt lgkmcnt(3)\n\t" \
  "v_mfma_f32_16x16x32_bf16 v[88:91], v[168:171], v[208:211], v[88:91]\n\t" \
  "ds_read_b128 v[224:227], v252 offset:14336\n\t" \
  "s_waitcnt lgkmcnt(3)\n\t" \
  "v_mfma_f32_16x16x32_bf16 v[88:91], v[172:175], v[212:215], v[88:91]\n\t" \
  "ds_read_b128 v[228:231], v253 offset:14336\n\t" \
  "v_mfma_f32_16x16x32_bf16 v[92:95], v[176:179], v[208:211], v[92:95]\n\t" \
  "v_mfma_f32_16x16x32_bf16 v[92:95], v[180:183], v[212:215], v[92:95]\n\t" \
  "s_add_u32 m0, s89, 108544\n\t" \
  "v_mfma_f32_16x16x32_bf16 v[96:99], v[184:187], v[208:211], v[96:99]\n\t" \
  "buffer_load_dwordx4 v250, s[48:51], s53 offen offset:0 lds\n\t" \
  "v_mfma_f32_16x16x32_bf16 v[96:99], v[188:191], v[212:215], v[96:99]\n\t" \
  "s_add_u32 m0, s89, 109568\n\t" \
  "v_mfma_f32_16x16x32_bf16 v[100:103], v[192:195], v[208:211], v[100:103]\n\t" \
  "buffer_load_dwordx4 v251, s[48:51], s53 offen offset:0 lds\n\t" \
  "v_mfma_f32_16x16x32_bf16 v[100:103], v[196:199], v[212:215], v[100:103]\n\t" \
  "s_waitcnt vmcnt(13) lgkmcnt(0)\n\t" \
  "s_barrier\n\t" \
  "v_add_u32 v252, 53248, v252\n\t" \
  "v_add_u32 v253, 53248, v253\n\t" \
  "v_mfma_f32_16x16x32_bf16 v[104:107], v[168:171], v[216:219], v[104:107]\n\t" \
  "ds_read_b128 v[200:203], v252 offset:0\n\t" \
  "v_mfma_f32_16x16x32_bf16 v[104:107], v[172:175], v[220:223], v[104:107]\n\t" \
  "ds_read_b128 v[204:207], v253 offset:0\n\t" \
  "s_add_u32 s54, s54, 1\n\t" \
  "s_cmp_eq_u32 s54, 10\n\t" \
  "s_cselect_b32 s54, 2, s54\n\t" \
  "v_mfma_f32_16x16x32_bf16 v[108:111], v[176:179], v[216:219], v[108:111]\n\t" \
  "v_mfma_f32_16x16x32_bf16 v[108:111], v[180:183], v[220:223], v[108:111]\n\t" \
  "s_lshl_b32 s52, s54, 7\n\t" \
  "s_add_u32 s52, s52, s42\n\t" \
  "v_mfma_f32_16x16x32_bf16 v[112:115], v[184:187], v[216:219], v[112:115]\n\t" \
  "s_lshl_b32 s53, s54, 7\n\t" \
  "s_add_u32 s53, s53, s43\n\t" \
  "v_mfma_f32_16x16x32_bf16 v[112:115], v[188:191], v[220:223], v[112:115]\n\t" \
  "v_mfma_f32_16x16x32_bf16 v[116:119], v[192:195], v[216:219], v[116:119]\n\t" \
  "v_add_u32 v254, 53248, v254\n\t" \
  "v_add_u32 v255, 53248, v255\n\t" \
  "v_mfma_f32_16x16x32_bf16 v[116:119], v[196:199], v[220:223], v[116:119]\n\t" \
  "v_mfma_f32_16x16x32_bf16 v[120:123], v[168:171], v[224:227], v[120:123]\n\t" \
  "ds_read_b128 v[208:211], v252 offset:8192\n\t" \
  "v_mfma_f32_16x16x32_bf16 v[120:123], v[172:175], v[228:231], v[120:123]\n\t" \
  "ds_read_b128 v[212:215], v253 offset:8192\n\t" \
  "ds_read_b128 v[168:171], v254 offset:0\n\t" \
  "ds_read_b128 v[172:175], v255 offset:0\n\t" \
  "v_mfma_f32_16x16x32_bf16 v[124:127], v[176:179], v[224:227], v[124:127]\n\t" \
  "v_mfma_f32_16x16x32_bf16 v[124:127], v[180:183], v[228:231], v[124:127]\n\t" \
  "ds_read_b128 v[176:179], v254 offset:2048\n\t" \
  "ds_read_b128 v[180:183], v255 offset:2048\n\t" \
  "v_mfma_f32_16x16x32_bf16 v[128:131], v[184:187], v[224:227], v[128:131]\n\t" \
  "v_mfma_f32_16x16x32_bf16 v[128:131], v[188:191], v[228:231], v[128:131]\n\t" \
  "ds_read_b128 v[184:187], v254 offset:4096\n\t" \
  "ds_read_b128 v[188:191], v255 offset:4096\n\t" \
  "v_mfma_f32_16x16x32_bf16 v[132:135], v[192:195], v[224:227], v[132:135]\n\t" \
  "v_mfma_f32_16x16x32_bf16 v[132:135], v[196:199], v[228:231], v[132:135]\n\t" \
  "ds_read_b128 v[192:195], v254 offset:6144\n\t" \
  "ds_read_b128 v[196:199], v255 offset:6144\n\t" \
  "s_waitcnt lgkmcnt(7)\n\t" \
  "v_mfma_f32_16x16x32_bf16 v[8:11], v[168:171], v[200:203], v[8:11]\n\t" \
  "ds_read_b128 v[216:219], v252 offset:2048\n\t" \
  "s_waitcnt lgkmcnt(7)\n\t" \
  "v_mfma_f32_16x16x32_bf16 v[8:11], v[172:175], v[204:207], v[8:11]\n\t" \
  "ds_read_b128 v[220:223], v253 offset:2048\n\t" \
  "s_waitcnt lgkmcnt(7)\n\t" \
  "v_mfma_f32_16x16x32_bf16 v[12:15], v[176:179], v[200:203], v[12:15]\n\t" \
  "s_waitcnt lgkmcnt(6)\n\t" \
  "v_mfma_f32_16x16x32_bf16 v[12:15], v[180:183], v[204:207], v[12:15]\n\t" \
  "s_add_u32 m0, s88, 0\n\t" \
  "s_waitcnt lgkmcnt(5)\n\t" \
  "v_mfma_f32_16x16x32_bf16 v[16:19], v[184:187], v[200:203], v[16:19]\n\t" \
  "buffer_load_dwordx4 v240, s[44:47], s52 offen offset:0 lds\n\t" \
  "s_waitcnt lgkmcnt(4)\n\t" \
  "v_mfma_f32_16x16x32_bf16 v[16:19], v[188:191], v[204:207], v[16:19]\n\t" \
  "s_waitcnt lgkmcnt(3)\n\t" \
  "v_mfma_f32_16x16x32_bf16 v[20:23], v[192:195], v[200:203], v[20:23]\n\t" \
  "buffer_load_dwordx4 v241, s[44:47], s52 offen offset:1024 lds\n\t" \
  "s_waitcnt lgkmcnt(2)\n\t" \
  "v_mfma_f32_16x16x32_bf16 v[20:23], v[196:199], v[204:207], v[20:23]\n\t" \
  "v_mfma_f32_16x16x32_bf16 v[24:27], v[168:171], v[208:211], v[24:27]\n\t" \
  "ds_read_b128 v[224:227], v252 offset:10240\n\t" \
  "v_mfma_f32_16x16x32_bf16 v[24:27], v[172:175], v[212:215], v[24:27]\n\t" \
  "ds_read_b128 v[228:231], v253 offset:10240\n\t" \
  "v_mfma_f32_16x16x32_bf16 v[28:31], v[176:179], v[208:211], v[28:31]\n\t" \
  "v_mfma_f32_16x16x32_bf16 v[28:31], v[180:183], v[212:215], v[28:31]\n\t" \
  "v_mfma_f32_16x16x32_bf16 v[32:35], v[184:187], v[208:211], v[32:35]\n\t" \
  "buffer_load_dwordx4 v242, s[44:47], s52 offen offset:2048 lds\n\t" \
  "v_mfma_f32_16x16x32_bf16 v[32:35], v[188:191], v[212:215], v[32:35]\n\t" \
  "v_mfma_f32_16x16x32_bf16 v[36:39], v[192:195], v[208:211], v[36:39]\n\t" \
  "buffer_load_dwordx4 v243, s[44:47], s52 offen offset:3072 lds\n\t" \
  "v_mfma_f32_16x16x32_bf16 v[36:39], v[196:199], v[212:215], v[36:39]\n\t" \
  "s_waitcnt lgkmcnt(3)\n\t" \
  "v_mfma_f32_16x16x32_bf16 v[40:43], v[168:171], v[216:219], v[40:43]\n\t" \
  "ds_read_b128 v[200:203], v252 offset:4096\n\t" \
  "s_waitcnt lgkmcnt(3)\n\t" \
  "v_mfma_f32_16x16x32_bf16 v[40:43], v[172:175], v[220:223], v[40:43]\n\t" \
  "ds_read_b128 v[204:207], v253 offset:4096\n\t" \
  "v_mfma_f32_16x16x32_bf16 v[44:47], v[176:179], v[216:219], v[44:47]\n\t" \
  "v_mfma_f32_16x16x32_bf16 v[44:47], v[180:183], v[220:223], v[44:47]\n\t" \
  "s_add_u32 m0, s88, 4096\n\t" \
  "v_mfma_f32_16x16x32_bf16 v[48:51], v[184:187], v[216:219], v[48:51]\n\t" \
  "buffer_load_dwordx4 v244, s[44:47], s52 offen offset:0 lds\n\t" \
  "v_mfma_f32_16x16x32_bf16 v[48:51], v[188:191], v[220:223], v[48:51]\n\t" \
  "v_mfma_f32_16x16x32_bf16 v[52:55], v[192:195], v[216:219], v[52:55]\n\t" \
  "buffer_load_dwordx4 v245, s[44:47], s52 offen offset:1024 lds\n\t" \
  "v_mfma_f32_16x16x32_bf16 v[52:55], v[196:199], v[220:223], v[52:55]\n\t" \
  "s_waitcnt lgkmcnt(3)\n\t" \
  "v_mfma_f32_16x16x32_bf16 v[56:59], v[168:171], v[224:227], v[56:59]\n\t" \
  "ds_read_b128 v[208:211], v252 offset:12288\n\t" \
  "s_waitcnt lgkmcnt(3)\n\t" \
  "v_mfma_f32_16x16x32_bf16 v[56:59], v[172:175], v[228:231], v[56:59]\n\t" \
  "ds_read_b128 v[212:215], v253 offset:12288\n\t" \
  "v_mfma_f32_16x16x32_bf16 v[60:63], v[176:179], v[224:227], v[60:63]\n\t" \
  "v_mfma_f32_16x16x32_bf16 v[60:63], v[180:183], v[228:231], v[60:63]\n\t" \
  "v_mfma_f32_16x16x32_bf16 v[64:67], v[184:187], v[224:227], v[64:67]\n\t" \
  "buffer_load_dwordx4 v246, s[44:47], s52 offen offset:2048 lds\n\t" \
  "v_mfma_f32_16x16x32_bf16 v[64:67], v[188:191], v[228:231], v[64:67]\n\t" \
  "v_mfma_f32_16x16x32_bf16 v[68:71], v[192:195], v[224:227], v[68:71]\n\t" \
  "buffer_load_dwordx4 v247, s[44:47], s52 offen offset:3072 lds\n\t" \
  "v_mfma_f32_16x16x32_bf16 v[68:71], v[196:199], v[228:231], v[68:71]\n\t" \
  "s_waitcnt lgkmcnt(3)\n\t" \
  "v_mfma_f32_16x16x32_bf16 v[72:75], v[168:171], v[200:203], v[72:75]\n\t" \
  "ds_read_b128 v[216:219], v252 offset:6144\n\t" \
  "s_waitcnt lgkmcnt(3)\n\t" \
  "v_mfma_f32_16x16x32_bf16 v[72:75], v[172:175], v[204:207], v[72:75]\n\t" \
  "ds_read_b128 v[220:223], v253 offset:6144\n\t" \
  "v_mfma_f32_16x16x32_bf16 v[76:79], v[176:179], v[200:203], v[76:79]\n\t" \
  "v_mfma_f32_16x16x32_bf16 v[76:79], v[180:183], v[204:207], v[76:79]\n\t" \
  "s_add_u32 m0, s89, 0\n\t" \
  "v_mfma_f32_16x16x32_bf16 v[80:83], v[184:187], v[200:203], v[80:83]\n\t" \
  "buffer_load_dwordx4 v248, s[48:51], s53 offen offset:0 lds\n\t" \
  "v_mfma_f32_16x16x32_bf16 v[80:83], v[188:191], v[204:207], v[80:83]\n\t" \
  "s_add_u32 m0, s89, 1024\n\t" \
  "v_mfma_f32_16x16x32_bf16 v[84:87], v[192:195], v[200:203], v[84:87]\n\t" \
  "buffer_load_dwordx4 v249, s[48:51], s53 offen offset:0 lds\n\t" \
  "v_mfma_f32_16x16x32_bf16 v[84:87], v[196:199], v[204:207], v[84:87]\n\t" \
  "s_waitcnt lgkmcnt(3)\n\t" \
  "v_mfma_f32_16x16x32_bf16 v[88:91], v[168:171], v[208:211], v[88:91]\n\t" \
  "ds_read_b128 v[224:227], v252 offset:14336\n\t" \
  "s_waitcnt lgkmcnt(3)\n\t" \
  "v_mfma_f32_16x16x32_bf16 v[88:91], v[172:175], v[212:215], v[88:91]\n\t" \
  "ds_read_b128 v[228:231], v253 offset:14336\n\t" \
  "v_mfma_f32_16x16x32_bf16 v[92:95], v[176:179], v[208:211], v[92:95]\n\t" \
  "v_mfma_f32_16x16x32_bf16 v[92:95], v[180:183], v[212:215], v[92:95]\n\t" \
  "s_add_u32 m0, s89, 2048\n\t" \
  "v_mfma_f32_16x16x32_bf16 v[96:99], v[184:187], v[208:211], v[96:99]\n\t" \
  "buffer_load_dwordx4 v250, s[48:51], s53 offen offset:0 lds\n\t" \
  "v_mfma_f32_16x16x32_bf16 v[96:99], v[188:191], v[212:215], v[96:99]\n\t" \
  "s_add_u32 m0, s89, 3072\n\t" \
  "v_mfma_f32_16x16x32_bf16 v[100:103], v[192:195], v[208:211], v[100:103]\n\t" \
  "buffer_load_dwordx4 v251, s[48:51], s53 offen offset:0 lds\n\t" \
  "v_mfma_f32_16x16x32_bf16 v[100:103], v[196:199], v[212:215], v[100:103]\n\t" \
  "s_waitcnt vmcnt(12) lgkmcnt(0)\n\t" \
  "s_barrier\n\t" \
  "v_add_u32 v252, 53248, v252\n\t" \
  "v_add_u32 v253, 53248, v253\n\t" \
  "v_mfma_f32_16x16x32_bf16 v[104:107], v[168:171], v[216:219], v[104:107]\n\t" \
  "ds_read_b128 v[200:203], v252 offset:0\n\t" \
  "v_mfma_f32_16x16x32_bf16 v[104:107], v[172:175], v[220:223], v[104:107]\n\t" \
  "ds_read_b128 v[204:207], v253 offset:0\n\t" \
  "s_add_u32 s54, s54, 1\n\t" \
  "s_cmp_eq_u32 s54, 10\n\t" \
  "s_cselect_b32 s54, 2, s54\n\t" \
  "v_mfma_f32_16x16x32_bf16 v[108:111], v[176:179], v[216:219], v[108:111]\n\t" \
  "v_mfma_f32_16x16x32_bf16 v[108:111], v[180:183], v[220:223], v[108:111]\n\t" \
  "s_lshl_b32 s52, s54, 7\n\t" \
  "s_add_u32 s52, s52, s42\n\t" \
  "v_mfma_f32_16x16x32_bf16 v[112:115], v[184:187], v[216:219], v[112:115]\n\t" \
  "s_lshl_b32 s53, s54, 7\n\t" \
  "s_add_u32 s53, s53, s43\n\t" \
  "v_mfma_f32_16x16x32_bf16 v[112:115], v[188:191], v[220:223], v[112:115]\n\t" \
  "v_mfma_f32_16x16x32_bf16 v[116:119], v[192:195], v[216:219], v[116:119]\n\t" \
  "v_add_u32 v254, 53248, v254\n\t" \
  "v_add_u32 v255, 53248, v255\n\t" \
  "v_mfma_f32_16x16x32_bf16 v[116:119], v[196:199], v[220:223], v[116:119]\n\t" \
  "v_mfma_f32_16x16x32_bf16 v[120:123], v[168:171], v[224:227], v[120:123]\n\t" \
  "ds_read_b128 v[208:211], v252 offset:8192\n\t" \
  "v_mfma_f32_16x16x32_bf16 v[120:123], v[172:175], v[228:231], v[120:123]\n\t" \
  "ds_read_b128 v[212:215], v253 offset:8192\n\t" \
  "ds_read_b128 v[168:171], v254 offset:0\n\t" \
  "ds_read_b128 v[172:175], v255 offset:0\n\t" \
  "v_mfma_f32_16x16x32_bf16 v[124:127], v[176:179], v[224:227], v[124:127]\n\t" \
  "v_mfma_f32_16x16x32_bf16 v[124:127], v[180:183], v[228:231], v[124:127]\n\t" \
  "ds_read_b128 v[176:179], v254 offset:2048\n\t" \
  "ds_read_b128 v[180:183], v255 offset:2048\n\t" \
  "v_mfma_f32_16x16x32_bf16 v[128:131], v[184:187], v[224:227], v[128:131]\n\t" \
  "v_mfma_f32_16x16x32_bf16 v[128:131], v[188:191], v[228:231], v[128:131]\n\t" \
  "ds_read_b128 v[184:187], v254 offset:4096\n\t" \
  "ds_read_b128 v[188:191], v255 offset:4096\n\t" \
  "v_mfma_f32_16x16x32_bf16 v[132:135], v[192:195], v[224:227], v[132:135]\n\t" \
  "v_mfma_f32_16x16x32_bf16 v[132:135], v[196:199], v[228:231], v[132:135]\n\t" \
  "ds_read_b128 v[192:195], v254 offset:6144\n\t" \
  "ds_read_b128 v[196:199], v255 offset:6144\n\t" \
  "s_waitcnt lgkmcnt(7)\n\t" \
  "v_mfma_f32_16x16x32_bf16 v[8:11], v[168:171], v[200:203], v[8:11]\n\t" \
  "ds_read_b128 v[216:219], v252 offset:2048\n\t" \
  "s_waitcnt lgkmcnt(7)\n\t" \
  "v_mfma_f32_16x16x32_bf16 v[8:11], v[172:175], v[204:207], v[8:11]\n\t" \
  "ds_read_b128 v[220:223], v253 offset:2048\n\t" \
  "s_waitcnt lgkmcnt(7)\n\t" \
  "v_mfma_f32_16x16x32_bf16 v[12:15], v[176:179], v[200:203], v[12:15]\n\t" \
  "s_waitcnt lgkmcnt(6)\n\t" \
  "v_mfma_f32_16x16x32_bf16 v[12:15], v[180:183], v[204:207], v[12:15]\n\t" \
  "s_add_u32 m0, s88, 53248\n\t" \
  "s_waitcnt lgkmcnt(5)\n\t" \
  "v_mfma_f32_16x16x32_bf16 v[16:19], v[184:187], v[200:203], v[16:19]\n\t" \
  "buffer_load_dwordx4 v240, s[44:47], s52 offen offset:0 lds\n\t" \
  "s_waitcnt lgkmcnt(4)\n\t" \
  "v_mfma_f32_16x16x32_bf16 v[16:19], v[188:191], v[204:207], v[16:19]\n\t" \
  "s_waitcnt lgkmcnt(3)\n\t" \
  "v_mfma_f32_16x16x32_bf16 v[20:23], v[192:195], v[200:203], v[20:23]\n\t" \
  "buffer_load_dwordx4 v241, s[44:47], s52 offen offset:1024 lds\n\t" \
  "s_waitcnt lgkmcnt(2)\n\t" \
  "v_mfma_f32_16x16x32_bf16 v[20:23], v[196:199], v[204:207], v[20:23]\n\t" \
  "v_mfma_f32_16x16x32_bf16 v[24:27], v[168:171], v[208:211], v[24:27]\n\t" \
  "ds_read_b128 v[224:227], v252 offset:10240\n\t" \
  "v_mfma_f32_16x16x32_bf16 v[24:27], v[172:175], v[212:215], v[24:27]\n\t" \
  "ds_read_b128 v[228:231], v253 offset:10240\n\t" \
  "v_mfma_f32_16x16x32_bf16 v[28:31], v[176:179], v[208:211], v[28:31]\n\t" \
  "v_mfma_f32_16x16x32_bf16 v[28:31], v[180:183], v[212:215], v[28:31]\n\t" \
  "v_mfma_f32_16x16x32_bf16 v[32:35], v[184:187], v[208:211], v[32:35]\n\t" \
  "buffer_load_dwordx4 v242, s[44:47], s52 offen offset:2048 lds\n\t" \
  "v_mfma_f32_16x16x32_bf16 v[32:35], v[188:191], v[212:215], v[32:35]\n\t" \
  "v_mfma_f32_16x16x32_bf16 v[36:39], v[192:195], v[208:211], v[36:39]\n\t" \
  "buffer_load_dwordx4 v243, s[44:47], s52 offen offset:3072 lds\n\t" \
  "v_mfma_f32_16x16x32_bf16 v[36:39], v[196:199], v[212:215], v[36:39]\n\t" \
  "s_waitcnt lgkmcnt(3)\n\t" \
  "v_mfma_f32_16x16x32_bf16 v[40:43], v[168:171], v[216:219], v[40:43]\n\t" \
  "ds_read_b128 v[200:203], v252 offset:4096\n\t" \
  "s_waitcnt lgkmcnt(3)\n\t" \
  "v_mfma_f32_16x16x32_bf16 v[40:43], v[172:175], v[220:223], v[40:43]\n\t" \
  "ds_read_b128 v[204:207], v253 offset:4096\n\t" \
  "v_mfma_f32_16x16x32_bf16 v[44:47], v[176:179], v[216:219], v[44:47]\n\t" \
  "v_mfma_f32_16x16x32_bf16 v[44:47], v[180:183], v[220:223], v[44:47]\n\t" \
  "s_add_u32 m0, s88, 57344\n\t" \
  "v_mfma_f32_16x16x32_bf16 v[48:51], v[184:187], v[216:219], v[48:51]\n\t" \
  "buffer_load_dwordx4 v244, s[44:47], s52 offen offset:0 lds\n\t" \
  "v_mfma_f32_16x16x32_bf16 v[48:51], v[188:191], v[220:223], v[48:51]\n\t" \
  "v_mfma_f32_16x16x32_bf16 v[52:55], v[192:195], v[216:219], v[52:55]\n\t" \
  "buffer_load_dwordx4 v245, s[44:47], s52 offen offset:1024 lds\n\t" \
  "v_mfma_f32_16x16x32_bf16 v[52:55], v[196:199], v[220:223], v[52:55]\n\t" \
  "s_waitcnt lgkmcnt(3)\n\t" \
  "v_mfma_f32_16x16x32_bf16 v[56:59], v[168:171], v[224:227], v[56:59]\n\t" \
  "ds_read_b128 v[208:211], v252 offset:12288\n\t" \
  "s_waitcnt lgkmcnt(3)\n\t" \
  "v_mfma_f32_16x16x32_bf16 v[56:59], v[172:175], v[228:231], v[56:59]\n\t" \
  "ds_read_b128 v[212:215], v253 offset:12288\n\t" \
  "v_mfma_f32_16x16x32_bf16 v[60:63], v[176:179], v[224:227], v[60:63]\n\t" \
  "v_mfma_f32_16x16x32_bf16 v[60:63], v[180:183], v[228:231], v[60:63]\n\t" \
  "v_mfma_f32_16x16x32_bf16 v[64:67], v[184:187], v[224:227], v[64:67]\n\t" \
  "buffer_load_dwordx4 v246, s[44:47], s52 offen offset:2048 lds\n\t" \
  "v_mfma_f32_16x16x32_bf16 v[64:67], v[188:191], v[228:231], v[64:67]\n\t" \
  "v_mfma_f32_16x16x32_bf16 v[68:71], v[192:195], v[224:227], v[68:71]\n\t" \
  "buffer_load_dwordx4 v247, s[44:47], s52 offen offset:3072 lds\n\t" \
  "v_mfma_f32_16x16x32_bf16 v[68:71], v[196:199], v[228:231], v[68:71]\n\t" \
  "s_waitcnt lgkmcnt(3)\n\t" \
  "v_mfma_f32_16x16x32_bf16 v[72:75], v[168:171], v[200:203], v[72:75]\n\t" \
  "ds_read_b128 v[216:219], v252 offset:6144\n\t" \
  "s_waitcnt lgkmcnt(3)\n\t" \
  "v_mfma_f32_16x16x32_bf16 v[72:75], v[172:175], v[204:207], v[72:75]\n\t" \
  "ds_read_b128 v[220:223], v253 offset:6144\n\t" \
  "v_mfma_f32_16x16x32_bf16 v[76:79], v[176:179], v[200:203], v[76:79]\n\t" \
  "buffer_load_dword v7, v6, s[64:67], 0 offen sc1\n\t" \
  "v_mfma_f32_16x16x32_bf16 v[76:79], v[180:183], v[204:207], v[76:79]\n\t" \
  "s_add_u32 m0, s89, 53248\n\t" \
  "v_mfma_f32_16x16x32_bf16 v[80:83], v[184:187], v[200:203], v[80:83]\n\t" \
  "buffer_load_dwordx4 v248, s[48:51], s53 offen offset:0 lds\n\t" \
  "v_mfma_f32_16x16x32_bf16 v[80:83], v[188:191], v[204:207], v[80:83]\n\t" \
  "s_add_u32 m0, s89, 54272\n\t" \
  "v_mfma_f32_16x16x32_bf16 v[84:87], v[192:195], v[200:203], v[84:87]\n\t" \
  "buffer_load_dwordx4 v249, s[48:51], s53 offen offset:0 lds\n\t" \
  "v_mfma_f32_16x16x32_bf16 v[84:87], v[196:199], v[204:207], v[84:87]\n\t" \
  "s_waitcnt lgkmcnt(3)\n\t" \
  "v_mfma_f32_16x16x32_bf16 v[88:91], v[168:171], v[208:211], v[88:91]\n\t" \
  "ds_read_b128 v[224:227], v252 offset:14336\n\t" \
  "s_waitcnt lgkmcnt(3)\n\t" \
  "v_mfma_f32_16x16x32_bf16 v[88:91], v[172:175], v[212:215], v[88:91]\n\t" \
  "ds_read_b128 v[228:231], v253 offset:14336\n\t" \
  "v_mfma_f32_16x16x32_bf16 v[92:95], v[176:179], v[208:211], v[92:95]\n\t" \
  "v_mfma_f32_16x16x32_bf16 v[92:95], v[180:183], v[212:215], v[92:95]\n\t" \
  "s_add_u32 m0, s89, 55296\n\t" \
  "v_mfma_f32_16x16x32_bf16 v[96:99], v[184:187], v[208:211], v[96:99]\n\t" \
  "buffer_load_dwordx4 v250, s[48:51], s53 offen offset:0 lds\n\t" \
  "v_mfma_f32_16x16x32_bf16 v[96:99], v[188:191], v[212:215], v[96:99]\n\t" \
  "s_add_u32 m0, s89, 56320\n\t" \
  "v_mfma_f32_16x16x32_bf16 v[100:103], v[192:195], v[208:211], v[100:103]\n\t" \
  "buffer_load_dwordx4 v251, s[48:51], s53 offen offset:0 lds\n\t" \
  "v_mfma_f32_16x16x32_bf16 v[100:103], v[196:199], v[212:215], v[100:103]\n\t" \
  "s_waitcnt vmcnt(13) lgkmcnt(0)\n\t" \
  "s_barrier\n\t" \
  "v_add_u32 v252, 4294860800, v252\n\t" \
  "v_add_u32 v253, 4294860800, v253\n\t" \
  "v_mfma_f32_16x16x32_bf16 v[104:107], v[168:171], v[216:219], v[104:107]\n\t" \
  "ds_read_b128 v[200:203], v252 offset:0\n\t" \
  "v_mfma_f32_16x16x32_bf16 v[104:107], v[172:175], v[220:223], v[104:107]\n\t" \
  "ds_read_b128 v[204:207], v253 offset:0\n\t" \
  "s_add_u32 s54, s54, 1\n\t" \
  "s_cmp_eq_u32 s54, 10\n\t" \
  "s_cselect_b32 s54, 2, s54\n\t" \
  "v_mfma_f32_16x16x32_bf16 v[108:111], v[176:179], v[216:219], v[108:111]\n\t" \
  "v_mfma_f32_16x16x32_bf16 v[108:111], v[180:183], v[220:223], v[108:111]\n\t" \
  "s_lshl_b32 s52, s54, 7\n\t" \
  "s_add_u32 s52, s52, s42\n\t" \
  "v_mfma_f32_16x16x32_bf16 v[112:115], v[184:187], v[216:219], v[112:115]\n\t" \
  "s_lshl_b32 s53, s54, 7\n\t" \
  "s_add_u32 s53, s53, s43\n\t" \
  "v_mfma_f32_16x16x32_bf16 v[112:115], v[188:191], v[220:223], v[112:115]\n\t" \
  "v_mfma_f32_16x16x32_bf16 v[116:119], v[192:195], v[216:219], v[116:119]\n\t" \
  "v_add_u32 v254, 4294860800, v254\n\t" \
  "v_add_u32 v255, 4294860800, v255\n\t" \
  "v_mfma_f32_16x16x32_bf16 v[116:119], v[196:199], v[220:223], v[116:119]\n\t" \
  "v_mfma_f32_16x16x32_bf16 v[120:123], v[168:171], v[224:227], v[120:123]\n\t" \
  "ds_read_b128 v[208:211], v252 offset:8192\n\t" \
  "v_mfma_f32_16x16x32_bf16 v[120:123], v[172:175], v[228:231], v[120:123]\n\t" \
  "ds_read_b128 v[212:215], v253 offset:8192\n\t" \
  "ds_read_b128 v[168:171], v254 offset:0\n\t" \
  "ds_read_b128 v[172:175], v255 offset:0\n\t" \
  "v_mfma_f32_16x16x32_bf16 v[124:127], v[176:179], v[224:227], v[124:127]\n\t" \
  "v_mfma_f32_16x16x32_bf16 v[124:127], v[180:183], v[228:231], v[124:127]\n\t" \
  "ds_read_b128 v[176:179], v254 offset:2048\n\t" \
  "ds_read_b128 v[180:183], v255 offset:2048\n\t" \
  "v_mfma_f32_16x16x32_bf16 v[128:131], v[184:187], v[224:227], v[128:131]\n\t" \
  "v_mfma_f32_16x16x32_bf16 v[128:131], v[188:191], v[228:231], v[128:131]\n\t" \
  "ds_read_b128 v[184:187], v254 offset:4096\n\t" \
  "ds_read_b128 v[188:191], v255 offset:4096\n\t" \
  "v_mfma_f32_16x16x32_bf16 v[132:135], v[192:195], v[224:227], v[132:135]\n\t" \
  "v_mfma_f32_16x16x32_bf16 v[132:135], v[196:199], v[228:231], v[132:135]\n\t" \
  "ds_read_b128 v[192:195], v254 offset:6144\n\t" \
  "ds_read_b128 v[196:199], v255 offset:6144\n\t" \
  "v_cmp_ne_u32 vcc, s74, v7\n\t" \
  "v_add_u32 v3, 4096, v4\n\t" \
  "s_nop 3\n\t" \
  "s_cbranch_vccz P1ssm_WX_%=\n\t" \
  "P1ssm_W_%=:\n\t" \
  "buffer_load_dword v7, v6, s[64:67], 0 offen sc1\n\t" \
  "s_sleep 4\n\t" \
  "s_add_u32 s75, s75, 1\n\t" \
  "s_cmp_gt_u32 s75, 1048576\n\t" \
  "s_cbranch_scc1 P1ssm_WX_%=\n\t" \
  "v_cmp_ne_u32 vcc, s74, v7\n\t" \
  "s_nop 4\n\t" \
  "s_cbranch_vccnz P1ssm_W_%=\n\t" \
  "P1ssm_WX_%=:\n\t" \
  "s_waitcnt lgkmcnt(7)\n\t" \
  "v_mfma_f32_16x16x32_bf16 v[8:11], v[168:171], v[200:203], v[8:11]\n\t" \
  "ds_read_b128 v[216:219], v252 offset:2048\n\t" \
  "s_waitcnt lgkmcnt(7)\n\t" \
  "v_mfma_f32_16x16x32_bf16 v[8:11], v[172:175], v[204:207], v[8:11]\n\t" \
  "ds_read_b128 v[220:223], v253 offset:2048\n\t" \
  "s_waitcnt lgkmcnt(7)\n\t" \
  "v_mfma_f32_16x16x32_bf16 v[12:15], v[176:179], v[200:203], v[12:15]\n\t" \
  "s_add_u32 m0, s88, 106496\n\t" \
  "s_waitcnt lgkmcnt(6)\n\t" \
  "v_mfma_f32_16x16x32_bf16 v[12:15], v[180:183], v[204:207], v[12:15]\n\t" \
  "buffer_load_dwordx4 v240, s[44:47], s52 offen offset:0 lds\n\t" \
  "s_waitcnt lgkmcnt(5)\n\t" \
  "v_mfma_f32_16x16x32_bf16 v[16:19], v[184:187], v[200:203], v[16:19]\n\t" \
  "s_waitcnt lgkmcnt(4)\n\t" \
  "v_mfma_f32_16x16x32_bf16 v[16:19], v[188:191], v[204:207], v[16:19]\n\t" \
  "buffer_load_dwordx4 v241, s[44:47], s52 offen offset:1024 lds\n\t" \
  "s_waitcnt lgkmcnt(3)\n\t" \
  "v_mfma_f32_16x16x32_bf16 v[20:23], v[192:195], v[200:203], v[20:23]\n\t" \
  "s_waitcnt lgkmcnt(2)\n\t" \
  "v_mfma_f32_16x16x32_bf16 v[20:23], v[196:199], v[204:207], v[20:23]\n\t" \
  "buffer_load_dwordx4 v242, s[44:47], s52 offen offset:2048 lds\n\t" \
  "v_mfma_f32_16x16x32_bf16 v[24:27], v[168:171], v[208:211], v[24:27]\n\t" \
  "ds_read_b128 v[224:227], v252 offset:10240\n\t" \
  "v_mfma_f32_16x16x32_bf16 v[24:27], v[172:175], v[212:215], v[24:27]\n\t" \
  "ds_read_b128 v[228:231], v253 offset:10240\n\t" \
  "v_mfma_f32_16x16x32_bf16 v[28:31], v[176:179], v[208:211], v[28:31]\n\t" \
  "v_mfma_f32_16x16x32_bf16 v[28:31], v[180:183], v[212:215], v[28:31]\n\t" \
  "buffer_load_dwordx4 v243, s[44:47], s52 offen offset:3072 lds\n\t" \
  "v_mfma_f32_16x16x32_bf16 v[32:35], v[184:187], v[208:211], v[32:35]\n\t" \
  "s_add_u32 m0, s88, 110592\n\t" \
  "v_mfma_f32_16x16x32_bf16 v[32:35], v[188:191], v[212:215], v[32:35]\n\t" \
  "buffer_load_dwordx4 v244, s[44:47], s52 offen offset:0 lds\n\t" \
  "v_mfma_f32_16x16x32_bf16 v[36:39], v[192:195], v[208:211], v[36:39]\n\t" \
  "v_mfma_f32_16x16x32_bf16 v[36:39], v[196:199], v[212:215], v[36:39]\n\t" \
  "buffer_load_dwordx4 v245, s[44:47], s52 offen offset:1024 lds\n\t" \
  "s_waitcnt lgkmcnt(3)\n\t" \
  "v_mfma_f32_16x16x32_bf16 v[40:43], v[168:171], v[216:219], v[40:43]\n\t" \
  "ds_read_b128 v[200:203], v252 offset:4096\n\t" \
  "s_waitcnt lgkmcnt(3)\n\t" \
  "v_mfma_f32_16x16x32_bf16 v[40:43], v[172:175], v[220:223], v[40:43]\n\t" \
  "ds_read_b128 v[204:207], v253 offset:4096\n\t" \
  "v_mfma_f32_16x16x32_bf16 v[44:47], v[176:179], v[216:219], v[44:47]\n\t" \
  "v_mfma_f32_16x16x32_bf16 v[44:47], v[180:183], v[220:223], v[44:47]\n\t" \
  "buffer_load_dwordx4 v246, s[44:47], s52 offen offset:2048 lds\n\t" \
  "v_mfma_f32_16x16x32_bf16 v[48:51], v[184:187], v[216:219], v[48:51]\n\t" \
  "v_mfma_f32_16x16x32_bf16 v[48:51], v[188:191], v[220:223], v[48:51]\n\t" \
  "buffer_load_dwordx4 v247, s[44:47], s52 offen offset:3072 lds\n\t" \
  "v_mfma_f32_16x16x32_bf16 v[52:55], v[192:195], v[216:219], v[52:55]\n\t" \
  "s_add_u32 m0, s89, 106496\n\t" \
  "v_mfma_f32_16x16x32_bf16 v[52:55], v[196:199], v[220:223], v[52:55]\n\t" \
  "buffer_load_dwordx4 v248, s[48:51], s53 offen offset:0 lds\n\t" \
  "s_waitcnt lgkmcnt(3)\n\t" \
  "v_mfma_f32_16x16x32_bf16 v[56:59], v[168:171], v[224:227], v[56:59]\n\t" \
  "ds_read_b128 v[208:211], v252 offset:12288\n\t" \
  "s_waitcnt lgkmcnt(3)\n\t" \
  "v_mfma_f32_16x16x32_bf16 v[56:59], v[172:175], v[228:231], v[56:59]\n\t" \
  "ds_read_b128 v[212:215], v253 offset:12288\n\t" \
  "v_mfma_f32_16x16x32_bf16 v[60:63], v[176:179], v[224:227], v[60:63]\n\t" \
  "s_add_u32 m0, s89, 107520\n\t" \
  "v_mfma_f32_16x16x32_bf16 v[60:63], v[180:183], v[228:231], v[60:63]\n\t" \
  "buffer_load_dwordx4 v249, s[48:51], s53 offen offset:0 lds\n\t" \
  "v_mfma_f32_16x16x32_bf16 v[64:67], v[184:187], v[224:227], v[64:67]\n\t" \
  "s_add_u32 m0, s89, 108544\n\t" \
  "v_mfma_f32_16x16x32_bf16 v[64:67], v[188:191], v[228:231], v[64:67]\n\t" \
  "buffer_load_dwordx4 v250, s[48:51], s53 offen offset:0 lds\n\t" \
  "v_mfma_f32_16x16x32_bf16 v[68:71], v[192:195], v[224:227], v[68:71]\n\t" \
  "s_add_u32 m0, s89, 109568\n\t" \
  "v_mfma_f32_16x16x32_bf16 v[68:71], v[196:199], v[228:231], v[68:71]\n\t" \
  "buffer_load_dwordx4 v251, s[48:51], s53 offen offset:0 lds\n\t" \
  "s_waitcnt lgkmcnt(3)\n\t" \
  "v_mfma_f32_16x16x32_bf16 v[72:75], v[168:171], v[200:203], v[72:75]\n\t" \
  "ds_read_b128 v[216:219], v252 offset:6144\n\t" \
  "s_waitcnt lgkmcnt(3)\n\t" \
  "v_mfma_f32_16x16x32_bf16 v[72:75], v[172:175], v[204:207], v[72:75]\n\t" \
  "ds_read_b128 v[220:223], v253 offset:6144\n\t" \
  "v_mfma_f32_16x16x32_bf16 v[76:79], v[176:179], v[200:203], v[76:79]\n\t" \
  "v_mfma_f32_16x16x32_bf16 v[76:79], v[180:183], v[204:207], v[76:79]\n\t" \
  "s_add_u32 s55, s72, 0\n\t" \
  "buffer_load_dwordx4 a[0:3], v4, s[64:67], s55 offen offset:0 sc1\n\t" \
  "v_mfma_f32_16x16x32_bf16 v[80:83], v[184:187], v[200:203], v[80:83]\n\t" \
  "v_mfma_f32_16x16x32_bf16 v[80:83], v[188:191], v[204:207], v[80:83]\n\t" \
  "buffer_load_dwordx4 a[4:7], v4, s[64:67], s55 offen offset:1024 sc1\n\t" \
  "v_mfma_f32_16x16x32_bf16 v[84:87], v[192:195], v[200:203], v[84:87]\n\t" \
  "v_mfma_f32_16x16x32_bf16 v[84:87], v[196:199], v[204:207], v[84:87]\n\t" \
  "buffer_load_dwordx4 a[8:11], v4, s[64:67], s55 offen offset:2048 sc1\n\t" \
  "s_waitcnt lgkmcnt(3)\n\t" \
  "v_mfma_f32_16x16x32_bf16 v[88:91], v[168:171], v[208:211], v[88:91]\n\t" \
  "ds_read_b128 v[224:227], v252 offset:14336\n\t" \
  "s_waitcnt lgkmcnt(3)\n\t" \
  "v_mfma_f32_16x16x32_bf16 v[88:91], v[172:175], v[212:215], v[88:91]\n\t" \
  "ds_read_b128 v[228:231], v253 offset:14336\n\t" \
  "v_mfma_f32_16x16x32_bf16 v[92:95], v[176:179], v[208:211], v[92:95]\n\t" \
  "v_mfma_f32_16x16x32_bf16 v[92:95], v[180:183], v[212:215], v[92:95]\n\t" \
  "buffer_load_dwordx4 a[12:15], v4, s[64:67], s55 offen offset:3072 sc1\n\t" \
  "v_mfma_f32_16x16x32_bf16 v[96:99], v[184:187], v[208:211], v[96:99]\n\t" \
  "v_mfma_f32_16x16x32_bf16 v[96:99], v[188:191], v[212:215], v[96:99]\n\t" \
  "buffer_load_dwordx4 a[16:19], v3, s[64:67], s55 offen offset:0 sc1\n\t" \
  "v_mfma_f32_16x16x32_bf16 v[100:103], v[192:195], v[208:211], v[100:103]\n\t" \
  "v_mfma_f32_16x16x32_bf16 v[100:103], v[196:199], v[212:215], v[100:103]\n\t" \
  "s_add_u32 s55, s72, 40960\n\t" \
  "buffer_load_dwordx4 a[20:23], v4, s[64:67], s55 offen offset:0 sc1\n\t" \
  "s_waitcnt vmcnt(18) lgkmcnt(0)\n\t" \
  "s_barrier\n\t" \
  "v_add_u32 v252, 53248, v252\n\t" \
  "v_add_u32 v253, 53248, v253\n\t" \
  "v_mfma_f32_16x16x32_bf16 v[104:107], v[168:171], v[216:219], v[104:107]\n\t" \
  "ds_read_b128 v[200:203], v252 offset:0\n\t" \
  "v_mfma_f32_16x16x32_bf16 v[104:107], v[172:175], v[220:223], v[104:107]\n\t" \
  "ds_read_b128 v[204:207], v253 offset:0\n\t" \
  "s_add_u32 s54, s54, 1\n\t" \
  "s_cmp_eq_u32 s54, 10\n\t" \
  "s_cselect_b32 s54, 2, s54\n\t" \
  "v_mfma_f32_16x16x32_bf16 v[108:111], v[176:179], v[216:219], v[108:111]\n\t" \
  "v_mfma_f32_16x16x32_bf16 v[108:111], v[180:183], v[220:223], v[108:111]\n\t" \
  "buffer_load_dwordx4 a[24:27], v4, s[64:67], s55 offen offset:1024 sc1\n\t" \
  "s_lshl_b32 s52, s54, 7\n\t" \
  "s_add_u32 s52, s52, s42\n\t" \
  "v_mfma_f32_16x16x32_bf16 v[112:115], v[184:187], v[216:219], v[112:115]\n\t" \
  "s_lshl_b32 s53, s54, 7\n\t" \
  "s_add_u32 s53, s53, s43\n\t" \
  "v_mfma_f32_16x16x32_bf16 v[112:115], v[188:191], v[220:223], v[112:115]\n\t" \
  "v_mfma_f32_16x16x32_bf16 v[116:119], v[192:195], v[216:219], v[116:119]\n\t" \
  "v_add_u32 v254, 53248, v254\n\t" \
  "v_add_u32 v255, 53248, v255\n\t" \
  "v_mfma_f32_16x16x32_bf16 v[116:119], v[196:199], v[220:223], v[116:119]\n\t" \
  "v_mfma_f32_16x16x32_bf16 v[120:123], v[168:171], v[224:227], v[120:123]\n\t" \
  "ds_read_b128 v[208:211], v252 offset:8192\n\t" \
  "v_mfma_f32_16x16x32_bf16 v[120:123], v[172:175], v[228:231], v[120:123]\n\t" \
  "ds_read_b128 v[212:215], v253 offset:8192\n\t" \
  "ds_read_b128 v[168:171], v254 offset:0\n\t" \
  "ds_read_b128 v[172:175], v255 offset:0\n\t" \
  "v_mfma_f32_16x16x32_bf16 v[124:127], v[176:179], v[224:227], v[124:127]\n\t" \
  "v_mfma_f32_16x16x32_bf16 v[124:127], v[180:183], v[228:231], v[124:127]\n\t" \
  "ds_read_b128 v[176:179], v254 offset:2048\n\t" \
  "ds_read_b128 v[180:183], v255 offset:2048\n\t" \
  "v_mfma_f32_16x16x32_bf16 v[128:131], v[184:187], v[224:227], v[128:131]\n\t" \
  "v_mfma_f32_16x16x32_bf16 v[128:131], v[188:191], v[228:231], v[128:131]\n\t" \
  "ds_read_b128 v[184:187], v254 offset:4096\n\t" \
  "ds_read_b128 v[188:191], v255 offset:4096\n\t" \
  "v_mfma_f32_16x16x32_bf16 v[132:135], v[192:195], v[224:227], v[132:135]\n\t" \
  "v_mfma_f32_16x16x32_bf16 v[132:135], v[196:199], v[228:231], v[132:135]\n\t" \
  "ds_read_b128 v[192:195], v254 offset:6144\n\t" \
  "ds_read_b128 v[196:199], v255 offset:6144\n\t" \
  "s_waitcnt lgkmcnt(7)\n\t" \
  "v_mfma_f32_16x16x32_bf16 v[8:11], v[168:171], v[200:203], v[8:11]\n\t" \
  "ds_read_b128 v[216:219], v252 offset:2048\n\t" \
  "s_waitcnt lgkmcnt(7)\n\t" \
  "v_mfma_f32_16x16x32_bf16 v[8:11], v[172:175], v[204:207], v[8:11]\n\t" \
  "ds_read_b128 v[220:223], v253 offset:2048\n\t" \
  "s_waitcnt lgkmcnt(7)\n\t" \
  "v_mfma_f32_16x16x32_bf16 v[12:15], v[176:179], v[200:203], v[12:15]\n\t" \
  "s_add_u32 m0, s88, 0\n\t" \
  "s_waitcnt lgkmcnt(6)\n\t" \
  "v_mfma_f32_16x16x32_bf16 v[12:15], v[180:183], v[204:207], v[12:15]\n\t" \
  "buffer_load_dwordx4 v240, s[44:47], s52 offen offset:0 lds\n\t" \
  "s_waitcnt lgkmcnt(5)\n\t" \
  "v_mfma_f32_16x16x32_bf16 v[16:19], v[184:187], v[200:203], v[16:19]\n\t" \
  "s_waitcnt lgkmcnt(4)\n\t" \
  "v_mfma_f32_16x16x32_bf16 v[16:19], v[188:191], v[204:207], v[16:19]\n\t" \
  "buffer_load_dwordx4 v241, s[44:47], s52 offen offset:1024 lds\n\t" \
  "s_waitcnt lgkmcnt(3)\n\t" \
  "v_mfma_f32_16x16x32_bf16 v[20:23], v[192:195], v[200:203], v[20:23]\n\t" \
  "s_waitcnt lgkmcnt(2)\n\t" \
  "v_mfma_f32_16x16x32_bf16 v[20:23], v[196:199], v[204:207], v[20:23]\n\t" \
  "buffer_load_dwordx4 v242, s[44:47], s52 offen offset:2048 lds\n\t" \
  "v_mfma_f32_16x16x32_bf16 v[24:27], v[168:171], v[208:211], v[24:27]\n\t" \
  "ds_read_b128 v[224:227], v252 offset:10240\n\t" \
  "v_mfma_f32_16x16x32_bf16 v[24:27], v[172:175], v[212:215], v[24:27]\n\t" \
  "ds_read_b128 v[228:231], v253 offset:10240\n\t" \
  "v_mfma_f32_16x16x32_bf16 v[28:31], v[176:179], v[208:211], v[28:31]\n\t" \
  "v_mfma_f32_16x16x32_bf16 v[28:31], v[180:183], v[212:215], v[28:31]\n\t" \
  "buffer_load_dwordx4 v243, s[44:47], s52 offen offset:3072 lds\n\t" \
  "v_mfma_f32_16x16x32_bf16 v[32:35], v[184:187], v[208:211], v[32:35]\n\t" \
  "s_add_u32 m0, s88, 4096\n\t" \
  "v_mfma_f32_16x16x32_bf16 v[32:35], v[188:191], v[212:215], v[32:35]\n\t" \
  "buffer_load_dwordx4 v244, s[44:47], s52 offen offset:0 lds\n\t" \
  "v_mfma_f32_16x16x32_bf16 v[36:39], v[192:195], v[208:211], v[36:39]\n\t" \
  "v_mfma_f32_16x16x32_bf16 v[36:39], v[196:199], v[212:215], v[36:39]\n\t" \
  "buffer_load_dwordx4 v245, s[44:47], s52 offen offset:1024 lds\n\t" \
  "s_waitcnt lgkmcnt(3)\n\t" \
  "v_mfma_f32_16x16x32_bf16 v[40:43], v[168:171], v[216:219], v[40:43]\n\t" \
  "ds_read_b128 v[200:203], v252 offset:4096\n\t" \
  "s_waitcnt lgkmcnt(3)\n\t" \
  "v_mfma_f32_16x16x32_bf16 v[40:43], v[172:175], v[220:223], v[40:43]\n\t" \
  "ds_read_b128 v[204:207], v253 offset:4096\n\t" \
  "v_mfma_f32_16x16x32_bf16 v[44:47], v[176:179], v[216:219], v[44:47]\n\t" \
  "v_mfma_f32_16x16x32_bf16 v[44:47], v[180:183], v[220:223], v[44:47]\n\t" \
  "buffer_load_dwordx4 v246, s[44:47], s52 offen offset:2048 lds\n\t" \
  "v_mfma_f32_16x16x32_bf16 v[48:51], v[184:187], v[216:219], v[48:51]\n\t" \
  "v_mfma_f32_16x16x32_bf16 v[48:51], v[188:191], v[220:223], v[48:51]\n\t" \
  "buffer_load_dwordx4 v247, s[44:47], s52 offen offset:3072 lds\n\t" \
  "v_mfma_f32_16x16x32_bf16 v[52:55], v[192:195], v[216:219], v[52:55]\n\t" \
  "s_add_u32 m0, s89, 0\n\t" \
  "v_mfma_f32_16x16x32_bf16 v[52:55], v[196:199], v[220:223], v[52:55]\n\t" \
  "buffer_load_dwordx4 v248, s[48:51], s53 offen offset:0 lds\n\t" \
  "s_waitcnt lgkmcnt(3)\n\t" \
  "v_mfma_f32_16x16x32_bf16 v[56:59], v[168:171], v[224:227], v[56:59]\n\t" \
  "ds_read_b128 v[208:211], v252 offset:12288\n\t" \
  "s_waitcnt lgkmcnt(3)\n\t" \
  "v_mfma_f32_16x16x32_bf16 v[56:59], v[172:175], v[228:231], v[56:59]\n\t" \
  "ds_read_b128 v[212:215], v253 offset:12288\n\t" \
  "v_mfma_f32_16x16x32_bf16 v[60:63], v[176:179], v[224:227], v[60:63]\n\t" \
  "s_add_u32 m0, s89, 1024\n\t" \
  "v_mfma_f32_16x16x32_bf16 v[60:63], v[180:183], v[228:231], v[60:63]\n\t" \
  "buffer_load_dwordx4 v249, s[48:51], s53 offen offset:0 lds\n\t" \
  "v_mfma_f32_16x16x32_bf16 v[64:67], v[184:187], v[224:227], v[64:67]\n\t" \
  "s_add_u32 m0, s89, 2048\n\t" \
  "v_mfma_f32_16x16x32_bf16 v[64:67], v[188:191], v[228:231], v[64:67]\n\t" \
  "buffer_load_dwordx4 v250, s[48:51], s53 offen offset:0 lds\n\t" \
  "v_mfma_f32_16x16x32_bf16 v[68:71], v[192:195], v[224:227], v[68:71]\n\t" \
  "s_add_u32 m0, s89, 3072\n\t" \
  "v_mfma_f32_16x16x32_bf16 v[68:71], v[196:199], v[228:231], v[68:71]\n\t" \
  "buffer_load_dwordx4 v251, s[48:51], s53 offen offset:0 lds\n\t" \
  "s_waitcnt lgkmcnt(3)\n\t" \
  "v_mfma_f32_16x16x32_bf16 v[72:75], v[168:171], v[200:203], v[72:75]\n\t" \
  "ds_read_b128 v[216:219], v252 offset:6144\n\t" \
  "s_waitcnt lgkmcnt(3)\n\t" \
  "v_mfma_f32_16x16x32_bf16 v[72:75], v[172:175], v[204:207], v[72:75]\n\t" \
  "ds_read_b128 v[220:223], v253 offset:6144\n\t" \
  "v_mfma_f32_16x16x32_bf16 v[76:79], v[176:179], v[200:203], v[76:79]\n\t" \
  "v_mfma_f32_16x16x32_bf16 v[76:79], v[180:183], v[204:207], v[76:79]\n\t" \
  "s_add_u32 s55, s72, 40960\n\t" \
  "buffer_load_dwordx4 a[28:31], v4, s[64:67], s55 offen offset:2048 sc1\n\t" \
  "v_mfma_f32_16x16x32_bf16 v[80:83], v[184:187], v[200:203], v[80:83]\n\t" \
  "v_mfma_f32_16x16x32_bf16 v[80:83], v[188:191], v[204:207], v[80:83]\n\t" \
  "buffer_load_dwordx4 a[32:35], v4, s[64:67], s55 offen offset:3072 sc1\n\t" \
  "v_mfma_f32_16x16x32_bf16 v[84:87], v[192:195], v[200:203], v[84:87]\n\t" \
  "v_mfma_f32_16x16x32_bf16 v[84:87], v[196:199], v[204:207], v[84:87]\n\t" \
  "buffer_load_dwordx4 a[36:39], v3, s[64:67], s55 offen offset:0 sc1\n\t" \
  "s_waitcnt lgkmcnt(3)\n\t" \
  "v_mfma_f32_16x16x32_bf16 v[88:91], v[168:171], v[208:211], v[88:91]\n\t" \
  "ds_read_b128 v[224:227], v252 offset:14336\n\t" \
  "s_waitcnt lgkmcnt(3)\n\t" \
  "v_mfma_f32_16x16x32_bf16 v[88:91], v[172:175], v[212:215], v[88:91]\n\t" \
  "ds_read_b128 v[228:231], v253 offset:14336\n\t" \
  "v_mfma_f32_16x16x32_bf16 v[92:95], v[176:179], v[208:211], v[92:95]\n\t" \
  "v_mfma_f32_16x16x32_bf16 v[92:95], v[180:183], v[212:215], v[92:95]\n\t" \
  "s_add_u32 s55, s72, 81920\n\t" \
  "buffer_load_dwordx4 a[40:43], v4, s[64:67], s55 offen offset:0 sc1\n\t" \
  "v_mfma_f32_16x16x32_bf16 v[96:99], v[184:187], v[208:211], v[96:99]\n\t" \
  "v_mfma_f32_16x16x32_bf16 v[96:99], v[188:191], v[212:215], v[96:99]\n\t" \
  "buffer_load_dwordx4 a[44:47], v4, s[64:67], s55 offen offset:1024 sc1\n\t" \
  "v_mfma_f32_16x16x32_bf16 v[100:103], v[192:195], v[208:211], v[100:103]\n\t" \
  "v_mfma_f32_16x16x32_bf16 v[100:103], v[196:199], v[212:215], v[100:103]\n\t" \
  "buffer_load_dwordx4 a[48:51], v4, s[64:67], s55 offen offset:2048 sc1\n\t" \
  "s_waitcnt vmcnt(25) lgkmcnt(0)\n\t" \
  "s_barrier\n\t" \
  "v_add_u32 v252, 53248, v252\n\t" \
  "v_add_u32 v253, 53248, v253\n\t" \
  "v_mfma_f32_16x16x32_bf16 v[104:107], v[168:171], v[216:219], v[104:107]\n\t" \
  "ds_read_b128 v[200:203], v252 offset:0\n\t" \
  "v_mfma_f32_16x16x32_bf16 v[104:107], v[172:175], v[220:223], v[104:107]\n\t" \
  "ds_read_b128 v[204:207], v253 offset:0\n\t" \
  "v_mfma_f32_16x16x32_bf16 v[108:111], v[176:179], v[216:219], v[108:111]\n\t" \
  "v_mfma_f32_16x16x32_bf16 v[108:111], v[180:183], v[220:223], v[108:111]\n\t" \
  "buffer_load_dwordx4 a[52:55], v4, s[64:67], s55 offen offset:3072 sc1\n\t" \
  "v_mfma_f32_16x16x32_bf16 v[112:115], v[184:187], v[216:219], v[112:115]\n\t" \
  "v_mfma_f32_16x16x32_bf16 v[112:115], v[188:191], v[220:223], v[112:115]\n\t" \
  "v_mfma_f32_16x16x32_bf16 v[116:119], v[192:195], v[216:219], v[116:119]\n\t" \
  "v_add_u32 v254, 53248, v254\n\t" \
  "v_add_u32 v255, 53248, v255\n\t" \
  "v_mfma_f32_16x16x32_bf16 v[116:119], v[196:199], v[220:223], v[116:119]\n\t" \
  "v_mfma_f32_16x16x32_bf16 v[120:123], v[168:171], v[224:227], v[120:123]\n\t" \
  "ds_read_b128 v[208:211], v252 offset:8192\n\t" \
  "v_mfma_f32_16x16x32_bf16 v[120:123], v[172:175], v[228:231], v[120:123]\n\t" \
  "ds_read_b128 v[212:215], v253 offset:8192\n\t" \
  "ds_read_b128 v[168:171], v254 offset:0\n\t" \
  "ds_read_b128 v[172:175], v255 offset:0\n\t" \
  "v_mfma_f32_16x16x32_bf16 v[124:127], v[176:179], v[224:227], v[124:127]\n\t" \
  "v_mfma_f32_16x16x32_bf16 v[124:127], v[180:183], v[228:231], v[124:127]\n\t" \
  "ds_read_b128 v[176:179], v254 offset:2048\n\t" \
  "ds_read_b128 v[180:183], v255 offset:2048\n\t" \
  "v_mfma_f32_16x16x32_bf16 v[128:131], v[184:187], v[224:227], v[128:131]\n\t" \
  "v_mfma_f32_16x16x32_bf16 v[128:131], v[188:191], v[228:231], v[128:131]\n\t" \
  "ds_read_b128 v[184:187], v254 offset:4096\n\t" \
  "ds_read_b128 v[188:191], v255 offset:4096\n\t" \
  "v_mfma_f32_16x16x32_bf16 v[132:135], v[192:195], v[224:227], v[132:135]\n\t" \
  "v_mfma_f32_16x16x32_bf16 v[132:135], v[196:199], v[228:231], v[132:135]\n\t" \
  "ds_read_b128 v[192:195], v254 offset:6144\n\t" \
  "ds_read_b128 v[196:199], v255 offset:6144\n\t" \
  "s_waitcnt lgkmcnt(7)\n\t" \
  "v_mfma_f32_16x16x32_bf16 v[8:11], v[168:171], v[200:203], v[8:11]\n\t" \
  "ds_read_b128 v[216:219], v252 offset:2048\n\t" \
  "s_waitcnt lgkmcnt(7)\n\t" \
  "v_mfma_f32_16x16x32_bf16 v[8:11], v[172:175], v[204:207], v[8:11]\n\t" \
  "ds_read_b128 v[220:223], v253 offset:2048\n\t" \
  "v_lshlrev_b32 v5, 3, v4\n\t" \
  "v_min_u32 v5, 1152, v5\n\t" \
  "global_load_dword a241, v5, s[92:93]\n\t" \
  "s_waitcnt lgkmcnt(7)\n\t" \
  "v_mfma_f32_16x16x32_bf16 v[12:15], v[176:179], v[200:203], v[12:15]\n\t" \
  "s_waitcnt lgkmcnt(6)\n\t" \
  "v_mfma_f32_16x16x32_bf16 v[12:15], v[180:183], v[204:207], v[12:15]\n\t" \
  "global_load_dword a242, v5, s[94:95]\n\t" \
  "s_waitcnt lgkmcnt(5)\n\t" \
  "v_mfma_f32_16x16x32_bf16 v[16:19], v[184:187], v[200:203], v[16:19]\n\t" \
  "s_waitcnt lgkmcnt(4)\n\t" \
  "v_mfma_f32_16x16x32_bf16 v[16:19], v[188:191], v[204:207], v[16:19]\n\t" \
  "s_add_u32 s55, s72, 81920\n\t" \
  "buffer_load_dwordx4 a[56:59], v3, s[64:67], s55 offen offset:0 sc1\n\t" \
  "s_waitcnt lgkmcnt(3)\n\t" \
  "v_mfma_f32_16x16x32_bf16 v[20:23], v[192:195], v[200:203], v[20:23]\n\t" \
  "s_waitcnt lgkmcnt(2)\n\t" \
  "v_mfma_f32_16x16x32_bf16 v[20:23], v[196:199], v[204:207], v[20:23]\n\t" \
  "s_add_u32 s55, s72, 122880\n\t" \
  "buffer_load_dwordx4 a[60:63], v4, s[64:67], s55 offen offset:0 sc1\n\t" \
  "v_mfma_f32_16x16x32_bf16 v[24:27], v[168:171], v[208:211], v[24:27]\n\t" \
  "ds_read_b128 v[224:227], v252 offset:10240\n\t" \
  "v_mfma_f32_16x16x32_bf16 v[24:27], v[172:175], v[212:215], v[24:27]\n\t" \
  "ds_read_b128 v[228:231], v253 offset:10240\n\t" \
  "buffer_load_dwordx4 a[64:67], v4, s[64:67], s55 offen offset:1024 sc1\n\t" \
  "v_mfma_f32_16x16x32_bf16 v[28:31], v[176:179], v[208:211], v[28:31]\n\t" \
  "v_mfma_f32_16x16x32_bf16 v[28:31], v[180:183], v[212:215], v[28:31]\n\t" \
  "buffer_load_dwordx4 a[68:71], v4, s[64:67], s55 offen offset:2048 sc1\n\t" \
  "v_mfma_f32_16x16x32_bf16 v[32:35], v[184:187], v[208:211], v[32:35]\n\t" \
  "v_mfma_f32_16x16x32_bf16 v[32:35], v[188:191], v[212:215], v[32:35]\n\t" \
  "buffer_load_dwordx4 a[72:75], v4, s[64:67], s55 offen offset:3072 sc1\n\t" \
  "v_mfma_f32_16x16x32_bf16 v[36:39], v[192:195], v[208:211], v[36:39]\n\t" \
  "v_mfma_f32_16x16x32_bf16 v[36:39], v[196:199], v[212:215], v[36:39]\n\t" \
  "buffer_load_dwordx4 a[76:79], v3, s[64:67], s55 offen offset:0 sc1\n\t" \
  "s_waitcnt lgkmcnt(3)\n\t" \
  "v_mfma_f32_16x16x32_bf16 v[40:43], v[168:171], v[216:219], v[40:43]\n\t" \
  "ds_read_b128 v[200:203], v252 offset:4096\n\t" \
  "s_waitcnt lgkmcnt(3)\n\t" \
  "v_mfma_f32_16x16x32_bf16 v[40:43], v[172:175], v[220:223], v[40:43]\n\t" \
  "ds_read_b128 v[204:207], v253 offset:4096\n\t" \
  "s_add_u32 s55, s72, 163840\n\t" \
  "buffer_load_dwordx4 a[80:83], v4, s[64:67], s55 offen offset:0 sc1\n\t" \
  "v_mfma_f32_16x16x32_bf16 v[44:47], v[176:179], v[216:219], v[44:47]\n\t" \
  "v_mfma_f32_16x16x32_bf16 v[44:47], v[180:183], v[220:223], v[44:47]\n\t" \
  "buffer_load_dwordx4 a[84:87], v4, s[64:67], s55 offen offset:1024 sc1\n\t" \
  "v_mfma_f32_16x16x32_bf16 v[48:51], v[184:187], v[216:219], v[48:51]\n\t" \
  "v_mfma_f32_16x16x32_bf16 v[48:51], v[188:191], v[220:223], v[48:51]\n\t" \
  "buffer_load_dwordx4 a[88:91], v4, s[64:67], s55 offen offset:2048 sc1\n\t" \
  "v_mfma_f32_16x16x32_bf16 v[52:55], v[192:195], v[216:219], v[52:55]\n\t" \
  "v_mfma_f32_16x16x32_bf16 v[52:55], v[196:199], v[220:223], v[52:55]\n\t" \
  "buffer_load_dwordx4 a[92:95], v4, s[64:67], s55 offen offset:3072 sc1\n\t" \
  "s_waitcnt lgkmcnt(3)\n\t" \
  "v_mfma_f32_16x16x32_bf16 v[56:59], v[168:171], v[224:227], v[56:59]\n\t" \
  "ds_read_b128 v[208:211], v252 offset:12288\n\t" \
  "s_waitcnt lgkmcnt(3)\n\t" \
  "v_mfma_f32_16x16x32_bf16 v[56:59], v[172:175], v[228:231], v[56:59]\n\t" \
  "ds_read_b128 v[212:215], v253 offset:12288\n\t" \
  "buffer_load_dwordx4 a[96:99], v3, s[64:67], s55 offen offset:0 sc1\n\t" \
  "v_mfma_f32_16x16x32_bf16 v[60:63], v[176:179], v[224:227], v[60:63]\n\t" \
  "v_mfma_f32_16x16x32_bf16 v[60:63], v[180:183], v[228:231], v[60:63]\n\t" \
  "s_add_u32 s55, s72, 204800\n\t" \
  "buffer_load_dwordx4 a[100:103], v4, s[64:67], s55 offen offset:0 sc1\n\t" \
  "v_mfma_f32_16x16x32_bf16 v[64:67], v[184:187], v[224:227], v[64:67]\n\t" \
  "v_mfma_f32_16x16x32_bf16 v[64:67], v[188:191], v[228:231], v[64:67]\n\t" \
  "buffer_load_dwordx4 a[104:107], v4, s[64:67], s55 offen offset:1024 sc1\n\t" \
  "v_mfma_f32_16x16x32_bf16 v[68:71], v[192:195], v[224:227], v[68:71]\n\t" \
  "v_mfma_f32_16x16x32_bf16 v[68:71], v[196:199], v[228:231], v[68:71]\n\t" \
  "buffer_load_dwordx4 a[108:111], v4, s[64:67], s55 offen offset:2048 sc1\n\t" \
  "s_waitcnt lgkmcnt(3)\n\t" \
  "v_mfma_f32_16x16x32_bf16 v[72:75], v[168:171], v[200:203], v[72:75]\n\t" \
  "ds_read_b128 v[216:219], v252 offset:6144\n\t" \
  "s_waitcnt lgkmcnt(3)\n\t" \
  "v_mfma_f32_16x16x32_bf16 v[72:75], v[172:175], v[204:207], v[72:75]\n\t" \
  "ds_read_b128 v[220:223], v253 offset:6144\n\t" \
  "buffer_load_dwordx4 a[112:115], v4, s[64:67], s55 offen offset:3072 sc1\n\t" \
  "v_mfma_f32_16x16x32_bf16 v[76:79], v[176:179], v[200:203], v[76:79]\n\t" \
  "v_mfma_f32_16x16x32_bf16 v[76:79], v[180:183], v[204:207], v[76:79]\n\t" \
  "buffer_load_dwordx4 a[116:119], v3, s[64:67], s55 offen offset:0 sc1\n\t" \
  "v_mfma_f32_16x16x32_bf16 v[80:83], v[184:187], v[200:203], v[80:83]\n\t" \
  "v_mfma_f32_16x16x32_bf16 v[80:83], v[188:191], v[204:207], v[80:83]\n\t" \
  "s_add_u32 s55, s72, 245760\n\t" \
  "buffer_load_dwordx4 a[120:123], v4, s[64:67], s55 offen offset:0 sc1\n\t" \
  "v_mfma_f32_16x16x32_bf16 v[84:87], v[192:195], v[200:203], v[84:87]\n\t" \
  "v_mfma_f32_16x16x32_bf16 v[84:87], v[196:199], v[204:207], v[84:87]\n\t" \
  "buffer_load_dwordx4 a[124:127], v4, s[64:67], s55 offen offset:1024 sc1\n\t" \
  "s_waitcnt lgkmcnt(3)\n\t" \
  "v_mfma_f32_16x16x32_bf16 v[88:91], v[168:171], v[208:211], v[88:91]\n\t" \
  "ds_read_b128 v[224:227], v252 offset:14336\n\t" \
  "s_waitcnt lgkmcnt(3)\n\t" \
  "v_mfma_f32_16x16x32_bf16 v[88:91], v[172:175], v[212:215], v[88:91]\n\t" \
  "ds_read_b128 v[228:231], v253 offset:14336\n\t" \
  "buffer_load_dwordx4 a[128:131], v4, s[64:67], s55 offen offset:2048 sc1\n\t" \
  "v_mfma_f32_16x16x32_bf16 v[92:95], v[176:179], v[208:211], v[92:95]\n\t" \
  "v_mfma_f32_16x16x32_bf16 v[92:95], v[180:183], v[212:215], v[92:95]\n\t" \
  "buffer_load_dwordx4 a[132:135], v4, s[64:67], s55 offen offset:3072 sc1\n\t" \
  "v_mfma_f32_16x16x32_bf16 v[96:99], v[184:187], v[208:211], v[96:99]\n\t" \
  "v_mfma_f32_16x16x32_bf16 v[96:99], v[188:191], v[212:215], v[96:99]\n\t" \
  "buffer_load_dwordx4 a[136:139], v3, s[64:67], s55 offen offset:0 sc1\n\t" \
  "v_mfma_f32_16x16x32_bf16 v[100:103], v[192:195], v[208:211], v[100:103]\n\t" \
  "v_mfma_f32_16x16x32_bf16 v[100:103], v[196:199], v[212:215], v[100:103]\n\t" \
  "s_mov_b32 s55, s96\n\t" \
  "buffer_load_dwordx4 a[140:143], v4, s[64:67], s55 offen offset:0 sc1\n\t" \
  "s_waitcnt vmcnt(31) lgkmcnt(0)\n\t" \
  "s_barrier\n\t" \
  "v_add_u32 v252, 4294860800, v252\n\t" \
  "v_add_u32 v253, 4294860800, v253\n\t" \
  "v_mfma_f32_16x16x32_bf16 v[104:107], v[168:171], v[216:219], v[104:107]\n\t" \
  "ds_read_b128 v[200:203], v252 offset:0\n\t" \
  "v_mfma_f32_16x16x32_bf16 v[104:107], v[172:175], v[220:223], v[104:107]\n\t" \
  "ds_read_b128 v[204:207], v253 offset:0\n\t" \
  "buffer_load_dwordx4 a[144:147], v4, s[64:67], s55 offen offset:1024 sc1\n\t" \
  "v_mfma_f32_16x16x32_bf16 v[108:111], v[176:179], v[216:219], v[108:111]\n\t" \
  "v_mfma_f32_16x16x32_bf16 v[108:111], v[180:183], v[220:223], v[108:111]\n\t" \
  "buffer_load_dwordx4 a[148:151], v4, s[64:67], s55 offen offset:2048 sc1\n\t" \
  "v_mfma_f32_16x16x32_bf16 v[112:115], v[184:187], v[216:219], v[112:115]\n\t" \
  "v_mfma_f32_16x16x32_bf16 v[112:115], v[188:191], v[220:223], v[112:115]\n\t" \
  "buffer_load_dwordx4 a[152:155], v4, s[64:67], s55 offen offset:3072 sc1\n\t" \
  "v_mfma_f32_16x16x32_bf16 v[116:119], v[192:195], v[216:219], v[116:119]\n\t" \
  "v_add_u32 v254, 4294860800, v254\n\t" \
  "v_add_u32 v255, 4294860800, v255\n\t" \
  "v_mfma_f32_16x16x32_bf16 v[116:119], v[196:199], v[220:223], v[116:119]\n\t" \
  "buffer_load_dwordx4 a[156:159], v3, s[64:67], s55 offen offset:0 sc1\n\t" \
  "v_mfma_f32_16x16x32_bf16 v[120:123], v[168:171], v[224:227], v[120:123]\n\t" \
  "ds_read_b128 v[208:211], v252 offset:8192\n\t" \
  "v_mfma_f32_16x16x32_bf16 v[120:123], v[172:175], v[228:231], v[120:123]\n\t" \
  "ds_read_b128 v[212:215], v253 offset:8192\n\t" \
  "ds_read_b128 v[168:171], v254 offset:0\n\t" \
  "ds_read_b128 v[172:175], v255 offset:0\n\t" \
  "v_mfma_f32_16x16x32_bf16 v[124:127], v[176:179], v[224:227], v[124:127]\n\t" \
  "v_mfma_f32_16x16x32_bf16 v[124:127], v[180:183], v[228:231], v[124:127]\n\t" \
  "ds_read_b128 v[176:179], v254 offset:2048\n\t" \
  "ds_read_b128 v[180:183], v255 offset:2048\n\t" \
  "v_mfma_f32_16x16x32_bf16 v[128:131], v[184:187], v[224:227], v[128:131]\n\t" \
  "v_mfma_f32_16x16x32_bf16 v[128:131], v[188:191], v[228:231], v[128:131]\n\t" \
  "ds_read_b128 v[184:187], v254 offset:4096\n\t" \
  "ds_read_b128 v[188:191], v255 offset:4096\n\t" \
  "v_mfma_f32_16x16x32_bf16 v[132:135], v[192:195], v[224:227], v[132:135]\n\t" \
  "v_mfma_f32_16x16x32_bf16 v[132:135], v[196:199], v[228:231], v[132:135]\n\t" \
  "ds_read_b128 v[192:195], v254 offset:6144\n\t" \
  "ds_read_b128 v[196:199], v255 offset:6144\n\t" \
  "s_waitcnt lgkmcnt(7)\n\t" \
  "v_mfma_f32_16x16x32_bf16 v[8:11], v[168:171], v[200:203], v[8:11]\n\t" \
  "ds_read_b128 v[216:219], v252 offset:2048\n\t" \
  "s_add_u32 m0, s91, 0\n\t" \
  "s_add_u32 s55, s72, 4294803456\n\t" \
  "s_waitcnt lgkmcnt(7)\n\t" \
  "v_mfma_f32_16x16x32_bf16 v[8:11], v[172:175], v[204:207], v[8:11]\n\t" \
  "ds_read_b128 v[220:223], v253 offset:2048\n\t" \
  "buffer_load_dwordx4 v4, s[64:67], s55 offen offset:0 sc1 lds\n\t" \
  "s_waitcnt lgkmcnt(7)\n\t" \
  "v_mfma_f32_16x16x32_bf16 v[12:15], v[176:179], v[200:203], v[12:15]\n\t" \
  "s_waitcnt lgkmcnt(6)\n\t" \
  "v_mfma_f32_16x16x32_bf16 v[12:15], v[180:183], v[204:207], v[12:15]\n\t" \
  "buffer_load_dwordx4 v4, s[64:67], s55 offen offset:1024 sc1 lds\n\t" \
  "s_waitcnt lgkmcnt(5)\n\t" \
  "v_mfma_f32_16x16x32_bf16 v[16:19], v[184:187], v[200:203], v[16:19]\n\t" \
  "s_waitcnt lgkmcnt(4)\n\t" \
  "v_mfma_f32_16x16x32_bf16 v[16:19], v[188:191], v[204:207], v[16:19]\n\t" \
  "buffer_load_dwordx4 v4, s[64:67], s55 offen offset:2048 sc1 lds\n\t" \
  "s_waitcnt lgkmcnt(3)\n\t" \
  "v_mfma_f32_16x16x32_bf16 v[20:23], v[192:195], v[200:203], v[20:23]\n\t" \
  "s_waitcnt lgkmcnt(2)\n\t" \
  "v_mfma_f32_16x16x32_bf16 v[20:23], v[196:199], v[204:207], v[20:23]\n\t" \
  "buffer_load_dwordx4 v4, s[64:67], s55 offen offset:3072 sc1 lds\n\t" \
  "v_mfma_f32_16x16x32_bf16 v[24:27], v[168:171], v[208:211], v[24:27]\n\t" \
  "ds_read_b128 v[224:227], v252 offset:10240\n\t" \
  "s_add_u32 m0, s91, 4096\n\t" \
  "s_add_u32 s55, s72, 4294807552\n\t" \
  "v_mfma_f32_16x16x32_bf16 v[24:27], v[172:175], v[212:215], v[24:27]\n\t" \
  "ds_read_b128 v[228:231], v253 offset:10240\n\t" \
  "buffer_load_dwordx4 v4, s[64:67], s55 offen offset:0 sc1 lds\n\t" \
  "v_mfma_f32_16x16x32_bf16 v[28:31], v[176:179], v[208:211], v[28:31]\n\t" \
  "s_add_u32 m0, s91, 5120\n\t" \
  "s_add_u32 s55, s72, 4294844416\n\t" \
  "v_readlane_b32 s76, v2, 1\n\t" \
  "v_mfma_f32_16x16x32_bf16 v[28:31], v[180:183], v[212:215], v[28:31]\n\t" \
  "buffer_load_dwordx4 v4, s[64:67], s55 offen offset:0 sc1 lds\n\t" \
  "v_readlane_b32 s77, v2, 29\n\t" \
  "v_mfma_f32_16x16x32_bf16 v[32:35], v[184:187], v[208:211], v[32:35]\n\t" \
  "v_readlane_b32 s78, v2, 0\n\t" \
  "v_mfma_f32_16x16x32_bf16 v[32:35], v[188:191], v[212:215], v[32:35]\n\t" \
  "buffer_load_dwordx4 v4, s[64:67], s55 offen offset:1024 sc1 lds\n\t" \
  "v_readlane_b32 s79, v2, 25\n\t" \
  "v_mfma_f32_16x16x32_bf16 v[36:39], v[192:195], v[208:211], v[36:39]\n\t" \
  "v_mfma_f32_16x16x32_bf16 v[36:39], v[196:199], v[212:215], v[36:39]\n\t" \
  "buffer_load_dwordx4 v4, s[64:67], s55 offen offset:2048 sc1 lds\n\t" \
  "s_waitcnt lgkmcnt(3)\n\t" \
  "v_mfma_f32_16x16x32_bf16 v[40:43], v[168:171], v[216:219], v[40:43]\n\t" \
  "ds_read_b128 v[200:203], v252 offset:4096\n\t" \
  "s_waitcnt lgkmcnt(3)\n\t" \
  "v_mfma_f32_16x16x32_bf16 v[40:43], v[172:175], v[220:223], v[40:43]\n\t" \
  "ds_read_b128 v[204:207], v253 offset:4096\n\t" \
  "buffer_load_dwordx4 v4, s[64:67], s55 offen offset:3072 sc1 lds\n\t" \
  "v_mfma_f32_16x16x32_bf16 v[44:47], v[176:179], v[216:219], v[44:47]\n\t" \
  "s_add_u32 m0, s91, 9216\n\t" \
  "s_add_u32 s55, s72, 4294848512\n\t" \
  "v_readlane_b32 s80, v2, 26\n\t" \
  "v_mfma_f32_16x16x32_bf16 v[44:47], v[180:183], v[220:223], v[44:47]\n\t" \
  "buffer_load_dwordx4 v4, s[64:67], s55 offen offset:0 sc1 lds\n\t" \
  "v_readlane_b32 s81, v2, 27\n\t" \
  "v_mfma_f32_16x16x32_bf16 v[48:51], v[184:187], v[216:219], v[48:51]\n\t" \
  "s_add_u32 m0, s91, 10240\n\t" \
  "s_add_u32 s55, s72, 4294885376\n\t" \
  "v_readlane_b32 s82, v2, 28\n\t" \
  "v_mfma_f32_16x16x32_bf16 v[48:51], v[188:191], v[220:223], v[48:51]\n\t" \
  "buffer_load_dwordx4 v4, s[64:67], s55 offen offset:0 sc1 lds\n\t" \
  "v_readlane_b32 s83, v2, 23\n\t" \
  "v_mfma_f32_16x16x32_bf16 v[52:55], v[192:195], v[216:219], v[52:55]\n\t" \
  "v_mfma_f32_16x16x32_bf16 v[52:55], v[196:199], v[220:223], v[52:55]\n\t" \
  "buffer_load_dwordx4 v4, s[64:67], s55 offen offset:1024 sc1 lds\n\t" \
  "s_waitcnt lgkmcnt(3)\n\t" \
  "v_mfma_f32_16x16x32_bf16 v[56:59], v[168:171], v[224:227], v[56:59]\n\t" \
  "ds_read_b128 v[208:211], v252 offset:12288\n\t" \
  "s_waitcnt lgkmcnt(3)\n\t" \
  "v_mfma_f32_16x16x32_bf16 v[56:59], v[172:175], v[228:231], v[56:59]\n\t" \
  "ds_read_b128 v[212:215], v253 offset:12288\n\t" \
  "buffer_load_dwordx4 v4, s[64:67], s55 offen offset:2048 sc1 lds\n\t" \
  "v_mfma_f32_16x16x32_bf16 v[60:63], v[176:179], v[224:227], v[60:63]\n\t" \
  "v_mfma_f32_16x16x32_bf16 v[60:63], v[180:183], v[228:231], v[60:63]\n\t" \
  "buffer_load_dwordx4 v4, s[64:67], s55 offen offset:3072 sc1 lds\n\t" \
  "v_mfma_f32_16x16x32_bf16 v[64:67], v[184:187], v[224:227], v[64:67]\n\t" \
  "s_add_u32 m0, s91, 14336\n\t" \
  "s_add_u32 s55, s72, 4294889472\n\t" \
  "v_mfma_f32_16x16x32_bf16 v[64:67], v[188:191], v[228:231], v[64:67]\n\t" \
  "buffer_load_dwordx4 v4, s[64:67], s55 offen offset:0 sc1 lds\n\t" \
  "v_mfma_f32_16x16x32_bf16 v[68:71], v[192:195], v[224:227], v[68:71]\n\t" \
  "s_add_u32 m0, s91, 15360\n\t" \
  "s_add_u32 s55, s72, 4294926336\n\t" \
  "v_mfma_f32_16x16x32_bf16 v[68:71], v[196:199], v[228:231], v[68:71]\n\t" \
  "buffer_load_dwordx4 v4, s[64:67], s55 offen offset:0 sc1 lds\n\t" \
  "s_waitcnt lgkmcnt(3)\n\t" \
  "v_mfma_f32_16x16x32_bf16 v[72:75], v[168:171], v[200:203], v[72:75]\n\t" \
  "ds_read_b128 v[216:219], v252 offset:6144\n\t" \
  "s_waitcnt lgkmcnt(3)\n\t" \
  "v_mfma_f32_16x16x32_bf16 v[72:75], v[172:175], v[204:207], v[72:75]\n\t" \
  "ds_read_b128 v[220:223], v253 offset:6144\n\t" \
  "buffer_load_dwordx4 v4, s[64:67], s55 offen offset:1024 sc1 lds\n\t" \
  "v_mfma_f32_16x16x32_bf16 v[76:79], v[176:179], v[200:203], v[76:79]\n\t" \
  "v_mfma_f32_16x16x32_bf16 v[76:79], v[180:183], v[204:207], v[76:79]\n\t" \
  "buffer_load_dwordx4 v4, s[64:67], s55 offen offset:2048 sc1 lds\n\t" \
  "v_mfma_f32_16x16x32_bf16 v[80:83], v[184:187], v[200:203], v[80:83]\n\t" \
  "v_mfma_f32_16x16x32_bf16 v[80:83], v[188:191], v[204:207], v[80:83]\n\t" \
  "buffer_load_dwordx4 v4, s[64:67], s55 offen offset:3072 sc1 lds\n\t" \
  "v_mfma_f32_16x16x32_bf16 v[84:87], v[192:195], v[200:203], v[84:87]\n\t" \
  "s_add_u32 m0, s91, 19456\n\t" \
  "s_add_u32 s55, s72, 4294930432\n\t" \
  "v_mfma_f32_16x16x32_bf16 v[84:87], v[196:199], v[204:207], v[84:87]\n\t" \
  "buffer_load_dwordx4 v4, s[64:67], s55 offen offset:0 sc1 lds\n\t" \
  "s_waitcnt lgkmcnt(3)\n\t" \
  "v_mfma_f32_16x16x32_bf16 v[88:91], v[168:171], v[208:211], v[88:91]\n\t" \
  "ds_read_b128 v[224:227], v252 offset:14336\n\t" \
  "s_waitcnt lgkmcnt(3)\n\t" \
  "v_mfma_f32_16x16x32_bf16 v[88:91], v[172:175], v[212:215], v[88:91]\n\t" \
  "ds_read_b128 v[228:231], v253 offset:14336\n\t" \
  "v_lshrrev_b32 v242, 8, v4\n\t" \
  "v_lshlrev_b32 v242, 3, v242\n\t" \
  "v_and_b32 v241, 0xf0, v4\n\t" \
  "v_mul_u32_u24 v241, 80, v241\n\t" \
  "v_add_u32 v241, v241, v242\n\t" \
  "global_load_dwordx2 a[244:245], v241, s[86:87] offset:0\n\t" \
  "v_mfma_f32_16x16x32_bf16 v[92:95], v[176:179], v[208:211], v[92:95]\n\t" \
  "v_mfma_f32_16x16x32_bf16 v[92:95], v[180:183], v[212:215], v[92:95]\n\t" \
  "global_load_dwordx2 a[246:247], v241, s[86:87] offset:32\n\t" \
  "v_mfma_f32_16x16x32_bf16 v[96:99], v[184:187], v[208:211], v[96:99]\n\t" \
  "v_mfma_f32_16x16x32_bf16 v[96:99], v[188:191], v[212:215], v[96:99]\n\t" \
  "global_load_dwordx2 a[248:249], v241, s[86:87] offset:64\n\t" \
  "v_mfma_f32_16x16x32_bf16 v[100:103], v[192:195], v[208:211], v[100:103]\n\t" \
  "v_mfma_f32_16x16x32_bf16 v[100:103], v[196:199], v[212:215], v[100:103]\n\t" \
  "global_load_dwordx2 a[250:251], v241, s[86:87] offset:96\n\t" \
  "s_waitcnt lgkmcnt(3)\n\t" \
  "v_mfma_f32_16x16x32_bf16 v[104:107], v[168:171], v[216:219], v[104:107]\n\t" \
  "s_waitcnt lgkmcnt(2)\n\t" \
  "v_mfma_f32_16x16x32_bf16 v[104:107], v[172:175], v[220:223], v[104:107]\n\t" \
  "global_load_dwordx2 a[252:253], v241, s[86:87] offset:128\n\t" \
  "v_mfma_f32_16x16x32_bf16 v[108:111], v[176:179], v[216:219], v[108:111]\n\t" \
  "v_mfma_f32_16x16x32_bf16 v[108:111], v[180:183], v[220:223], v[108:111]\n\t" \
  "v_mfma_f32_16x16x32_bf16 v[112:115], v[184:187], v[216:219], v[112:115]\n\t" \
  "v_mfma_f32_16x16x32_bf16 v[112:115], v[188:191], v[220:223], v[112:115]\n\t" \
  "v_mfma_f32_16x16x32_bf16 v[116:119], v[192:195], v[216:219], v[116:119]\n\t" \
  "v_mfma_f32_16x16x32_bf16 v[116:119], v[196:199], v[220:223], v[116:119]\n\t" \
  "s_waitcnt lgkmcnt(1)\n\t" \
  "v_mfma_f32_16x16x32_bf16 v[120:123], v[168:171], v[224:227], v[120:123]\n\t" \
  "s_waitcnt lgkmcnt(0)\n\t" \
  "v_mfma_f32_16x16x32_bf16 v[120:123], v[172:175], v[228:231], v[120:123]\n\t" \
  "v_mfma_f32_16x16x32_bf16 v[124:127], v[176:179], v[224:227], v[124:127]\n\t" \
  "v_mfma_f32_16x16x32_bf16 v[124:127], v[180:183], v[228:231], v[124:127]\n\t" \
  "v_mfma_f32_16x16x32_bf16 v[128:131], v[184:187], v[224:227], v[128:131]\n\t" \
  "v_mfma_f32_16x16x32_bf16 v[128:131], v[188:191], v[228:231], v[128:131]\n\t" \
  "v_mfma_f32_16x16x32_bf16 v[132:135], v[192:195], v[224:227], v[132:135]\n\t" \
  "v_mfma_f32_16x16x32_bf16 v[132:135], v[196:199], v[228:231], v[132:135]\n\t" \
  "s_waitcnt lgkmcnt(0)\n\t" \
  "s_barrier\n\t" \
  "s_nop 7\n\t" \
  "s_mov_b64 s[44:45], s[40:41]\n\t" \
  "s_mov_b32 s46, 16531712\n\t" \
  "s_mov_b32 s47, 0x20000\n\t" \
  "v_add_u32 v5, 4096, v4\n\t" \
  "s_mov_b32 s63, 0\n\t" \
  "v_lshrrev_b32 v214, 8, v4\n\t" \
  "v_and_b32 v215, 0xf0, v4\n\t" \
  "v_lshlrev_b32 v212, 9, v214\n\t" \
  "v_lshl_add_u32 v212, v215, 1, v212\n\t" \
  "v_add_u32 v212, s76, v212\n\t" \
  "v_lshrrev_b32 v213, 1, v4\n\t" \
  "v_add_u32 v213, s77, v213\n\t" \
  "v_pk_fma_f32 v[196:197], v[140:141], v[24:25], v[10:11] op_sel:[1,0,0] neg_lo:[1,0,0] neg_hi:[1,0,0]\n\t" \
  "v_pk_fma_f32 v[204:205], v[140:141], v[8:9], v[26:27] op_sel:[1,0,0]\n\t" \
  "v_pk_fma_f32 v[198:199], v[142:143], v[56:57], v[42:43] op_sel:[1,0,0] neg_lo:[1,0,0] neg_hi:[1,0,0]\n\t" \
  "v_pk_fma_f32 v[206:207], v[142:143], v[40:41], v[58:59] op_sel:[1,0,0]\n\t" \
  "v_pk_fma_f32 v[200:201], v[144:145], v[88:89], v[74:75] op_sel:[1,0,0] neg_lo:[1,0,0] neg_hi:[1,0,0]\n\t" \
  "v_pk_fma_f32 v[208:209], v[144:145], v[72:73], v[90:91] op_sel:[1,0,0]\n\t" \
  "v_pk_fma_f32 v[202:203], v[146:147], v[120:121], v[106:107] op_sel:[1,0,0] neg_lo:[1,0,0] neg_hi:[1,0,0]\n\t" \
  "v_pk_fma_f32 v[210:211], v[146:147], v[104:105], v[122:123] op_sel:[1,0,0]\n\t" \
  "s_add_u32 s61, s73, 0\n\t" \
  "buffer_load_dwordx4 a[160:163], v4, s[44:47], s61 offen offset:0 sc1\n\t" \
  "v_pk_fma_f32 v[180:181], v[140:141], v[8:9], v[196:197] op_sel_hi:[0,1,1]\n\t" \
  "v_pk_fma_f32 v[188:189], v[140:141], v[24:25], v[204:205] op_sel_hi:[0,1,1]\n\t" \
  "v_pk_fma_f32 v[182:183], v[142:143], v[40:41], v[198:199] op_sel_hi:[0,1,1]\n\t" \
  "v_pk_fma_f32 v[190:191], v[142:143], v[56:57], v[206:207] op_sel_hi:[0,1,1]\n\t" \
  "v_pk_fma_f32 v[184:185], v[144:145], v[72:73], v[200:201] op_sel_hi:[0,1,1]\n\t" \
  "v_pk_fma_f32 v[192:193], v[144:145], v[88:89], v[208:209] op_sel_hi:[0,1,1]\n\t" \
  "v_pk_fma_f32 v[186:187], v[146:147], v[104:105], v[202:203] op_sel_hi:[0,1,1]\n\t" \
  "v_pk_fma_f32 v[194:195], v[146:147], v[120:121], v[210:211] op_sel_hi:[0,1,1]\n\t" \
  "v_pk_fma_f32 v[196:197], v[140:141], v[188:189], v[12:13] op_sel:[1,0,0] neg_lo:[1,0,0] neg_hi:[1,0,0]\n\t" \
  "v_pk_fma_f32 v[204:205], v[140:141], v[180:181], v[28:29] op_sel:[1,0,0]\n\t" \
  "v_pk_fma_f32 v[198:199], v[142:143], v[190:191], v[44:45] op_sel:[1,0,0] neg_lo:[1,0,0] neg_hi:[1,0,0]\n\t" \
  "v_pk_fma_f32 v[206:207], v[142:143], v[182:183], v[60:61] op_sel:[1,0,0]\n\t" \
  "v_pk_fma_f32 v[200:201], v[144:145], v[192:193], v[76:77] op_sel:[1,0,0] neg_lo:[1,0,0] neg_hi:[1,0,0]\n\t" \
  "v_pk_fma_f32 v[208:209], v[144:145], v[184:185], v[92:93] op_sel:[1,0,0]\n\t" \
  "v_pk_fma_f32 v[202:203], v[146:147], v[194:195], v[108:109] op_sel:[1,0,0] neg_lo:[1,0,0] neg_hi:[1,0,0]\n\t" \
  "v_pk_fma_f32 v[210:211], v[146:147], v[186:187], v[124:125] op_sel:[1,0,0]\n\t" \
  "buffer_load_dwordx4 a[164:167], v4, s[44:47], s61 offen offset:1024 sc1\n\t" \
  "v_pk_fma_f32 v[180:181], v[140:141], v[180:181], v[196:197] op_sel_hi:[0,1,1]\n\t" \
  "v_pk_fma_f32 v[188:189], v[140:141], v[188:189], v[204:205] op_sel_hi:[0,1,1]\n\t" \
  "v_pk_fma_f32 v[182:183], v[142:143], v[182:183], v[198:199] op_sel_hi:[0,1,1]\n\t" \
  "v_pk_fma_f32 v[190:191], v[142:143], v[190:191], v[206:207] op_sel_hi:[0,1,1]\n\t" \
  "v_pk_fma_f32 v[184:185], v[144:145], v[184:185], v[200:201] op_sel_hi:[0,1,1]\n\t" \
  "v_pk_fma_f32 v[192:193], v[144:145], v[192:193], v[208:209] op_sel_hi:[0,1,1]\n\t" \
  "v_pk_fma_f32 v[186:187], v[146:147], v[186:187], v[202:203] op_sel_hi:[0,1,1]\n\t" \
  "v_pk_fma_f32 v[194:195], v[146:147], v[194:195], v[210:211] op_sel_hi:[0,1,1]\n\t" \
  "v_pk_fma_f32 v[196:197], v[140:141], v[188:189], v[14:15] op_sel:[1,0,0] neg_lo:[1,0,0] neg_hi:[1,0,0]\n\t" \
  "v_pk_fma_f32 v[204:205], v[140:141], v[180:181], v[30:31] op_sel:[1,0,0]\n\t" \
  "v_pk_fma_f32 v[198:199], v[142:143], v[190:191], v[46:47] op_sel:[1,0,0] neg_lo:[1,0,0] neg_hi:[1,0,0]\n\t" \
  "v_pk_fma_f32 v[206:207], v[142:143], v[182:183], v[62:63] op_sel:[1,0,0]\n\t" \
  "v_pk_fma_f32 v[200:201], v[144:145], v[192:193], v[78:79] op_sel:[1,0,0] neg_lo:[1,0,0] neg_hi:[1,0,0]\n\t" \
  "v_pk_fma_f32 v[208:209], v[144:145], v[184:185], v[94:95] op_sel:[1,0,0]\n\t" \
  "v_pk_fma_f32 v[202:203], v[146:147], v[194:195], v[110:111] op_sel:[1,0,0] neg_lo:[1,0,0] neg_hi:[1,0,0]\n\t" \
  "v_pk_fma_f32 v[210:211], v[146:147], v[186:187], v[126:127] op_sel:[1,0,0]\n\t" \
  "buffer_load_dwordx4 a[168:171], v4, s[44:47], s61 offen offset:2048 sc1\n\t" \
  "v_pk_fma_f32 v[180:181], v[140:141], v[180:181], v[196:197] op_sel_hi:[0,1,1]\n\t" \
  "v_pk_fma_f32 v[188:189], v[140:141], v[188:189], v[204:205] op_sel_hi:[0,1,1]\n\t" \
  "v_pk_fma_f32 v[182:183], v[142:143], v[182:183], v[198:199] op_sel_hi:[0,1,1]\n\t" \
  "v_pk_fma_f32 v[190:191], v[142:143], v[190:191], v[206:207] op_sel_hi:[0,1,1]\n\t" \
  "v_pk_fma_f32 v[184:185], v[144:145], v[184:185], v[200:201] op_sel_hi:[0,1,1]\n\t" \
  "v_pk_fma_f32 v[192:193], v[144:145], v[192:193], v[208:209] op_sel_hi:[0,1,1]\n\t" \
  "v_pk_fma_f32 v[186:187], v[146:147], v[186:187], v[202:203] op_sel_hi:[0,1,1]\n\t" \
  "v_pk_fma_f32 v[194:195], v[146:147], v[194:195], v[210:211] op_sel_hi:[0,1,1]\n\t" \
  "v_pk_fma_f32 v[196:197], v[140:141], v[188:189], v[16:17] op_sel:[1,0,0] neg_lo:[1,0,0] neg_hi:[1,0,0]\n\t" \
  "v_pk_fma_f32 v[204:205], v[140:141], v[180:181], v[32:33] op_sel:[1,0,0]\n\t" \
  "v_pk_fma_f32 v[198:199], v[142:143], v[190:191], v[48:49] op_sel:[1,0,0] neg_lo:[1,0,0] neg_hi:[1,0,0]\n\t" \
  "v_pk_fma_f32 v[206:207], v[142:143], v[182:183], v[64:65] op_sel:[1,0,0]\n\t" \
  "v_pk_fma_f32 v[200:201], v[144:145], v[192:193], v[80:81] op_sel:[1,0,0] neg_lo:[1,0,0] neg_hi:[1,0,0]\n\t" \
  "v_pk_fma_f32 v[208:209], v[144:145], v[184:185], v[96:97] op_sel:[1,0,0]\n\t" \
  "v_pk_fma_f32 v[202:203], v[146:147], v[194:195], v[112:113] op_sel:[1,0,0] neg_lo:[1,0,0] neg_hi:[1,0,0]\n\t" \
  "v_pk_fma_f32 v[210:211], v[146:147], v[186:187], v[128:129] op_sel:[1,0,0]\n\t" \
  "buffer_load_dwordx4 a[172:175], v4, s[44:47], s61 offen offset:3072 sc1\n\t" \
  "v_pk_fma_f32 v[180:181], v[140:141], v[180:181], v[196:197] op_sel_hi:[0,1,1]\n\t" \
  "v_pk_fma_f32 v[188:189], v[140:141], v[188:189], v[204:205] op_sel_hi:[0,1,1]\n\t" \
  "v_pk_fma_f32 v[182:183], v[142:143], v[182:183], v[198:199] op_sel_hi:[0,1,1]\n\t" \
  "v_pk_fma_f32 v[190:191], v[142:143], v[190:191], v[206:207] op_sel_hi:[0,1,1]\n\t" \
  "v_pk_fma_f32 v[184:185], v[144:145], v[184:185], v[200:201] op_sel_hi:[0,1,1]\n\t" \
  "v_pk_fma_f32 v[192:193], v[144:145], v[192:193], v[208:209] op_sel_hi:[0,1,1]\n\t" \
  "v_pk_fma_f32 v[186:187], v[146:147], v[186:187], v[202:203] op_sel_hi:[0,1,1]\n\t" \
  "v_pk_fma_f32 v[194:195], v[146:147], v[194:195], v[210:211] op_sel_hi:[0,1,1]\n\t" \
  "v_pk_fma_f32 v[196:197], v[140:141], v[188:189], v[18:19] op_sel:[1,0,0] neg_lo:[1,0,0] neg_hi:[1,0,0]\n\t" \
  "v_pk_fma_f32 v[204:205], v[140:141], v[180:181], v[34:35] op_sel:[1,0,0]\n\t" \
  "v_pk_fma_f32 v[198:199], v[142:143], v[190:191], v[50:51] op_sel:[1,0,0] neg_lo:[1,0,0] neg_hi:[1,0,0]\n\t" \
  "v_pk_fma_f32 v[206:207], v[142:143], v[182:183], v[66:67] op_sel:[1,0,0]\n\t" \
  "v_pk_fma_f32 v[200:201], v[144:145], v[192:193], v[82:83] op_sel:[1,0,0] neg_lo:[1,0,0] neg_hi:[1,0,0]\n\t" \
  "v_pk_fma_f32 v[208:209], v[144:145], v[184:185], v[98:99] op_sel:[1,0,0]\n\t" \
  "v_pk_fma_f32 v[202:203], v[146:147], v[194:195], v[114:115] op_sel:[1,0,0] neg_lo:[1,0,0] neg_hi:[1,0,0]\n\t" \
  "v_pk_fma_f32 v[210:211], v[146:147], v[186:187], v[130:131] op_sel:[1,0,0]\n\t" \
  "buffer_load_dwordx4 a[176:179], v5, s[44:47], s61 offen offset:0 sc1\n\t" \
  "v_pk_fma_f32 v[180:181], v[140:141], v[180:181], v[196:197] op_sel_hi:[0,1,1]\n\t" \
  "v_pk_fma_f32 v[188:189], v[140:141], v[188:189], v[204:205] op_sel_hi:[0,1,1]\n\t" \
  "v_pk_fma_f32 v[182:183], v[142:143], v[182:183], v[198:199] op_sel_hi:[0,1,1]\n\t" \
  "v_pk_fma_f32 v[190:191], v[142:143], v[190:191], v[206:207] op_sel_hi:[0,1,1]\n\t" \
  "v_pk_fma_f32 v[184:185], v[144:145], v[184:185], v[200:201] op_sel_hi:[0,1,1]\n\t" \
  "v_pk_fma_f32 v[192:193], v[144:145], v[192:193], v[208:209] op_sel_hi:[0,1,1]\n\t" \
  "v_pk_fma_f32 v[186:187], v[146:147], v[186:187], v[202:203] op_sel_hi:[0,1,1]\n\t" \
  "v_pk_fma_f32 v[194:195], v[146:147], v[194:195], v[210:211] op_sel_hi:[0,1,1]\n\t" \
  "v_pk_fma_f32 v[196:197], v[140:141], v[188:189], v[20:21] op_sel:[1,0,0] neg_lo:[1,0,0] neg_hi:[1,0,0]\n\t" \
  "v_pk_fma_f32 v[204:205], v[140:141], v[180:181], v[36:37] op_sel:[1,0,0]\n\t" \
  "v_pk_fma_f32 v[198:199], v[142:143], v[190:191], v[52:53] op_sel:[1,0,0] neg_lo:[1,0,0] neg_hi:[1,0,0]\n\t" \
  "v_pk_fma_f32 v[206:207], v[142:143], v[182:183], v[68:69] op_sel:[1,0,0]\n\t" \
  "v_pk_fma_f32 v[200:201], v[144:145], v[192:193], v[84:85] op_sel:[1,0,0] neg_lo:[1,0,0] neg_hi:[1,0,0]\n\t" \
  "v_pk_fma_f32 v[208:209], v[144:145], v[184:185], v[100:101] op_sel:[1,0,0]\n\t" \
  "v_pk_fma_f32 v[202:203], v[146:147], v[194:195], v[116:117] op_sel:[1,0,0] neg_lo:[1,0,0] neg_hi:[1,0,0]\n\t" \
  "v_pk_fma_f32 v[210:211], v[146:147], v[186:187], v[132:133] op_sel:[1,0,0]\n\t" \
  "s_add_u32 s61, s73, 40960\n\t" \
  "buffer_load_dwordx4 a[180:183], v4, s[44:47], s61 offen offset:0 sc1\n\t" \
  "v_pk_fma_f32 v[180:181], v[140:141], v[180:181], v[196:197] op_sel_hi:[0,1,1]\n\t" \
  "v_pk_fma_f32 v[188:189], v[140:141], v[188:189], v[204:205] op_sel_hi:[0,1,1]\n\t" \
  "v_pk_fma_f32 v[182:183], v[142:143], v[182:183], v[198:199] op_sel_hi:[0,1,1]\n\t" \
  "v_pk_fma_f32 v[190:191], v[142:143], v[190:191], v[206:207] op_sel_hi:[0,1,1]\n\t" \
  "v_pk_fma_f32 v[184:185], v[144:145], v[184:185], v[200:201] op_sel_hi:[0,1,1]\n\t" \
  "v_pk_fma_f32 v[192:193], v[144:145], v[192:193], v[208:209] op_sel_hi:[0,1,1]\n\t" \
  "v_pk_fma_f32 v[186:187], v[146:147], v[186:187], v[202:203] op_sel_hi:[0,1,1]\n\t" \
  "v_pk_fma_f32 v[194:195], v[146:147], v[194:195], v[210:211] op_sel_hi:[0,1,1]\n\t" \
  "v_pk_fma_f32 v[196:197], v[140:141], v[188:189], v[22:23] op_sel:[1,0,0] neg_lo:[1,0,0] neg_hi:[1,0,0]\n\t" \
  "v_pk_fma_f32 v[204:205], v[140:141], v[180:181], v[38:39] op_sel:[1,0,0]\n\t" \
  "v_pk_fma_f32 v[198:199], v[142:143], v[190:191], v[54:55] op_sel:[1,0,0] neg_lo:[1,0,0] neg_hi:[1,0,0]\n\t" \
  "v_pk_fma_f32 v[206:207], v[142:143], v[182:183], v[70:71] op_sel:[1,0,0]\n\t" \
  "v_pk_fma_f32 v[200:201], v[144:145], v[192:193], v[86:87] op_sel:[1,0,0] neg_lo:[1,0,0] neg_hi:[1,0,0]\n\t" \
  "v_pk_fma_f32 v[208:209], v[144:145], v[184:185], v[102:103] op_sel:[1,0,0]\n\t" \
  "v_pk_fma_f32 v[202:203], v[146:147], v[194:195], v[118:119] op_sel:[1,0,0] neg_lo:[1,0,0] neg_hi:[1,0,0]\n\t" \
  "v_pk_fma_f32 v[210:211], v[146:147], v[186:187], v[134:135] op_sel:[1,0,0]\n\t" \
  "buffer_load_dwordx4 a[184:187], v4, s[44:47], s61 offen offset:1024 sc1\n\t" \
  "v_pk_fma_f32 v[180:181], v[140:141], v[180:181], v[196:197] op_sel_hi:[0,1,1]\n\t" \
  "v_pk_fma_f32 v[188:189], v[140:141], v[188:189], v[204:205] op_sel_hi:[0,1,1]\n\t" \
  "v_pk_fma_f32 v[182:183], v[142:143], v[182:183], v[198:199] op_sel_hi:[0,1,1]\n\t" \
  "v_pk_fma_f32 v[190:191], v[142:143], v[190:191], v[206:207] op_sel_hi:[0,1,1]\n\t" \
  "v_pk_fma_f32 v[184:185], v[144:145], v[184:185], v[200:201] op_sel_hi:[0,1,1]\n\t" \
  "v_pk_fma_f32 v[192:193], v[144:145], v[192:193], v[208:209] op_sel_hi:[0,1,1]\n\t" \
  "v_pk_fma_f32 v[186:187], v[146:147], v[186:187], v[202:203] op_sel_hi:[0,1,1]\n\t" \
  "v_pk_fma_f32 v[194:195], v[146:147], v[194:195], v[210:211] op_sel_hi:[0,1,1]\n\t" \
  "v_pk_mul_f32 v[196:197], v[140:141], v[140:141] op_sel_hi:[0,1]\n\t" \
  "v_pk_mul_f32 v[198:199], v[142:143], v[142:143] op_sel_hi:[0,1]\n\t" \
  "v_pk_mul_f32 v[200:201], v[144:145], v[144:145] op_sel_hi:[0,1]\n\t" \
  "v_pk_mul_f32 v[202:203], v[146:147], v[146:147] op_sel_hi:[0,1]\n\t" \
  "v_mov_b32 v172, v180\n\t" \
  "v_mov_b32 v173, v188\n\t" \
  "v_mov_b32 v174, v182\n\t" \
  "v_mov_b32 v175, v190\n\t" \
  "v_mov_b32 v176, v184\n\t" \
  "v_mov_b32 v177, v192\n\t" \
  "v_mov_b32 v178, v186\n\t" \
  "v_mov_b32 v179, v194\n\t" \
  "v_pk_fma_f32 v[156:157], v[140:141], v[140:141], v[196:197] op_sel:[1,1,0] op_sel_hi:[1,0,1] neg_lo:[1,0,0]\n\t" \
  "v_pk_fma_f32 v[158:159], v[142:143], v[142:143], v[198:199] op_sel:[1,1,0] op_sel_hi:[1,0,1] neg_lo:[1,0,0]\n\t" \
  "v_pk_fma_f32 v[160:161], v[144:145], v[144:145], v[200:201] op_sel:[1,1,0] op_sel_hi:[1,0,1] neg_lo:[1,0,0]\n\t" \
  "v_pk_fma_f32 v[162:163], v[146:147], v[146:147], v[202:203] op_sel:[1,1,0] op_sel_hi:[1,0,1] neg_lo:[1,0,0]\n\t" \
  "v_pk_mul_f32 v[196:197], v[156:157], v[156:157] op_sel_hi:[0,1]\n\t" \
  "v_pk_mul_f32 v[198:199], v[158:159], v[158:159] op_sel_hi:[0,1]\n\t" \
  "v_pk_mul_f32 v[200:201], v[160:161], v[160:161] op_sel_hi:[0,1]\n\t" \
  "v_pk_mul_f32 v[202:203], v[162:163], v[162:163] op_sel_hi:[0,1]\n\t" \
  "v_mov_b32 v188, v181\n\t" \
  "v_mov_b32 v190, v183\n\t" \
  "v_mov_b32 v192, v185\n\t" \
  "v_mov_b32 v194, v187\n\t" \
  "v_pk_fma_f32 v[156:157], v[156:157], v[156:157], v[196:197] op_sel:[1,1,0] op_sel_hi:[1,0,1] neg_lo:[1,0,0]\n\t" \
  "v_pk_fma_f32 v[158:159], v[158:159], v[158:159], v[198:199] op_sel:[1,1,0] op_sel_hi:[1,0,1] neg_lo:[1,0,0]\n\t" \
  "v_pk_fma_f32 v[160:161], v[160:161], v[160:161], v[200:201] op_sel:[1,1,0] op_sel_hi:[1,0,1] neg_lo:[1,0,0]\n\t" \
  "v_pk_fma_f32 v[162:163], v[162:163], v[162:163], v[202:203] op_sel:[1,1,0] op_sel_hi:[1,0,1] neg_lo:[1,0,0]\n\t" \
  "v_pk_mul_f32 v[196:197], v[156:157], v[156:157] op_sel_hi:[0,1]\n\t" \
  "v_pk_mul_f32 v[198:199], v[158:159], v[158:159] op_sel_hi:[0,1]\n\t" \
  "v_pk_mul_f32 v[200:201], v[160:161], v[160:161] op_sel_hi:[0,1]\n\t" \
  "v_pk_mul_f32 v[202:203], v[162:163], v[162:163] op_sel_hi:[0,1]\n\t" \
  "v_pk_fma_f32 v[156:157], v[156:157], v[156:157], v[196:197] op_sel:[1,1,0] op_sel_hi:[1,0,1] neg_lo:[1,0,0]\n\t" \
  "v_pk_fma_f32 v[158:159], v[158:159], v[158:159], v[198:199] op_sel:[1,1,0] op_sel_hi:[1,0,1] neg_lo:[1,0,0]\n\t" \
  "v_pk_fma_f32 v[160:161], v[160:161], v[160:161], v[200:201] op_sel:[1,1,0] op_sel_hi:[1,0,1] neg_lo:[1,0,0]\n\t" \
  "v_pk_fma_f32 v[162:163], v[162:163], v[162:163], v[202:203] op_sel:[1,1,0] op_sel_hi:[1,0,1] neg_lo:[1,0,0]\n\t" \
  "v_pk_fma_f32 v[196:197], v[156:157], v[172:173], v[188:189] op_sel_hi:[0,1,1]\n\t" \
  "v_pk_fma_f32 v[198:199], v[158:159], v[174:175], v[190:191] op_sel_hi:[0,1,1]\n\t" \
  "v_pk_fma_f32 v[200:201], v[160:161], v[176:177], v[192:193] op_sel_hi:[0,1,1]\n\t" \
  "v_pk_fma_f32 v[202:203], v[162:163], v[178:179], v[194:195] op_sel_hi:[0,1,1]\n\t" \
  "v_pk_fma_f32 v[216:217], v[156:157], v[172:173], v[196:197] op_sel:[1,1,0] op_sel_hi:[1,0,1] neg_lo:[1,0,0]\n\t" \
  "v_pk_fma_f32 v[218:219], v[158:159], v[174:175], v[198:199] op_sel:[1,1,0] op_sel_hi:[1,0,1] neg_lo:[1,0,0]\n\t" \
  "v_pk_fma_f32 v[220:221], v[160:161], v[176:177], v[200:201] op_sel:[1,1,0] op_sel_hi:[1,0,1] neg_lo:[1,0,0]\n\t" \
  "v_pk_fma_f32 v[222:223], v[162:163], v[178:179], v[202:203] op_sel:[1,1,0] op_sel_hi:[1,0,1] neg_lo:[1,0,0]\n\t" \
  "ds_write_b128 v212, v[216:219] offset:16384\n\t" \
  "ds_write_b128 v212, v[220:223] offset:16400\n\t" \
  "buffer_load_dwordx4 a[188:191], v4, s[44:47], s61 offen offset:2048 sc1\n\t" \
  "buffer_load_dwordx4 a[192:195], v4, s[44:47], s61 offen offset:3072 sc1\n\t" \
  "s_waitcnt lgkmcnt(0)\n\t" \
  "s_barrier\n\t" \
  "s_cmp_lg_u32 s78, 0\n\t" \
  "s_cbranch_scc1 ST_A_%=\n\t" \
  "v_add_u32 v214, 143360, v213\n\t" \
  "ds_read_b64 v[220:221], v213 offset:16384\n\t" \
  "ds_read_b64 v[222:223], v213 offset:16896\n\t" \
  "ds_read_b64 v[224:225], v213 offset:17408\n\t" \
  "ds_read_b64 v[226:227], v213 offset:17920\n\t" \
  "ds_read_b64 v[228:229], v213 offset:36864\n\t" \
  "ds_read_b64 v[230:231], v213 offset:37376\n\t" \
  "ds_read_b64 v[232:233], v213 offset:37888\n\t" \
  "ds_read_b64 v[234:235], v213 offset:38400\n\t" \
  "ds_read_b64 v[236:237], v213 offset:57344\n\t" \
  "ds_read_b64 v[238:239], v213 offset:57856\n\t" \
  "ds_read_b64 v[240:241], v213 offset:58368\n\t" \
  "ds_read_b64 v[242:243], v213 offset:58880\n\t" \
  "ds_read_b64 v[244:245], v214 offset:16384\n\t" \
  "ds_read_b64 v[246:247], v214 offset:16896\n\t" \
  "ds_read_b64 v[248:249], v214 offset:17408\n\t" \
  "ds_read_b64 v[250:251], v214 offset:17920\n\t" \
  "v_pk_mul_f32 v[254:255], v[136:137], v[136:137] op_sel_hi:[0,1]\n\t" \
  "s_nop 0\n\t" \
  "v_pk_fma_f32 v[252:253], v[136:137], v[136:137], v[254:255] op_sel:[1,1,0] op_sel_hi:[1,0,1] neg_lo:[1,0,0]\n\t" \
  "s_nop 0\n\t" \
  "v_pk_mul_f32 v[254:255], v[252:253], v[252:253] op_sel_hi:[0,1]\n\t" \
  "s_nop 0\n\t" \
  "v_pk_fma_f32 v[252:253], v[252:253], v[252:253], v[254:255] op_sel:[1,1,0] op_sel_hi:[1,0,1] neg_lo:[1,0,0]\n\t" \
  "s_nop 0\n\t" \
  "v_pk_mul_f32 v[254:255], v[252:253], v[252:253] op_sel_hi:[0,1]\n\t" \
  "s_nop 0\n\t" \
  "v_pk_fma_f32 v[252:253], v[252:253], v[252:253], v[254:255] op_sel:[1,1,0] op_sel_hi:[1,0,1] neg_lo:[1,0,0]\n\t" \
  "s_nop 0\n\t" \
  "v_pk_mul_f32 v[254:255], v[252:253], v[252:253] op_sel_hi:[0,1]\n\t" \
  "s_nop 0\n\t" \
  "v_pk_fma_f32 v[252:253], v[252:253], v[252:253], v[254:255] op_sel:[1,1,0] op_sel_hi:[1,0,1] neg_lo:[1,0,0]\n\t" \
  "s_nop 0\n\t" \
  "s_waitcnt lgkmcnt(0)\n\t" \
  "v_pk_fma_f32 v[254:255], v[252:253], v[220:221], v[222:223] op_sel_hi:[0,1,1]\n\t" \
  "v_pk_fma_f32 v[222:223], v[252:253], v[220:221], v[254:255] op_sel:[1,1,0] op_sel_hi:[1,0,1] neg_lo:[1,0,0]\n\t" \
  "v_pk_fma_f32 v[254:255], v[252:253], v[222:223], v[224:225] op_sel_hi:[0,1,1]\n\t" \
  "v_pk_fma_f32 v[224:225], v[252:253], v[222:223], v[254:255] op_sel:[1,1,0] op_sel_hi:[1,0,1] neg_lo:[1,0,0]\n\t" \
  "v_pk_fma_f32 v[254:255], v[252:253], v[224:225], v[226:227] op_sel_hi:[0,1,1]\n\t" \
  "v_pk_fma_f32 v[226:227], v[252:253], v[224:225], v[254:255] op_sel:[1,1,0] op_sel_hi:[1,0,1] neg_lo:[1,0,0]\n\t" \
  "v_pk_fma_f32 v[254:255], v[252:253], v[226:227], v[228:229] op_sel_hi:[0,1,1]\n\t" \
  "v_pk_fma_f32 v[228:229], v[252:253], v[226:227], v[254:255] op_sel:[1,1,0] op_sel_hi:[1,0,1] neg_lo:[1,0,0]\n\t" \
  "v_pk_fma_f32 v[254:255], v[252:253], v[228:229], v[230:231] op_sel_hi:[0,1,1]\n\t" \
  "v_pk_fma_f32 v[230:231], v[252:253], v[228:229], v[254:255] op_sel:[1,1,0] op_sel_hi:[1,0,1] neg_lo:[1,0,0]\n\t" \
  "v_pk_fma_f32 v[254:255], v[252:253], v[230:231], v[232:233] op_sel_hi:[0,1,1]\n\t" \
  "v_pk_fma_f32 v[232:233], v[252:253], v[230:231], v[254:255] op_sel:[1,1,0] op_sel_hi:[1,0,1] neg_lo:[1,0,0]\n\t" \
  "v_pk_fma_f32 v[254:255], v[252:253], v[232:233], v[234:235] op_sel_hi:[0,1,1]\n\t" \
  "v_pk_fma_f32 v[234:235], v[252:253], v[232:233], v[254:255] op_sel:[1,1,0] op_sel_hi:[1,0,1] neg_lo:[1,0,0]\n\t" \
  "v_pk_fma_f32 v[254:255], v[252:253], v[234:235], v[236:237] op_sel_hi:[0,1,1]\n\t" \
  "v_pk_fma_f32 v[236:237], v[252:253], v[234:235], v[254:255] op_sel:[1,1,0] op_sel_hi:[1,0,1] neg_lo:[1,0,0]\n\t" \
  "v_pk_fma_f32 v[254:255], v[252:253], v[236:237], v[238:239] op_sel_hi:[0,1,1]\n\t" \
  "v_pk_fma_f32 v[238:239], v[252:253], v[236:237], v[254:255] op_sel:[1,1,0] op_sel_hi:[1,0,1] neg_lo:[1,0,0]\n\t" \
  "v_pk_fma_f32 v[254:255], v[252:253], v[238:239], v[240:241] op_sel_hi:[0,1,1]\n\t" \
  "v_pk_fma_f32 v[240:241], v[252:253], v[238:239], v[254:255] op_sel:[1,1,0] op_sel_hi:[1,0,1] neg_lo:[1,0,0]\n\t" \
  "v_pk_fma_f32 v[254:255], v[252:253], v[240:241], v[242:243] op_sel_hi:[0,1,1]\n\t" \
  "v_pk_fma_f32 v[242:243], v[252:253], v[240:241], v[254:255] op_sel:[1,1,0] op_sel_hi:[1,0,1] neg_lo:[1,0,0]\n\t" \
  "v_pk_fma_f32 v[254:255], v[252:253], v[242:243], v[244:245] op_sel_hi:[0,1,1]\n\t" \
  "v_pk_fma_f32 v[244:245], v[252:253], v[242:243], v[254:255] op_sel:[1,1,0] op_sel_hi:[1,0,1] neg_lo:[1,0,0]\n\t" \
  "v_pk_fma_f32 v[254:255], v[252:253], v[244:245], v[246:247] op_sel_hi:[0,1,1]\n\t" \
  "v_pk_fma_f32 v[246:247], v[252:253], v[244:245], v[254:255] op_sel:[1,1,0] op_sel_hi:[1,0,1] neg_lo:[1,0,0]\n\t" \
  "v_pk_fma_f32 v[254:255], v[252:253], v[246:247], v[248:249] op_sel_hi:[0,1,1]\n\t" \
  "v_pk_fma_f32 v[248:249], v[252:253], v[246:247], v[254:255] op_sel:[1,1,0] op_sel_hi:[1,0,1] neg_lo:[1,0,0]\n\t" \
  "v_pk_fma_f32 v[254:255], v[252:253], v[248:249], v[250:251] op_sel_hi:[0,1,1]\n\t" \
  "v_pk_fma_f32 v[250:251], v[252:253], v[248:249], v[254:255] op_sel:[1,1,0] op_sel_hi:[1,0,1] neg_lo:[1,0,0]\n\t" \
  "v_mov_b32 v216, v250\n\t" \
  "v_mov_b32 v217, s74\n\t" \
  "v_mov_b32 v218, v251\n\t" \
  "v_mov_b32 v219, s74\n\t" \
  "buffer_store_dwordx4 v[216:219], v4, s[44:47], s80 offen sc1\n\t" \
  "v_mov_b32 v254, 0\n\t" \
  "v_mov_b32 v255, 0\n\t" \
  "ds_write_b64 v213, v[254:255] offset:16384\n\t" \
  "ds_write_b64 v213, v[220:221] offset:16896\n\t" \
  "ds_write_b64 v213, v[222:223] offset:17408\n\t" \
  "ds_write_b64 v213, v[224:225] offset:17920\n\t" \
  "ds_write_b64 v213, v[226:227] offset:36864\n\t" \
  "ds_write_b64 v213, v[228:229] offset:37376\n\t" \
  "ds_write_b64 v213, v[230:231] offset:37888\n\t" \
  "ds_write_b64 v213, v[232:233] offset:38400\n\t" \
  "ds_write_b64 v213, v[234:235] offset:57344\n\t" \
  "ds_write_b64 v213, v[236:237] offset:57856\n\t" \
  "ds_write_b64 v213, v[238:239] offset:58368\n\t" \
  "ds_write_b64 v213, v[240:241] offset:58880\n\t" \
  "ds_write_b64 v214, v[242:243] offset:16384\n\t" \
  "ds_write_b64 v214, v[244:245] offset:16896\n\t" \
  "ds_write_b64 v214, v[246:247] offset:17408\n\t" \
  "ds_write_b64 v214, v[248:249] offset:17920\n\t" \
  "v_pk_mul_f32 v[138:139], v[252:253], v[252:253] op_sel_hi:[0,1]\n\t" \
  "s_nop 0\n\t" \
  "v_pk_fma_f32 v[252:253], v[252:253], v[252:253], v[138:139] op_sel:[1,1,0] op_sel_hi:[1,0,1] neg_lo:[1,0,0]\n\t" \
  "s_nop 0\n\t" \
  "v_pk_mul_f32 v[138:139], v[252:253], v[252:253] op_sel_hi:[0,1]\n\t" \
  "s_nop 0\n\t" \
  "v_pk_fma_f32 v[252:253], v[252:253], v[252:253], v[138:139] op_sel:[1,1,0] op_sel_hi:[1,0,1] neg_lo:[1,0,0]\n\t" \
  "s_nop 0\n\t" \
  "v_pk_mul_f32 v[138:139], v[252:253], v[252:253] op_sel_hi:[0,1]\n\t" \
  "s_nop 0\n\t" \
  "v_pk_fma_f32 v[252:253], v[252:253], v[252:253], v[138:139] op_sel:[1,1,0] op_sel_hi:[1,0,1] neg_lo:[1,0,0]\n\t" \
  "s_nop 0\n\t" \
  "v_pk_mul_f32 v[138:139], v[252:253], v[252:253] op_sel_hi:[0,1]\n\t" \
  "s_nop 0\n\t" \
  "v_pk_fma_f32 v[252:253], v[252:253], v[252:253], v[138:139] op_sel:[1,1,0] op_sel_hi:[1,0,1] neg_lo:[1,0,0]\n\t" \
  "s_nop 0\n\t" \
  "ST_A_%=:\n\t" \
  "v_pk_mul_f32 v[180:181], v[148:149], v[24:25] op_sel:[1,0] neg_lo:[1,0] neg_hi:[1,0]\n\t" \
  "v_pk_mul_f32 v[182:183], v[148:149], v[8:9] op_sel:[1,0] neg_lo:[1,0] neg_hi:[1,0]\n\t" \
  "v_pk_mul_f32 v[184:185], v[148:149], v[26:27] op_sel:[1,0] neg_lo:[1,0] neg_hi:[1,0]\n\t" \
  "v_pk_mul_f32 v[186:187], v[148:149], v[10:11] op_sel:[1,0] neg_lo:[1,0] neg_hi:[1,0]\n\t" \
  "v_pk_mul_f32 v[188:189], v[148:149], v[28:29] op_sel:[1,0] neg_lo:[1,0] neg_hi:[1,0]\n\t" \
  "v_pk_mul_f32 v[190:191], v[148:149], v[12:13] op_sel:[1,0] neg_lo:[1,0] neg_hi:[1,0]\n\t" \
  "v_pk_mul_f32 v[192:193], v[148:149], v[30:31] op_sel:[1,0] neg_lo:[1,0] neg_hi:[1,0]\n\t" \
  "v_pk_mul_f32 v[194:195], v[148:149], v[14:15] op_sel:[1,0] neg_lo:[1,0] neg_hi:[1,0]\n\t" \
  "buffer_load_dwordx4 a[196:199], v5, s[44:47], s61 offen offset:0 sc1\n\t" \
  "v_pk_fma_f32 v[8:9], v[148:149], v[8:9], v[180:181] op_sel_hi:[0,1,1]\n\t" \
  "v_pk_fma_f32 v[24:25], v[148:149], v[24:25], v[182:183] op_sel_hi:[0,1,1] neg_lo:[1,0,0] neg_hi:[1,0,0]\n\t" \
  "v_pk_fma_f32 v[10:11], v[148:149], v[10:11], v[184:185] op_sel_hi:[0,1,1]\n\t" \
  "v_pk_fma_f32 v[26:27], v[148:149], v[26:27], v[186:187] op_sel_hi:[0,1,1] neg_lo:[1,0,0] neg_hi:[1,0,0]\n\t" \
  "v_pk_fma_f32 v[12:13], v[148:149], v[12:13], v[188:189] op_sel_hi:[0,1,1]\n\t" \
  "v_pk_fma_f32 v[28:29], v[148:149], v[28:29], v[190:191] op_sel_hi:[0,1,1] neg_lo:[1,0,0] neg_hi:[1,0,0]\n\t" \
  "v_pk_fma_f32 v[14:15], v[148:149], v[14:15], v[192:193] op_sel_hi:[0,1,1]\n\t" \
  "v_pk_fma_f32 v[30:31], v[148:149], v[30:31], v[194:195] op_sel_hi:[0,1,1] neg_lo:[1,0,0] neg_hi:[1,0,0]\n\t" \
  "v_pk_mul_f32 v[180:181], v[148:149], v[32:33] op_sel:[1,0] neg_lo:[1,0] neg_hi:[1,0]\n\t" \
  "v_pk_mul_f32 v[182:183], v[148:149], v[16:17] op_sel:[1,0] neg_lo:[1,0] neg_hi:[1,0]\n\t" \
  "v_pk_mul_f32 v[184:185], v[148:149], v[34:35] op_sel:[1,0] neg_lo:[1,0] neg_hi:[1,0]\n\t" \
  "v_pk_mul_f32 v[186:187], v[148:149], v[18:19] op_sel:[1,0] neg_lo:[1,0] neg_hi:[1,0]\n\t" \
  "v_pk_mul_f32 v[188:189], v[148:149], v[36:37] op_sel:[1,0] neg_lo:[1,0] neg_hi:[1,0]\n\t" \
  "v_pk_mul_f32 v[190:191], v[148:149], v[20:21] op_sel:[1,0] neg_lo:[1,0] neg_hi:[1,0]\n\t" \
  "v_pk_mul_f32 v[192:193], v[148:149], v[38:39] op_sel:[1,0] neg_lo:[1,0] neg_hi:[1,0]\n\t" \
  "v_pk_mul_f32 v[194:195], v[148:149], v[22:23] op_sel:[1,0] neg_lo:[1,0] neg_hi:[1,0]\n\t" \
  "s_add_u32 s61, s73, 81920\n\t" \
  "buffer_load_dwordx4 a[200:203], v4, s[44:47], s61 offen offset:0 sc1\n\t" \
  "v_pk_fma_f32 v[16:17], v[148:149], v[16:17], v[180:181] op_sel_hi:[0,1,1]\n\t" \
  "v_pk_fma_f32 v[32:33], v[148:149], v[32:33], v[182:183] op_sel_hi:[0,1,1] neg_lo:[1,0,0] neg_hi:[1,0,0]\n\t" \
  "v_pk_fma_f32 v[18:19], v[148:149], v[18:19], v[184:185] op_sel_hi:[0,1,1]\n\t" \
  "v_pk_fma_f32 v[34:35], v[148:149], v[34:35], v[186:187] op_sel_hi:[0,1,1] neg_lo:[1,0,0] neg_hi:[1,0,0]\n\t" \
  "v_pk_fma_f32 v[20:21], v[148:149], v[20:21], v[188:189] op_sel_hi:[0,1,1]\n\t" \
  "v_pk_fma_f32 v[36:37], v[148:149], v[36:37], v[190:191] op_sel_hi:[0,1,1] neg_lo:[1,0,0] neg_hi:[1,0,0]\n\t" \
  "v_pk_fma_f32 v[22:23], v[148:149], v[22:23], v[192:193] op_sel_hi:[0,1,1]\n\t" \
  "v_pk_fma_f32 v[38:39], v[148:149], v[38:39], v[194:195] op_sel_hi:[0,1,1] neg_lo:[1,0,0] neg_hi:[1,0,0]\n\t" \
  "v_pk_mul_f32 v[180:181], v[150:151], v[56:57] op_sel:[1,0] neg_lo:[1,0] neg_hi:[1,0]\n\t" \
  "v_pk_mul_f32 v[182:183], v[150:151], v[40:41] op_sel:[1,0] neg_lo:[1,0] neg_hi:[1,0]\n\t" \
  "v_pk_mul_f32 v[184:185], v[150:151], v[58:59] op_sel:[1,0] neg_lo:[1,0] neg_hi:[1,0]\n\t" \
  "v_pk_mul_f32 v[186:187], v[150:151], v[42:43] op_sel:[1,0] neg_lo:[1,0] neg_hi:[1,0]\n\t" \
  "v_pk_mul_f32 v[188:189], v[150:151], v[60:61] op_sel:[1,0] neg_lo:[1,0] neg_hi:[1,0]\n\t" \
  "v_pk_mul_f32 v[190:191], v[150:151], v[44:45] op_sel:[1,0] neg_lo:[1,0] neg_hi:[1,0]\n\t" \
  "v_pk_mul_f32 v[192:193], v[150:151], v[62:63] op_sel:[1,0] neg_lo:[1,0] neg_hi:[1,0]\n\t" \
  "v_pk_mul_f32 v[194:195], v[150:151], v[46:47] op_sel:[1,0] neg_lo:[1,0] neg_hi:[1,0]\n\t" \
  "buffer_load_dwordx4 a[204:207], v4, s[44:47], s61 offen offset:1024 sc1\n\t" \
  "v_pk_fma_f32 v[40:41], v[150:151], v[40:41], v[180:181] op_sel_hi:[0,1,1]\n\t" \
  "v_pk_fma_f32 v[56:57], v[150:151], v[56:57], v[182:183] op_sel_hi:[0,1,1] neg_lo:[1,0,0] neg_hi:[1,0,0]\n\t" \
  "v_pk_fma_f32 v[42:43], v[150:151], v[42:43], v[184:185] op_sel_hi:[0,1,1]\n\t" \
  "v_pk_fma_f32 v[58:59], v[150:151], v[58:59], v[186:187] op_sel_hi:[0,1,1] neg_lo:[1,0,0] neg_hi:[1,0,0]\n\t" \
  "v_pk_fma_f32 v[44:45], v[150:151], v[44:45], v[188:189] op_sel_hi:[0,1,1]\n\t" \
  "v_pk_fma_f32 v[60:61], v[150:151], v[60:61], v[190:191] op_sel_hi:[0,1,1] neg_lo:[1,0,0] neg_hi:[1,0,0]\n\t" \
  "v_pk_fma_f32 v[46:47], v[150:151], v[46:47], v[192:193] op_sel_hi:[0,1,1]\n\t" \
  "v_pk_fma_f32 v[62:63], v[150:151], v[62:63], v[194:195] op_sel_hi:[0,1,1] neg_lo:[1,0,0] neg_hi:[1,0,0]\n\t" \
  "v_pk_mul_f32 v[180:181], v[150:151], v[64:65] op_sel:[1,0] neg_lo:[1,0] neg_hi:[1,0]\n\t" \
  "v_pk_mul_f32 v[182:183], v[150:151], v[48:49] op_sel:[1,0] neg_lo:[1,0] neg_hi:[1,0]\n\t" \
  "v_pk_mul_f32 v[184:185], v[150:151], v[66:67] op_sel:[1,0] neg_lo:[1,0] neg_hi:[1,0]\n\t" \
  "v_pk_mul_f32 v[186:187], v[150:151], v[50:51] op_sel:[1,0] neg_lo:[1,0] neg_hi:[1,0]\n\t" \
  "v_pk_mul_f32 v[188:189], v[150:151], v[68:69] op_sel:[1,0] neg_lo:[1,0] neg_hi:[1,0]\n\t" \
  "v_pk_mul_f32 v[190:191], v[150:151], v[52:53] op_sel:[1,0] neg_lo:[1,0] neg_hi:[1,0]\n\t" \
  "v_pk_mul_f32 v[192:193], v[150:151], v[70:71] op_sel:[1,0] neg_lo:[1,0] neg_hi:[1,0]\n\t" \
  "v_pk_mul_f32 v[194:195], v[150:151], v[54:55] op_sel:[1,0] neg_lo:[1,0] neg_hi:[1,0]\n\t" \
  "v_pk_fma_f32 v[48:49], v[150:151], v[48:49], v[180:181] op_sel_hi:[0,1,1]\n\t" \
  "v_pk_fma_f32 v[64:65], v[150:151], v[64:65], v[182:183] op_sel_hi:[0,1,1] neg_lo:[1,0,0] neg_hi:[1,0,0]\n\t" \
  "v_pk_fma_f32 v[50:51], v[150:151], v[50:51], v[184:185] op_sel_hi:[0,1,1]\n\t" \
  "v_pk_fma_f32 v[66:67], v[150:151], v[66:67], v[186:187] op_sel_hi:[0,1,1] neg_lo:[1,0,0] neg_hi:[1,0,0]\n\t" \
  "v_pk_fma_f32 v[52:53], v[150:151], v[52:53], v[188:189] op_sel_hi:[0,1,1]\n\t" \
  "v_pk_fma_f32 v[68:69], v[150:151], v[68:69], v[190:191] op_sel_hi:[0,1,1] neg_lo:[1,0,0] neg_hi:[1,0,0]\n\t" \
  "v_pk_fma_f32 v[54:55], v[150:151], v[54:55], v[192:193] op_sel_hi:[0,1,1]\n\t" \
  "v_pk_fma_f32 v[70:71], v[150:151], v[70:71], v[194:195] op_sel_hi:[0,1,1] neg_lo:[1,0,0] neg_hi:[1,0,0]\n\t" \
  "v_pk_mul_f32 v[180:181], v[152:153], v[88:89] op_sel:[1,0] neg_lo:[1,0] neg_hi:[1,0]\n\t" \
  "v_pk_mul_f32 v[182:183], v[152:153], v[72:73] op_sel:[1,0] neg_lo:[1,0] neg_hi:[1,0]\n\t" \
  "v_pk_mul_f32 v[184:185], v[152:153], v[90:91] op_sel:[1,0] neg_lo:[1,0] neg_hi:[1,0]\n\t" \
  "v_pk_mul_f32 v[186:187], v[152:153], v[74:75] op_sel:[1,0] neg_lo:[1,0] neg_hi:[1,0]\n\t" \
  "v_pk_mul_f32 v[188:189], v[152:153], v[92:93] op_sel:[1,0] neg_lo:[1,0] neg_hi:[1,0]\n\t" \
  "v_pk_mul_f32 v[190:191], v[152:153], v[76:77] op_sel:[1,0] neg_lo:[1,0] neg_hi:[1,0]\n\t" \
  "v_pk_mul_f32 v[192:193], v[152:153], v[94:95] op_sel:[1,0] neg_lo:[1,0] neg_hi:[1,0]\n\t" \
  "v_pk_mul_f32 v[194:195], v[152:153], v[78:79] op_sel:[1,0] neg_lo:[1,0] neg_hi:[1,0]\n\t" \
  "v_pk_fma_f32 v[72:73], v[152:153], v[72:73], v[180:181] op_sel_hi:[0,1,1]\n\t" \
  "v_pk_fma_f32 v[88:89], v[152:153], v[88:89], v[182:183] op_sel_hi:[0,1,1] neg_lo:[1,0,0] neg_hi:[1,0,0]\n\t" \
  "v_pk_fma_f32 v[74:75], v[152:153], v[74:75], v[184:185] op_sel_hi:[0,1,1]\n\t" \
  "v_pk_fma_f32 v[90:91], v[152:153], v[90:91], v[186:187] op_sel_hi:[0,1,1] neg_lo:[1,0,0] neg_hi:[1,0,0]\n\t" \
  "v_pk_fma_f32 v[76:77], v[152:153], v[76:77], v[188:189] op_sel_hi:[0,1,1]\n\t" \
  "v_pk_fma_f32 v[92:93], v[152:153], v[92:93], v[190:191] op_sel_hi:[0,1,1] neg_lo:[1,0,0] neg_hi:[1,0,0]\n\t" \
  "v_pk_fma_f32 v[78:79], v[152:153], v[78:79], v[192:193] op_sel_hi:[0,1,1]\n\t" \
  "v_pk_fma_f32 v[94:95], v[152:153], v[94:95], v[194:195] op_sel_hi:[0,1,1] neg_lo:[1,0,0] neg_hi:[1,0,0]\n\t" \
  "v_pk_mul_f32 v[180:181], v[152:153], v[96:97] op_sel:[1,0] neg_lo:[1,0] neg_hi:[1,0]\n\t" \
  "v_pk_mul_f32 v[182:183], v[152:153], v[80:81] op_sel:[1,0] neg_lo:[1,0] neg_hi:[1,0]\n\t" \
  "v_pk_mul_f32 v[184:185], v[152:153], v[98:99] op_sel:[1,0] neg_lo:[1,0] neg_hi:[1,0]\n\t" \
  "v_pk_mul_f32 v[186:187], v[152:153], v[82:83] op_sel:[1,0] neg_lo:[1,0] neg_hi:[1,0]\n\t" \
  "v_pk_mul_f32 v[188:189], v[152:153], v[100:101] op_sel:[1,0] neg_lo:[1,0] neg_hi:[1,0]\n\t" \
  "v_pk_mul_f32 v[190:191], v[152:153], v[84:85] op_sel:[1,0] neg_lo:[1,0] neg_hi:[1,0]\n\t" \
  "v_pk_mul_f32 v[192:193], v[152:153], v[102:103] op_sel:[1,0] neg_lo:[1,0] neg_hi:[1,0]\n\t" \
  "v_pk_mul_f32 v[194:195], v[152:153], v[86:87] op_sel:[1,0] neg_lo:[1,0] neg_hi:[1,0]\n\t" \
  "v_pk_fma_f32 v[80:81], v[152:153], v[80:81], v[180:181] op_sel_hi:[0,1,1]\n\t" \
  "v_pk_fma_f32 v[96:97], v[152:153], v[96:97], v[182:183] op_sel_hi:[0,1,1] neg_lo:[1,0,0] neg_hi:[1,0,0]\n\t" \
  "v_pk_fma_f32 v[82:83], v[152:153], v[82:83], v[184:185] op_sel_hi:[0,1,1]\n\t" \
  "v_pk_fma_f32 v[98:99], v[152:153], v[98:99], v[186:187] op_sel_hi:[0,1,1] neg_lo:[1,0,0] neg_hi:[1,0,0]\n\t" \
  "v_pk_fma_f32 v[84:85], v[152:153], v[84:85], v[188:189] op_sel_hi:[0,1,1]\n\t" \
  "v_pk_fma_f32 v[100:101], v[152:153], v[100:101], v[190:191] op_sel_hi:[0,1,1] neg_lo:[1,0,0] neg_hi:[1,0,0]\n\t" \
  "v_pk_fma_f32 v[86:87], v[152:153], v[86:87], v[192:193] op_sel_hi:[0,1,1]\n\t" \
  "v_pk_fma_f32 v[102:103], v[152:153], v[102:103], v[194:195] op_sel_hi:[0,1,1] neg_lo:[1,0,0] neg_hi:[1,0,0]\n\t" \
  "v_pk_mul_f32 v[180:181], v[154:155], v[120:121] op_sel:[1,0] neg_lo:[1,0] neg_hi:[1,0]\n\t" \
  "v_pk_mul_f32 v[182:183], v[154:155], v[104:105] op_sel:[1,0] neg_lo:[1,0] neg_hi:[1,0]\n\t" \
  "v_pk_mul_f32 v[184:185], v[154:155], v[122:123] op_sel:[1,0] neg_lo:[1,0] neg_hi:[1,0]\n\t" \
  "v_pk_mul_f32 v[186:187], v[154:155], v[106:107] op_sel:[1,0] neg_lo:[1,0] neg_hi:[1,0]\n\t" \
  "v_pk_mul_f32 v[188:189], v[154:155], v[124:125] op_sel:[1,0] neg_lo:[1,0] neg_hi:[1,0]\n\t" \
  "v_pk_mul_f32 v[190:191], v[154:155], v[108:109] op_sel:[1,0] neg_lo:[1,0] neg_hi:[1,0]\n\t" \
  "v_pk_mul_f32 v[192:193], v[154:155], v[126:127] op_sel:[1,0] neg_lo:[1,0] neg_hi:[1,0]\n\t" \
  "v_pk_mul_f32 v[194:195], v[154:155], v[110:111] op_sel:[1,0] neg_lo:[1,0] neg_hi:[1,0]\n\t" \
  "v_pk_fma_f32 v[104:105], v[154:155], v[104:105], v[180:181] op_sel_hi:[0,1,1]\n\t" \
  "v_pk_fma_f32 v[120:121], v[154:155], v[120:121], v[182:183] op_sel_hi:[0,1,1] neg_lo:[1,0,0] neg_hi:[1,0,0]\n\t" \
  "v_pk_fma_f32 v[106:107], v[154:155], v[106:107], v[184:185] op_sel_hi:[0,1,1]\n\t" \
  "v_pk_fma_f32 v[122:123], v[154:155], v[122:123], v[186:187] op_sel_hi:[0,1,1] neg_lo:[1,0,0] neg_hi:[1,0,0]\n\t" \
  "v_pk_fma_f32 v[108:109], v[154:155], v[108:109], v[188:189] op_sel_hi:[0,1,1]\n\t" \
  "v_pk_fma_f32 v[124:125], v[154:155], v[124:125], v[190:191] op_sel_hi:[0,1,1] neg_lo:[1,0,0] neg_hi:[1,0,0]\n\t" \
  "v_pk_fma_f32 v[110:111], v[154:155], v[110:111], v[192:193] op_sel_hi:[0,1,1]\n\t" \
  "v_pk_fma_f32 v[126:127], v[154:155], v[126:127], v[194:195] op_sel_hi:[0,1,1] neg_lo:[1,0,0] neg_hi:[1,0,0]\n\t" \
  "v_pk_mul_f32 v[180:181], v[154:155], v[128:129] op_sel:[1,0] neg_lo:[1,0] neg_hi:[1,0]\n\t" \
  "v_pk_mul_f32 v[182:183], v[154:155], v[112:113] op_sel:[1,0] neg_lo:[1,0] neg_hi:[1,0]\n\t" \
  "v_pk_mul_f32 v[184:185], v[154:155], v[130:131] op_sel:[1,0] neg_lo:[1,0] neg_hi:[1,0]\n\t" \
  "v_pk_mul_f32 v[186:187], v[154:155], v[114:115] op_sel:[1,0] neg_lo:[1,0] neg_hi:[1,0]\n\t" \
  "v_pk_mul_f32 v[188:189], v[154:155], v[132:133] op_sel:[1,0] neg_lo:[1,0] neg_hi:[1,0]\n\t" \
  "v_pk_mul_f32 v[190:191], v[154:155], v[116:117] op_sel:[1,0] neg_lo:[1,0] neg_hi:[1,0]\n\t" \
  "v_pk_mul_f32 v[192:193], v[154:155], v[134:135] op_sel:[1,0] neg_lo:[1,0] neg_hi:[1,0]\n\t" \
  "v_pk_mul_f32 v[194:195], v[154:155], v[118:119] op_sel:[1,0] neg_lo:[1,0] neg_hi:[1,0]\n\t" \
  "v_pk_fma_f32 v[112:113], v[154:155], v[112:113], v[180:181] op_sel_hi:[0,1,1]\n\t" \
  "v_pk_fma_f32 v[128:129], v[154:155], v[128:129], v[182:183] op_sel_hi:[0,1,1] neg_lo:[1,0,0] neg_hi:[1,0,0]\n\t" \
  "v_pk_fma_f32 v[114:115], v[154:155], v[114:115], v[184:185] op_sel_hi:[0,1,1]\n\t" \
  "v_pk_fma_f32 v[130:131], v[154:155], v[130:131], v[186:187] op_sel_hi:[0,1,1] neg_lo:[1,0,0] neg_hi:[1,0,0]\n\t" \
  "v_pk_fma_f32 v[116:117], v[154:155], v[116:117], v[188:189] op_sel_hi:[0,1,1]\n\t" \
  "v_pk_fma_f32 v[132:133], v[154:155], v[132:133], v[190:191] op_sel_hi:[0,1,1] neg_lo:[1,0,0] neg_hi:[1,0,0]\n\t" \
  "v_pk_fma_f32 v[118:119], v[154:155], v[118:119], v[192:193] op_sel_hi:[0,1,1]\n\t" \
  "v_pk_fma_f32 v[134:135], v[154:155], v[134:135], v[194:195] op_sel_hi:[0,1,1] neg_lo:[1,0,0] neg_hi:[1,0,0]\n\t" \
  "v_and_b32 v214, 0x100, v4\n\t" \
  "v_and_b32 v215, 0x200, v4\n\t" \
  "v_cmp_ne_u32 s[52:53], 0, v214\n\t" \
  "v_cmp_ne_u32 s[54:55], 0, v215\n\t" \
  "s_bitcmp1_b32 s78, 0\n\t" \
  "s_cselect_b64 s[56:57], -1, 0\n\t" \
  "s_bitcmp1_b32 s78, 1\n\t" \
  "s_cselect_b64 s[58:59], -1, 0\n\t" \
  "v_pk_mul_f32 v[196:197], v[156:157], v[156:157] op_sel_hi:[0,1]\n\t" \
  "v_pk_mul_f32 v[198:199], v[158:159], v[158:159] op_sel_hi:[0,1]\n\t" \
  "v_pk_mul_f32 v[200:201], v[160:161], v[160:161] op_sel_hi:[0,1]\n\t" \
  "v_pk_mul_f32 v[202:203], v[162:163], v[162:163] op_sel_hi:[0,1]\n\t" \
  "v_pk_fma_f32 v[180:181], v[156:157], v[156:157], v[196:197] op_sel:[1,1,0] op_sel_hi:[1,0,1] neg_lo:[1,0,0]\n\t" \
  "v_pk_fma_f32 v[182:183], v[158:159], v[158:159], v[198:199] op_sel:[1,1,0] op_sel_hi:[1,0,1] neg_lo:[1,0,0]\n\t" \
  "v_pk_fma_f32 v[184:185], v[160:161], v[160:161], v[200:201] op_sel:[1,1,0] op_sel_hi:[1,0,1] neg_lo:[1,0,0]\n\t" \
  "v_pk_fma_f32 v[186:187], v[162:163], v[162:163], v[202:203] op_sel:[1,1,0] op_sel_hi:[1,0,1] neg_lo:[1,0,0]\n\t" \
  "v_cndmask_b32 v164, 1.0, v180, s[52:53]\n\t" \
  "v_cndmask_b32 v165, 0, v181, s[52:53]\n\t" \
  "v_cndmask_b32 v166, 1.0, v182, s[52:53]\n\t" \
  "v_cndmask_b32 v167, 0, v183, s[52:53]\n\t" \
  "v_cndmask_b32 v168, 1.0, v184, s[52:53]\n\t" \
  "v_cndmask_b32 v169, 0, v185, s[52:53]\n\t" \
  "v_cndmask_b32 v170, 1.0, v186, s[52:53]\n\t" \
  "v_cndmask_b32 v171, 0, v187, s[52:53]\n\t" \
  "v_pk_mul_f32 v[196:197], v[180:181], v[180:181] op_sel_hi:[0,1]\n\t" \
  "v_pk_mul_f32 v[198:199], v[182:183], v[182:183] op_sel_hi:[0,1]\n\t" \
  "v_pk_mul_f32 v[200:201], v[184:185], v[184:185] op_sel_hi:[0,1]\n\t" \
  "v_pk_mul_f32 v[202:203], v[186:187], v[186:187] op_sel_hi:[0,1]\n\t" \
  "v_pk_fma_f32 v[180:181], v[180:181], v[180:181], v[196:197] op_sel:[1,1,0] op_sel_hi:[1,0,1] neg_lo:[1,0,0]\n\t" \
  "v_pk_fma_f32 v[182:183], v[182:183], v[182:183], v[198:199] op_sel:[1,1,0] op_sel_hi:[1,0,1] neg_lo:[1,0,0]\n\t" \
  "v_pk_fma_f32 v[184:185], v[184:185], v[184:185], v[200:201] op_sel:[1,1,0] op_sel_hi:[1,0,1] neg_lo:[1,0,0]\n\t" \
  "v_pk_fma_f32 v[186:187], v[186:187], v[186:187], v[202:203] op_sel:[1,1,0] op_sel_hi:[1,0,1] neg_lo:[1,0,0]\n\t" \
  "v_pk_mul_f32 v[204:205], v[164:165], v[180:181] op_sel_hi:[0,1]\n\t" \
  "v_pk_mul_f32 v[206:207], v[166:167], v[182:183] op_sel_hi:[0,1]\n\t" \
  "v_pk_mul_f32 v[208:209], v[168:169], v[184:185] op_sel_hi:[0,1]\n\t" \
  "v_pk_mul_f32 v[210:211], v[170:171], v[186:187] op_sel_hi:[0,1]\n\t" \
  "v_pk_fma_f32 v[188:189], v[164:165], v[180:181], v[204:205] op_sel:[1,1,0] op_sel_hi:[1,0,1] neg_lo:[1,0,0]\n\t" \
  "v_pk_fma_f32 v[190:191], v[166:167], v[182:183], v[206:207] op_sel:[1,1,0] op_sel_hi:[1,0,1] neg_lo:[1,0,0]\n\t" \
  "v_pk_fma_f32 v[192:193], v[168:169], v[184:185], v[208:209] op_sel:[1,1,0] op_sel_hi:[1,0,1] neg_lo:[1,0,0]\n\t" \
  "v_pk_fma_f32 v[194:195], v[170:171], v[186:187], v[210:211] op_sel:[1,1,0] op_sel_hi:[1,0,1] neg_lo:[1,0,0]\n\t" \
  "v_cndmask_b32 v164, v164, v188, s[54:55]\n\t" \
  "v_cndmask_b32 v165, v165, v189, s[54:55]\n\t" \
  "v_cndmask_b32 v166, v166, v190, s[54:55]\n\t" \
  "v_cndmask_b32 v167, v167, v191, s[54:55]\n\t" \
  "v_cndmask_b32 v168, v168, v192, s[54:55]\n\t" \
  "v_cndmask_b32 v169, v169, v193, s[54:55]\n\t" \
  "v_cndmask_b32 v170, v170, v194, s[54:55]\n\t" \
  "v_cndmask_b32 v171, v171, v195, s[54:55]\n\t" \
  "v_pk_mul_f32 v[196:197], v[180:181], v[180:181] op_sel_hi:[0,1]\n\t" \
  "v_pk_mul_f32 v[198:199], v[182:183], v[182:183] op_sel_hi:[0,1]\n\t" \
  "v_pk_mul_f32 v[200:201], v[184:185], v[184:185] op_sel_hi:[0,1]\n\t" \
  "v_pk_mul_f32 v[202:203], v[186:187], v[186:187] op_sel_hi:[0,1]\n\t" \
  "v_pk_fma_f32 v[180:181], v[180:181], v[180:181], v[196:197] op_sel:[1,1,0] op_sel_hi:[1,0,1] neg_lo:[1,0,0]\n\t" \
  "v_pk_fma_f32 v[182:183], v[182:183], v[182:183], v[198:199] op_sel:[1,1,0] op_sel_hi:[1,0,1] neg_lo:[1,0,0]\n\t" \
  "v_pk_fma_f32 v[184:185], v[184:185], v[184:185], v[200:201] op_sel:[1,1,0] op_sel_hi:[1,0,1] neg_lo:[1,0,0]\n\t" \
  "v_pk_fma_f32 v[186:187], v[186:187], v[186:187], v[202:203] op_sel:[1,1,0] op_sel_hi:[1,0,1] neg_lo:[1,0,0]\n\t" \
  "v_pk_mul_f32 v[204:205], v[164:165], v[180:181] op_sel_hi:[0,1]\n\t" \
  "v_pk_mul_f32 v[206:207], v[166:167], v[182:183] op_sel_hi:[0,1]\n\t" \
  "v_pk_mul_f32 v[208:209], v[168:169], v[184:185] op_sel_hi:[0,1]\n\t" \
  "v_pk_mul_f32 v[210:211], v[170:171], v[186:187] op_sel_hi:[0,1]\n\t" \
  "v_pk_fma_f32 v[188:189], v[164:165], v[180:181], v[204:205] op_sel:[1,1,0] op_sel_hi:[1,0,1] neg_lo:[1,0,0]\n\t" \
  "v_pk_fma_f32 v[190:191], v[166:167], v[182:183], v[206:207] op_sel:[1,1,0] op_sel_hi:[1,0,1] neg_lo:[1,0,0]\n\t" \
  "v_pk_fma_f32 v[192:193], v[168:169], v[184:185], v[208:209] op_sel:[1,1,0] op_sel_hi:[1,0,1] neg_lo:[1,0,0]\n\t" \
  "v_pk_fma_f32 v[194:195], v[170:171], v[186:187], v[210:211] op_sel:[1,1,0] op_sel_hi:[1,0,1] neg_lo:[1,0,0]\n\t" \
  "v_cndmask_b32 v164, v164, v188, s[56:57]\n\t" \
  "v_cndmask_b32 v165, v165, v189, s[56:57]\n\t" \
  "v_cndmask_b32 v166, v166, v190, s[56:57]\n\t" \
  "v_cndmask_b32 v167, v167, v191, s[56:57]\n\t" \
  "v_cndmask_b32 v168, v168, v192, s[56:57]\n\t" \
  "v_cndmask_b32 v169, v169, v193, s[56:57]\n\t" \
  "v_cndmask_b32 v170, v170, v194, s[56:57]\n\t" \
  "v_cndmask_b32 v171, v171, v195, s[56:57]\n\t" \
  "v_pk_mul_f32 v[196:197], v[180:181], v[180:181] op_sel_hi:[0,1]\n\t" \
  "v_pk_mul_f32 v[198:199], v[182:183], v[182:183] op_sel_hi:[0,1]\n\t" \
  "v_pk_mul_f32 v[200:201], v[184:185], v[184:185] op_sel_hi:[0,1]\n\t" \
  "v_pk_mul_f32 v[202:203], v[186:187], v[186:187] op_sel_hi:[0,1]\n\t" \
  "v_pk_fma_f32 v[180:181], v[180:181], v[180:181], v[196:197] op_sel:[1,1,0] op_sel_hi:[1,0,1] neg_lo:[1,0,0]\n\t" \
  "v_pk_fma_f32 v[182:183], v[182:183], v[182:183], v[198:199] op_sel:[1,1,0] op_sel_hi:[1,0,1] neg_lo:[1,0,0]\n\t" \
  "v_pk_fma_f32 v[184:185], v[184:185], v[184:185], v[200:201] op_sel:[1,1,0] op_sel_hi:[1,0,1] neg_lo:[1,0,0]\n\t" \
  "v_pk_fma_f32 v[186:187], v[186:187], v[186:187], v[202:203] op_sel:[1,1,0] op_sel_hi:[1,0,1] neg_lo:[1,0,0]\n\t" \
  "v_pk_mul_f32 v[204:205], v[164:165], v[180:181] op_sel_hi:[0,1]\n\t" \
  "v_pk_mul_f32 v[206:207], v[166:167], v[182:183] op_sel_hi:[0,1]\n\t" \
  "v_pk_mul_f32 v[208:209], v[168:169], v[184:185] op_sel_hi:[0,1]\n\t" \
  "v_pk_mul_f32 v[210:211], v[170:171], v[186:187] op_sel_hi:[0,1]\n\t" \
  "v_pk_fma_f32 v[188:189], v[164:165], v[180:181], v[204:205] op_sel:[1,1,0] op_sel_hi:[1,0,1] neg_lo:[1,0,0]\n\t" \
  "v_pk_fma_f32 v[190:191], v[166:167], v[182:183], v[206:207] op_sel:[1,1,0] op_sel_hi:[1,0,1] neg_lo:[1,0,0]\n\t" \
  "v_pk_fma_f32 v[192:193], v[168:169], v[184:185], v[208:209] op_sel:[1,1,0] op_sel_hi:[1,0,1] neg_lo:[1,0,0]\n\t" \
  "v_pk_fma_f32 v[194:195], v[170:171], v[186:187], v[210:211] op_sel:[1,1,0] op_sel_hi:[1,0,1] neg_lo:[1,0,0]\n\t" \
  "v_cndmask_b32 v164, v164, v188, s[58:59]\n\t" \
  "v_cndmask_b32 v165, v165, v189, s[58:59]\n\t" \
  "v_cndmask_b32 v166, v166, v190, s[58:59]\n\t" \
  "v_cndmask_b32 v167, v167, v191, s[58:59]\n\t" \
  "v_cndmask_b32 v168, v168, v192, s[58:59]\n\t" \
  "v_cndmask_b32 v169, v169, v193, s[58:59]\n\t" \
  "v_cndmask_b32 v170, v170, v194, s[58:59]\n\t" \
  "v_cndmask_b32 v171, v171, v195, s[58:59]\n\t" \
  "v_and_b32 v216, 0xc0, v4\n\t" \
  "v_lshlrev_b32 v214, 6, v216\n\t" \
  "v_and_b32 v216, 0x300, v4\n\t" \
  "v_lshl_add_u32 v214, v216, 2, v214\n\t" \
  "v_and_b32 v217, 0x30, v4\n\t" \
  "v_lshl_add_u32 v214, v217, 4, v214\n\t" \
  "v_and_b32 v217, 0x70, v4\n\t" \
  "v_or_b32 v214, v214, v217\n\t" \
  "v_add_u32 v214, s76, v214\n\t" \
  "v_lshrrev_b32 v217, 4, v216\n\t" \
  "v_xor_b32 v215, v4, v217\n\t" \
  "v_add_u32 v215, s76, v215\n\t" \
  "v_xor_b32 v216, 64, v215\n\t" \
  "v_and_b32 v217, 0xf0, v4\n\t" \
  "v_lshlrev_b32 v217, 1, v217\n\t" \
  "v_add_u32 v217, s77, v217\n\t" \
  "s_cmp_lg_u32 s78, 0\n\t" \
  "s_cbranch_scc1 ST_B_%=\n\t" \
  "v_mov_b32 v248, 0\n\t" \
  "v_mov_b32 v249, 0\n\t" \
  "s_cmp_eq_u32 s79, 0\n\t" \
  "s_cbranch_scc1 ST_C_%=\n\t" \
  "ST_P_%=:\n\t" \
  "s_cmp_gt_u32 s79, 0\n\t" \
  "s_cselect_b32 s60, 0, 0\n\t" \
  "s_add_u32 s60, s60, s81\n\t" \
  "buffer_load_dwordx4 v[220:223], v4, s[44:47], s60 offen sc1\n\t" \
  "s_cmp_gt_u32 s79, 1\n\t" \
  "s_cselect_b32 s60, 10240, 0\n\t" \
  "s_add_u32 s60, s60, s81\n\t" \
  "buffer_load_dwordx4 v[224:227], v4, s[44:47], s60 offen sc1\n\t" \
  "s_cmp_gt_u32 s79, 2\n\t" \
  "s_cselect_b32 s60, 20480, 0\n\t" \
  "s_add_u32 s60, s60, s81\n\t" \
  "buffer_load_dwordx4 v[228:231], v4, s[44:47], s60 offen sc1\n\t" \
  "s_cmp_gt_u32 s79, 3\n\t" \
  "s_cselect_b32 s60, 30720, 0\n\t" \
  "s_add_u32 s60, s60, s81\n\t" \
  "buffer_load_dwordx4 v[232:235], v4, s[44:47], s60 offen sc1\n\t" \
  "s_cmp_gt_u32 s79, 4\n\t" \
  "s_cselect_b32 s60, 40960, 0\n\t" \
  "s_add_u32 s60, s60, s81\n\t" \
  "buffer_load_dwordx4 v[236:239], v4, s[44:47], s60 offen sc1\n\t" \
  "s_cmp_gt_u32 s79, 5\n\t" \
  "s_cselect_b32 s60, 51200, 0\n\t" \
  "s_add_u32 s60, s60, s81\n\t" \
  "buffer_load_dwordx4 v[240:243], v4, s[44:47], s60 offen sc1\n\t" \
  "s_cmp_gt_u32 s79, 6\n\t" \
  "s_cselect_b32 s60, 61440, 0\n\t" \
  "s_add_u32 s60, s60, s81\n\t" \
  "buffer_load_dwordx4 v[244:247], v4, s[44:47], s60 offen sc1\n\t" \
  "s_add_u32 s63, s63, 1\n\t" \
  "s_waitcnt vmcnt(0)\n\t" \
  "v_min3_u32 v250, v221, v223, v221\n\t" \
  "v_max3_u32 v251, v221, v223, v221\n\t" \
  "v_min3_u32 v250, v225, v227, v250\n\t" \
  "v_max3_u32 v251, v225, v227, v251\n\t" \
  "v_min3_u32 v250, v229, v231, v250\n\t" \
  "v_max3_u32 v251, v229, v231, v251\n\t" \
  "v_min3_u32 v250, v233, v235, v250\n\t" \
  "v_max3_u32 v251, v233, v235, v251\n\t" \
  "v_min3_u32 v250, v237, v239, v250\n\t" \
  "v_max3_u32 v251, v237, v239, v251\n\t" \
  "v_min3_u32 v250, v241, v243, v250\n\t" \
  "v_max3_u32 v251, v241, v243, v251\n\t" \
  "v_min3_u32 v250, v245, v247, v250\n\t" \
  "v_max3_u32 v251, v245, v247, v251\n\t" \
  "v_cmp_ne_u32 vcc, s74, v250\n\t" \
  "v_cmp_ne_u32 s[52:53], s74, v251\n\t" \
  "s_cmp_gt_u32 s63, 262144\n\t" \
  "s_cbranch_scc1 ST_Q_%=\n\t" \
  "s_or_b64 vcc, vcc, s[52:53]\n\t" \
  "s_cbranch_vccnz ST_P_%=\n\t" \
  "ST_Q_%=:\n\t" \
  "v_mov_b32 v248, v220\n\t" \
  "v_mov_b32 v249, v222\n\t" \
  "s_cmp_le_u32 s79, 1\n\t" \
  "s_cbranch_scc1 ST_C_%=\n\t" \
  "v_fma_f32 v250, -v253, v249, v224\n\t" \
  "v_fma_f32 v251, v253, v248, v226\n\t" \
  "v_fma_f32 v248, v252, v248, v250\n\t" \
  "v_fma_f32 v249, v252, v249, v251\n\t" \
  "s_cmp_le_u32 s79, 2\n\t" \
  "s_cbranch_scc1 ST_C_%=\n\t" \
  "v_fma_f32 v250, -v253, v249, v228\n\t" \
  "v_fma_f32 v251, v253, v248, v230\n\t" \
  "v_fma_f32 v248, v252, v248, v250\n\t" \
  "v_fma_f32 v249, v252, v249, v251\n\t" \
  "s_cmp_le_u32 s79, 3\n\t" \
  "s_cbranch_scc1 ST_C_%=\n\t" \
  "v_fma_f32 v250, -v253, v249, v232\n\t" \
  "v_fma_f32 v251, v253, v248, v234\n\t" \
  "v_fma_f32 v248, v252, v248, v250\n\t" \
  "v_fma_f32 v249, v252, v249, v251\n\t" \
  "s_cmp_le_u32 s79, 4\n\t" \
  "s_cbranch_scc1 ST_C_%=\n\t" \
  "v_fma_f32 v250, -v253, v249, v236\n\t" \
  "v_fma_f32 v251, v253, v248, v238\n\t" \
  "v_fma_f32 v248, v252, v248, v250\n\t" \
  "v_fma_f32 v249, v252, v249, v251\n\t" \
  "s_cmp_le_u32 s79, 5\n\t" \
  "s_cbranch_scc1 ST_C_%=\n\t" \
  "v_fma_f32 v250, -v253, v249, v240\n\t" \
  "v_fma_f32 v251, v253, v248, v242\n\t" \
  "v_fma_f32 v248, v252, v248, v250\n\t" \
  "v_fma_f32 v249, v252, v249, v251\n\t" \
  "s_cmp_le_u32 s79, 6\n\t" \
  "s_cbranch_scc1 ST_C_%=\n\t" \
  "v_fma_f32 v250, -v253, v249, v244\n\t" \
  "v_fma_f32 v251, v253, v248, v246\n\t" \
  "v_fma_f32 v248, v252, v248, v250\n\t" \
  "v_fma_f32 v249, v252, v249, v251\n\t" \
  "ST_C_%=:\n\t" \
  "ds_write_b64 v213, v[248:249] offset:18432\n\t" \
  "s_branch ST_D_%=\n\t" \
  "ST_B_%=:\n\t" \
  "s_add_u32 s61, s73, 81920\n\t" \
  "buffer_load_dwordx4 a[208:211], v4, s[44:47], s61 offen offset:2048 sc1\n\t" \
  "buffer_load_dwordx4 a[212:215], v4, s[44:47], s61 offen offset:3072 sc1\n\t" \
  "buffer_load_dwordx4 a[216:219], v5, s[44:47], s61 offen offset:0 sc1\n\t" \
  "s_add_u32 s61, s73, 122880\n\t" \
  "buffer_load_dwordx4 a[220:223], v4, s[44:47], s61 offen offset:0 sc1\n\t" \
  "buffer_load_dwordx4 a[224:227], v4, s[44:47], s61 offen offset:1024 sc1\n\t" \
  "buffer_load_dwordx4 a[228:231], v4, s[44:47], s61 offen offset:2048 sc1\n\t" \
  "buffer_load_dwordx4 a[232:235], v4, s[44:47], s61 offen offset:3072 sc1\n\t" \
  "buffer_load_dwordx4 a[236:239], v5, s[44:47], s61 offen offset:0 sc1\n\t" \
  "s_cmp_lg_u32 s83, 0\n\t" \
  "s_cbranch_scc1 ST_D_%=\n\t" \
  "s_cmp_lg_u32 s78, 1\n\t" \
  "s_cbranch_scc1 ST_D_%=\n\t" \
  "v_lshlrev_b32 v220, 5, v4\n\t" \
  "ST_EP_%=:\n\t" \
  "s_mov_b32 s60, 16302080\n\t" \
  "buffer_load_dword v221, v220, s[44:47], s60 offen sc1\n\t" \
  "s_mov_b32 s60, 16334848\n\t" \
  "buffer_load_dword v222, v220, s[44:47], s60 offen sc1\n\t" \
  "s_mov_b32 s60, 16367616\n\t" \
  "buffer_load_dword v223, v220, s[44:47], s60 offen sc1\n\t" \
  "s_mov_b32 s60, 16400384\n\t" \
  "buffer_load_dword v224, v220, s[44:47], s60 offen sc1\n\t" \
  "s_add_u32 s63, s63, 1\n\t" \
  "s_waitcnt vmcnt(0)\n\t" \
  "v_min3_u32 v225, v221, v222, v223\n\t" \
  "v_max3_u32 v226, v221, v222, v223\n\t" \
  "v_min_u32 v225, v225, v224\n\t" \
  "v_max_u32 v226, v226, v224\n\t" \
  "v_cmp_ne_u32 vcc, s74, v225\n\t" \
  "v_cmp_ne_u32 s[52:53], s74, v226\n\t" \
  "s_cmp_gt_u32 s63, 262144\n\t" \
  "s_cbranch_scc1 ST_EQ_%=\n\t" \
  "s_or_b64 vcc, vcc, s[52:53]\n\t" \
  "s_cbranch_vccnz ST_EP_%=\n\t" \
  "ST_EQ_%=:\n\t" \
  "v_mov_b32 v220, s74\n\t" \
  "v_mov_b32 v221, s74\n\t" \
  "v_mov_b32 v222, s74\n\t" \
  "v_mov_b32 v223, s74\n\t" \
  "s_mov_b32 s60, 16531456\n\t" \
  "s_mov_b64 exec, 0xff\n\t" \
  "buffer_store_dwordx4 v[220:223], v4, s[44:47], s60 offen sc1\n\t" \
  "s_mov_b64 exec, -1\n\t" \
  "s_nop 1\n\t" \
  "ST_D_%=:\n\t" \
  "s_waitcnt lgkmcnt(0)\n\t" \
  "s_barrier\n\t" \
  "ds_read_b128 v[220:223], v217 offset:18432\n\t" \
  "ds_read_b128 v[224:227], v217 offset:18448\n\t" \
  "ds_read_b128 v[228:231], v212 offset:16384\n\t" \
  "ds_read_b128 v[232:235], v212 offset:16400\n\t" \
  "s_waitcnt lgkmcnt(0)\n\t" \
  "v_pk_fma_f32 v[196:197], v[164:165], v[220:221], v[228:229] op_sel_hi:[0,1,1]\n\t" \
  "v_pk_fma_f32 v[198:199], v[166:167], v[222:223], v[230:231] op_sel_hi:[0,1,1]\n\t" \
  "v_pk_fma_f32 v[200:201], v[168:169], v[224:225], v[232:233] op_sel_hi:[0,1,1]\n\t" \
  "v_pk_fma_f32 v[202:203], v[170:171], v[226:227], v[234:235] op_sel_hi:[0,1,1]\n\t" \
  "v_pk_fma_f32 v[236:237], v[164:165], v[220:221], v[196:197] op_sel:[1,1,0] op_sel_hi:[1,0,1] neg_lo:[1,0,0]\n\t" \
  "v_pk_fma_f32 v[238:239], v[166:167], v[222:223], v[198:199] op_sel:[1,1,0] op_sel_hi:[1,0,1] neg_lo:[1,0,0]\n\t" \
  "v_pk_fma_f32 v[240:241], v[168:169], v[224:225], v[200:201] op_sel:[1,1,0] op_sel_hi:[1,0,1] neg_lo:[1,0,0]\n\t" \
  "v_pk_fma_f32 v[242:243], v[170:171], v[226:227], v[202:203] op_sel:[1,1,0] op_sel_hi:[1,0,1] neg_lo:[1,0,0]\n\t" \
  "v_pk_fma_f32 v[196:197], v[156:157], v[236:237], v[172:173] op_sel_hi:[0,1,1]\n\t" \
  "v_pk_fma_f32 v[198:199], v[158:159], v[238:239], v[174:175] op_sel_hi:[0,1,1]\n\t" \
  "v_pk_fma_f32 v[200:201], v[160:161], v[240:241], v[176:177] op_sel_hi:[0,1,1]\n\t" \
  "v_pk_fma_f32 v[202:203], v[162:163], v[242:243], v[178:179] op_sel_hi:[0,1,1]\n\t" \
  "v_pk_mul_f32 v[204:205], v[148:149], v[236:237] op_sel_hi:[0,1]\n\t" \
  "v_pk_mul_f32 v[206:207], v[150:151], v[238:239] op_sel_hi:[0,1]\n\t" \
  "v_pk_mul_f32 v[208:209], v[152:153], v[240:241] op_sel_hi:[0,1]\n\t" \
  "v_pk_mul_f32 v[210:211], v[154:155], v[242:243] op_sel_hi:[0,1]\n\t" \
  "v_pk_fma_f32 v[244:245], v[156:157], v[236:237], v[196:197] op_sel:[1,1,0] op_sel_hi:[1,0,1] neg_lo:[1,0,0]\n\t" \
  "v_pk_fma_f32 v[246:247], v[158:159], v[238:239], v[198:199] op_sel:[1,1,0] op_sel_hi:[1,0,1] neg_lo:[1,0,0]\n\t" \
  "v_pk_fma_f32 v[248:249], v[160:161], v[240:241], v[200:201] op_sel:[1,1,0] op_sel_hi:[1,0,1] neg_lo:[1,0,0]\n\t" \
  "v_pk_fma_f32 v[250:251], v[162:163], v[242:243], v[202:203] op_sel:[1,1,0] op_sel_hi:[1,0,1] neg_lo:[1,0,0]\n\t" \
  "v_pk_fma_f32 v[220:221], v[148:149], v[236:237], v[204:205] op_sel:[1,1,0] op_sel_hi:[1,0,1] neg_lo:[1,0,0]\n\t" \
  "v_pk_fma_f32 v[222:223], v[150:151], v[238:239], v[206:207] op_sel:[1,1,0] op_sel_hi:[1,0,1] neg_lo:[1,0,0]\n\t" \
  "v_pk_fma_f32 v[224:225], v[152:153], v[240:241], v[208:209] op_sel:[1,1,0] op_sel_hi:[1,0,1] neg_lo:[1,0,0]\n\t" \
  "v_pk_fma_f32 v[226:227], v[154:155], v[242:243], v[210:211] op_sel:[1,1,0] op_sel_hi:[1,0,1] neg_lo:[1,0,0]\n\t" \
  "v_pk_mul_f32 v[204:205], v[148:149], v[244:245] op_sel_hi:[0,1]\n\t" \
  "v_pk_mul_f32 v[206:207], v[150:151], v[246:247] op_sel_hi:[0,1]\n\t" \
  "v_pk_mul_f32 v[208:209], v[152:153], v[248:249] op_sel_hi:[0,1]\n\t" \
  "v_pk_mul_f32 v[210:211], v[154:155], v[250:251] op_sel_hi:[0,1]\n\t" \
  "v_mov_b32 v180, v220\n\t" \
  "v_mov_b32 v182, v222\n\t" \
  "v_mov_b32 v184, v224\n\t" \
  "v_mov_b32 v186, v226\n\t" \
  "v_pk_fma_f32 v[228:229], v[148:149], v[244:245], v[204:205] op_sel:[1,1,0] op_sel_hi:[1,0,1] neg_lo:[1,0,0]\n\t" \
  "v_pk_fma_f32 v[230:231], v[150:151], v[246:247], v[206:207] op_sel:[1,1,0] op_sel_hi:[1,0,1] neg_lo:[1,0,0]\n\t" \
  "v_pk_fma_f32 v[232:233], v[152:153], v[248:249], v[208:209] op_sel:[1,1,0] op_sel_hi:[1,0,1] neg_lo:[1,0,0]\n\t" \
  "v_pk_fma_f32 v[234:235], v[154:155], v[250:251], v[210:211] op_sel:[1,1,0] op_sel_hi:[1,0,1] neg_lo:[1,0,0]\n\t" \
  "v_xor_b32 v188, 0x80000000, v221\n\t" \
  "v_xor_b32 v190, 0x80000000, v223\n\t" \
  "v_xor_b32 v192, 0x80000000, v225\n\t" \
  "v_xor_b32 v194, 0x80000000, v227\n\t" \
  "v_mov_b32 v181, v228\n\t" \
  "v_xor_b32 v189, 0x80000000, v229\n\t" \
  "v_mov_b32 v183, v230\n\t" \
  "v_xor_b32 v191, 0x80000000, v231\n\t" \
  "v_mov_b32 v185, v232\n\t" \
  "v_xor_b32 v193, 0x80000000, v233\n\t" \
  "v_mov_b32 v187, v234\n\t" \
  "v_xor_b32 v195, 0x80000000, v235\n\t" \
  "v_pk_fma_f32 v[196:197], v[140:141], v[188:189], v[8:9] op_sel:[1,0,0]\n\t" \
  "v_pk_fma_f32 v[204:205], v[140:141], v[180:181], v[24:25] op_sel:[1,0,0] neg_lo:[1,0,0] neg_hi:[1,0,0]\n\t" \
  "v_pk_fma_f32 v[198:199], v[142:143], v[190:191], v[40:41] op_sel:[1,0,0]\n\t" \
  "v_pk_fma_f32 v[206:207], v[142:143], v[182:183], v[56:57] op_sel:[1,0,0] neg_lo:[1,0,0] neg_hi:[1,0,0]\n\t" \
  "v_pk_fma_f32 v[200:201], v[144:145], v[192:193], v[72:73] op_sel:[1,0,0]\n\t" \
  "v_pk_fma_f32 v[208:209], v[144:145], v[184:185], v[88:89] op_sel:[1,0,0] neg_lo:[1,0,0] neg_hi:[1,0,0]\n\t" \
  "v_pk_fma_f32 v[202:203], v[146:147], v[194:195], v[104:105] op_sel:[1,0,0]\n\t" \
  "v_pk_fma_f32 v[210:211], v[146:147], v[186:187], v[120:121] op_sel:[1,0,0] neg_lo:[1,0,0] neg_hi:[1,0,0]\n\t" \
  "v_xor_b32 v236, 0, v214\n\t" \
  "v_pk_fma_f32 v[180:181], v[140:141], v[180:181], v[196:197] op_sel_hi:[0,1,1]\n\t" \
  "v_pk_fma_f32 v[188:189], v[140:141], v[188:189], v[204:205] op_sel_hi:[0,1,1]\n\t" \
  "v_pk_fma_f32 v[182:183], v[142:143], v[182:183], v[198:199] op_sel_hi:[0,1,1]\n\t" \
  "v_pk_fma_f32 v[190:191], v[142:143], v[190:191], v[206:207] op_sel_hi:[0,1,1]\n\t" \
  "v_pk_fma_f32 v[184:185], v[144:145], v[184:185], v[200:201] op_sel_hi:[0,1,1]\n\t" \
  "v_pk_fma_f32 v[192:193], v[144:145], v[192:193], v[208:209] op_sel_hi:[0,1,1]\n\t" \
  "v_pk_fma_f32 v[186:187], v[146:147], v[186:187], v[202:203] op_sel_hi:[0,1,1]\n\t" \
  "v_pk_fma_f32 v[194:195], v[146:147], v[194:195], v[210:211] op_sel_hi:[0,1,1]\n\t" \
  "v_cvt_pk_bf16_f32 v220, v180, v182\n\t" \
  "v_cvt_pk_bf16_f32 v221, v184, v186\n\t" \
  "v_cvt_pk_bf16_f32 v222, v188, v190\n\t" \
  "v_cvt_pk_bf16_f32 v223, v192, v194\n\t" \
  "ds_write_b128 v236, v[220:223] offset:0\n\t" \
  "v_cvt_pk_bf16_f32 v224, v181, v183\n\t" \
  "v_cvt_pk_bf16_f32 v225, v185, v187\n\t" \
  "v_cvt_pk_bf16_f32 v226, v189, v191\n\t" \
  "v_cvt_pk_bf16_f32 v227, v193, v195\n\t" \
  "ds_write_b128 v236, v[224:227] offset:128\n\t" \
  "v_pk_fma_f32 v[196:197], v[140:141], v[188:189], v[10:11] op_sel:[1,0,0]\n\t" \
  "v_pk_fma_f32 v[204:205], v[140:141], v[180:181], v[26:27] op_sel:[1,0,0] neg_lo:[1,0,0] neg_hi:[1,0,0]\n\t" \
  "v_pk_fma_f32 v[198:199], v[142:143], v[190:191], v[42:43] op_sel:[1,0,0]\n\t" \
  "v_pk_fma_f32 v[206:207], v[142:143], v[182:183], v[58:59] op_sel:[1,0,0] neg_lo:[1,0,0] neg_hi:[1,0,0]\n\t" \
  "v_pk_fma_f32 v[200:201], v[144:145], v[192:193], v[74:75] op_sel:[1,0,0]\n\t" \
  "v_pk_fma_f32 v[208:209], v[144:145], v[184:185], v[90:91] op_sel:[1,0,0] neg_lo:[1,0,0] neg_hi:[1,0,0]\n\t" \
  "v_pk_fma_f32 v[202:203], v[146:147], v[194:195], v[106:107] op_sel:[1,0,0]\n\t" \
  "v_pk_fma_f32 v[210:211], v[146:147], v[186:187], v[122:123] op_sel:[1,0,0] neg_lo:[1,0,0] neg_hi:[1,0,0]\n\t" \
  "v_xor_b32 v237, 16, v214\n\t" \
  "v_pk_fma_f32 v[180:181], v[140:141], v[180:181], v[196:197] op_sel_hi:[0,1,1]\n\t" \
  "v_pk_fma_f32 v[188:189], v[140:141], v[188:189], v[204:205] op_sel_hi:[0,1,1]\n\t" \
  "v_pk_fma_f32 v[182:183], v[142:143], v[182:183], v[198:199] op_sel_hi:[0,1,1]\n\t" \
  "v_pk_fma_f32 v[190:191], v[142:143], v[190:191], v[206:207] op_sel_hi:[0,1,1]\n\t" \
  "v_pk_fma_f32 v[184:185], v[144:145], v[184:185], v[200:201] op_sel_hi:[0,1,1]\n\t" \
  "v_pk_fma_f32 v[192:193], v[144:145], v[192:193], v[208:209] op_sel_hi:[0,1,1]\n\t" \
  "v_pk_fma_f32 v[186:187], v[146:147], v[186:187], v[202:203] op_sel_hi:[0,1,1]\n\t" \
  "v_pk_fma_f32 v[194:195], v[146:147], v[194:195], v[210:211] op_sel_hi:[0,1,1]\n\t" \
  "v_cvt_pk_bf16_f32 v228, v180, v182\n\t" \
  "v_cvt_pk_bf16_f32 v229, v184, v186\n\t" \
  "v_cvt_pk_bf16_f32 v230, v188, v190\n\t" \
  "v_cvt_pk_bf16_f32 v231, v192, v194\n\t" \
  "ds_write_b128 v237, v[228:231] offset:0\n\t" \
  "v_cvt_pk_bf16_f32 v232, v181, v183\n\t" \
  "v_cvt_pk_bf16_f32 v233, v185, v187\n\t" \
  "v_cvt_pk_bf16_f32 v234, v189, v191\n\t" \
  "v_cvt_pk_bf16_f32 v235, v193, v195\n\t" \
  "ds_write_b128 v237, v[232:235] offset:128\n\t" \
  "v_pk_fma_f32 v[196:197], v[140:141], v[188:189], v[12:13] op_sel:[1,0,0]\n\t" \
  "v_pk_fma_f32 v[204:205], v[140:141], v[180:181], v[28:29] op_sel:[1,0,0] neg_lo:[1,0,0] neg_hi:[1,0,0]\n\t" \
  "v_pk_fma_f32 v[198:199], v[142:143], v[190:191], v[44:45] op_sel:[1,0,0]\n\t" \
  "v_pk_fma_f32 v[206:207], v[142:143], v[182:183], v[60:61] op_sel:[1,0,0] neg_lo:[1,0,0] neg_hi:[1,0,0]\n\t" \
  "v_pk_fma_f32 v[200:201], v[144:145], v[192:193], v[76:77] op_sel:[1,0,0]\n\t" \
  "v_pk_fma_f32 v[208:209], v[144:145], v[184:185], v[92:93] op_sel:[1,0,0] neg_lo:[1,0,0] neg_hi:[1,0,0]\n\t" \
  "v_pk_fma_f32 v[202:203], v[146:147], v[194:195], v[108:109] op_sel:[1,0,0]\n\t" \
  "v_pk_fma_f32 v[210:211], v[146:147], v[186:187], v[124:125] op_sel:[1,0,0] neg_lo:[1,0,0] neg_hi:[1,0,0]\n\t" \
  "v_xor_b32 v236, 32, v214\n\t" \
  "v_pk_fma_f32 v[180:181], v[140:141], v[180:181], v[196:197] op_sel_hi:[0,1,1]\n\t" \
  "v_pk_fma_f32 v[188:189], v[140:141], v[188:189], v[204:205] op_sel_hi:[0,1,1]\n\t" \
  "v_pk_fma_f32 v[182:183], v[142:143], v[182:183], v[198:199] op_sel_hi:[0,1,1]\n\t" \
  "v_pk_fma_f32 v[190:191], v[142:143], v[190:191], v[206:207] op_sel_hi:[0,1,1]\n\t" \
  "v_pk_fma_f32 v[184:185], v[144:145], v[184:185], v[200:201] op_sel_hi:[0,1,1]\n\t" \
  "v_pk_fma_f32 v[192:193], v[144:145], v[192:193], v[208:209] op_sel_hi:[0,1,1]\n\t" \
  "v_pk_fma_f32 v[186:187], v[146:147], v[186:187], v[202:203] op_sel_hi:[0,1,1]\n\t" \
  "v_pk_fma_f32 v[194:195], v[146:147], v[194:195], v[210:211] op_sel_hi:[0,1,1]\n\t" \
  "v_cvt_pk_bf16_f32 v220, v180, v182\n\t" \
  "v_cvt_pk_bf16_f32 v221, v184, v186\n\t" \
  "v_cvt_pk_bf16_f32 v222, v188, v190\n\t" \
  "v_cvt_pk_bf16_f32 v223, v192, v194\n\t" \
  "ds_write_b128 v236, v[220:223] offset:0\n\t" \
  "v_cvt_pk_bf16_f32 v224, v181, v183\n\t" \
  "v_cvt_pk_bf16_f32 v225, v185, v187\n\t" \
  "v_cvt_pk_bf16_f32 v226, v189, v191\n\t" \
  "v_cvt_pk_bf16_f32 v227, v193, v195\n\t" \
  "ds_write_b128 v236, v[224:227] offset:128\n\t" \
  "v_pk_fma_f32 v[196:197], v[140:141], v[188:189], v[14:15] op_sel:[1,0,0]\n\t" \
  "v_pk_fma_f32 v[204:205], v[140:141], v[180:181], v[30:31] op_sel:[1,0,0] neg_lo:[1,0,0] neg_hi:[1,0,0]\n\t" \
  "v_pk_fma_f32 v[198:199], v[142:143], v[190:191], v[46:47] op_sel:[1,0,0]\n\t" \
  "v_pk_fma_f32 v[206:207], v[142:143], v[182:183], v[62:63] op_sel:[1,0,0] neg_lo:[1,0,0] neg_hi:[1,0,0]\n\t" \
  "v_pk_fma_f32 v[200:201], v[144:145], v[192:193], v[78:79] op_sel:[1,0,0]\n\t" \
  "v_pk_fma_f32 v[208:209], v[144:145], v[184:185], v[94:95] op_sel:[1,0,0] neg_lo:[1,0,0] neg_hi:[1,0,0]\n\t" \
  "v_pk_fma_f32 v[202:203], v[146:147], v[194:195], v[110:111] op_sel:[1,0,0]\n\t" \
  "v_pk_fma_f32 v[210:211], v[146:147], v[186:187], v[126:127] op_sel:[1,0,0] neg_lo:[1,0,0] neg_hi:[1,0,0]\n\t" \
  "v_xor_b32 v237, 48, v214\n\t" \
  "v_pk_fma_f32 v[180:181], v[140:141], v[180:181], v[196:197] op_sel_hi:[0,1,1]\n\t" \
  "v_pk_fma_f32 v[188:189], v[140:141], v[188:189], v[204:205] op_sel_hi:[0,1,1]\n\t" \
  "v_pk_fma_f32 v[182:183], v[142:143], v[182:183], v[198:199] op_sel_hi:[0,1,1]\n\t" \
  "v_pk_fma_f32 v[190:191], v[142:143], v[190:191], v[206:207] op_sel_hi:[0,1,1]\n\t" \
  "v_pk_fma_f32 v[184:185], v[144:145], v[184:185], v[200:201] op_sel_hi:[0,1,1]\n\t" \
  "v_pk_fma_f32 v[192:193], v[144:145], v[192:193], v[208:209] op_sel_hi:[0,1,1]\n\t" \
  "v_pk_fma_f32 v[186:187], v[146:147], v[186:187], v[202:203] op_sel_hi:[0,1,1]\n\t" \
  "v_pk_fma_f32 v[194:195], v[146:147], v[194:195], v[210:211] op_sel_hi:[0,1,1]\n\t" \
  "v_cvt_pk_bf16_f32 v228, v180, v182\n\t" \
  "v_cvt_pk_bf16_f32 v229, v184, v186\n\t" \
  "v_cvt_pk_bf16_f32 v230, v188, v190\n\t" \
  "v_cvt_pk_bf16_f32 v231, v192, v194\n\t" \
  "ds_write_b128 v237, v[228:231] offset:0\n\t" \
  "v_cvt_pk_bf16_f32 v232, v181, v183\n\t" \
  "v_cvt_pk_bf16_f32 v233, v185, v187\n\t" \
  "v_cvt_pk_bf16_f32 v234, v189, v191\n\t" \
  "v_cvt_pk_bf16_f32 v235, v193, v195\n\t" \
  "ds_write_b128 v237, v[232:235] offset:128\n\t" \
  "v_pk_fma_f32 v[196:197], v[140:141], v[188:189], v[16:17] op_sel:[1,0,0]\n\t" \
  "v_pk_fma_f32 v[204:205], v[140:141], v[180:181], v[32:33] op_sel:[1,0,0] neg_lo:[1,0,0] neg_hi:[1,0,0]\n\t" \
  "v_pk_fma_f32 v[198:199], v[142:143], v[190:191], v[48:49] op_sel:[1,0,0]\n\t" \
  "v_pk_fma_f32 v[206:207], v[142:143], v[182:183], v[64:65] op_sel:[1,0,0] neg_lo:[1,0,0] neg_hi:[1,0,0]\n\t" \
  "v_pk_fma_f32 v[200:201], v[144:145], v[192:193], v[80:81] op_sel:[1,0,0]\n\t" \
  "v_pk_fma_f32 v[208:209], v[144:145], v[184:185], v[96:97] op_sel:[1,0,0] neg_lo:[1,0,0] neg_hi:[1,0,0]\n\t" \
  "v_pk_fma_f32 v[202:203], v[146:147], v[194:195], v[112:113] op_sel:[1,0,0]\n\t" \
  "v_pk_fma_f32 v[210:211], v[146:147], v[186:187], v[128:129] op_sel:[1,0,0] neg_lo:[1,0,0] neg_hi:[1,0,0]\n\t" \
  "v_xor_b32 v236, 64, v214\n\t" \
  "v_pk_fma_f32 v[180:181], v[140:141], v[180:181], v[196:197] op_sel_hi:[0,1,1]\n\t" \
  "v_pk_fma_f32 v[188:189], v[140:141], v[188:189], v[204:205] op_sel_hi:[0,1,1]\n\t" \
  "v_pk_fma_f32 v[182:183], v[142:143], v[182:183], v[198:199] op_sel_hi:[0,1,1]\n\t" \
  "v_pk_fma_f32 v[190:191], v[142:143], v[190:191], v[206:207] op_sel_hi:[0,1,1]\n\t" \
  "v_pk_fma_f32 v[184:185], v[144:145], v[184:185], v[200:201] op_sel_hi:[0,1,1]\n\t" \
  "v_pk_fma_f32 v[192:193], v[144:145], v[192:193], v[208:209] op_sel_hi:[0,1,1]\n\t" \
  "v_pk_fma_f32 v[186:187], v[146:147], v[186:187], v[202:203] op_sel_hi:[0,1,1]\n\t" \
  "v_pk_fma_f32 v[194:195], v[146:147], v[194:195], v[210:211] op_sel_hi:[0,1,1]\n\t" \
  "v_cvt_pk_bf16_f32 v220, v180, v182\n\t" \
  "v_cvt_pk_bf16_f32 v221, v184, v186\n\t" \
  "v_cvt_pk_bf16_f32 v222, v188, v190\n\t" \
  "v_cvt_pk_bf16_f32 v223, v192, v194\n\t" \
  "ds_write_b128 v236, v[220:223] offset:0\n\t" \
  "v_cvt_pk_bf16_f32 v224, v181, v183\n\t" \
  "v_cvt_pk_bf16_f32 v225, v185, v187\n\t" \
  "v_cvt_pk_bf16_f32 v226, v189, v191\n\t" \
  "v_cvt_pk_bf16_f32 v227, v193, v195\n\t" \
  "ds_write_b128 v236, v[224:227] offset:128\n\t" \
  "v_pk_fma_f32 v[196:197], v[140:141], v[188:189], v[18:19] op_sel:[1,0,0]\n\t" \
  "v_pk_fma_f32 v[204:205], v[140:141], v[180:181], v[34:35] op_sel:[1,0,0] neg_lo:[1,0,0] neg_hi:[1,0,0]\n\t" \
  "v_pk_fma_f32 v[198:199], v[142:143], v[190:191], v[50:51] op_sel:[1,0,0]\n\t" \
  "v_pk_fma_f32 v[206:207], v[142:143], v[182:183], v[66:67] op_sel:[1,0,0] neg_lo:[1,0,0] neg_hi:[1,0,0]\n\t" \
  "v_pk_fma_f32 v[200:201], v[144:145], v[192:193], v[82:83] op_sel:[1,0,0]\n\t" \
  "v_pk_fma_f32 v[208:209], v[144:145], v[184:185], v[98:99] op_sel:[1,0,0] neg_lo:[1,0,0] neg_hi:[1,0,0]\n\t" \
  "v_pk_fma_f32 v[202:203], v[146:147], v[194:195], v[114:115] op_sel:[1,0,0]\n\t" \
  "v_pk_fma_f32 v[210:211], v[146:147], v[186:187], v[130:131] op_sel:[1,0,0] neg_lo:[1,0,0] neg_hi:[1,0,0]\n\t" \
  "v_xor_b32 v237, 80, v214\n\t" \
  "v_pk_fma_f32 v[180:181], v[140:141], v[180:181], v[196:197] op_sel_hi:[0,1,1]\n\t" \
  "v_pk_fma_f32 v[188:189], v[140:141], v[188:189], v[204:205] op_sel_hi:[0,1,1]\n\t" \
  "v_pk_fma_f32 v[182:183], v[142:143], v[182:183], v[198:199] op_sel_hi:[0,1,1]\n\t" \
  "v_pk_fma_f32 v[190:191], v[142:143], v[190:191], v[206:207] op_sel_hi:[0,1,1]\n\t" \
  "v_pk_fma_f32 v[184:185], v[144:145], v[184:185], v[200:201] op_sel_hi:[0,1,1]\n\t" \
  "v_pk_fma_f32 v[192:193], v[144:145], v[192:193], v[208:209] op_sel_hi:[0,1,1]\n\t" \
  "v_pk_fma_f32 v[186:187], v[146:147], v[186:187], v[202:203] op_sel_hi:[0,1,1]\n\t" \
  "v_pk_fma_f32 v[194:195], v[146:147], v[194:195], v[210:211] op_sel_hi:[0,1,1]\n\t" \
  "v_cvt_pk_bf16_f32 v228, v180, v182\n\t" \
  "v_cvt_pk_bf16_f32 v229, v184, v186\n\t" \
  "v_cvt_pk_bf16_f32 v230, v188, v190\n\t" \
  "v_cvt_pk_bf16_f32 v231, v192, v194\n\t" \
  "ds_write_b128 v237, v[228:231] offset:0\n\t" \
  "v_cvt_pk_bf16_f32 v232, v181, v183\n\t" \
  "v_cvt_pk_bf16_f32 v233, v185, v187\n\t" \
  "v_cvt_pk_bf16_f32 v234, v189, v191\n\t" \
  "v_cvt_pk_bf16_f32 v235, v193, v195\n\t" \
  "ds_write_b128 v237, v[232:235] offset:128\n\t" \
  "v_pk_fma_f32 v[196:197], v[140:141], v[188:189], v[20:21] op_sel:[1,0,0]\n\t" \
  "v_pk_fma_f32 v[204:205], v[140:141], v[180:181], v[36:37] op_sel:[1,0,0] neg_lo:[1,0,0] neg_hi:[1,0,0]\n\t" \
  "v_pk_fma_f32 v[198:199], v[142:143], v[190:191], v[52:53] op_sel:[1,0,0]\n\t" \
  "v_pk_fma_f32 v[206:207], v[142:143], v[182:183], v[68:69] op_sel:[1,0,0] neg_lo:[1,0,0] neg_hi:[1,0,0]\n\t" \
  "v_pk_fma_f32 v[200:201], v[144:145], v[192:193], v[84:85] op_sel:[1,0,0]\n\t" \
  "v_pk_fma_f32 v[208:209], v[144:145], v[184:185], v[100:101] op_sel:[1,0,0] neg_lo:[1,0,0] neg_hi:[1,0,0]\n\t" \
  "v_pk_fma_f32 v[202:203], v[146:147], v[194:195], v[116:117] op_sel:[1,0,0]\n\t" \
  "v_pk_fma_f32 v[210:211], v[146:147], v[186:187], v[132:133] op_sel:[1,0,0] neg_lo:[1,0,0] neg_hi:[1,0,0]\n\t" \
  "v_xor_b32 v236, 96, v214\n\t" \
  "v_pk_fma_f32 v[180:181], v[140:141], v[180:181], v[196:197] op_sel_hi:[0,1,1]\n\t" \
  "v_pk_fma_f32 v[188:189], v[140:141], v[188:189], v[204:205] op_sel_hi:[0,1,1]\n\t" \
  "v_pk_fma_f32 v[182:183], v[142:143], v[182:183], v[198:199] op_sel_hi:[0,1,1]\n\t" \
  "v_pk_fma_f32 v[190:191], v[142:143], v[190:191], v[206:207] op_sel_hi:[0,1,1]\n\t" \
  "v_pk_fma_f32 v[184:185], v[144:145], v[184:185], v[200:201] op_sel_hi:[0,1,1]\n\t" \
  "v_pk_fma_f32 v[192:193], v[144:145], v[192:193], v[208:209] op_sel_hi:[0,1,1]\n\t" \
  "v_pk_fma_f32 v[186:187], v[146:147], v[186:187], v[202:203] op_sel_hi:[0,1,1]\n\t" \
  "v_pk_fma_f32 v[194:195], v[146:147], v[194:195], v[210:211] op_sel_hi:[0,1,1]\n\t" \
  "v_cvt_pk_bf16_f32 v220, v180, v182\n\t" \
  "v_cvt_pk_bf16_f32 v221, v184, v186\n\t" \
  "v_cvt_pk_bf16_f32 v222, v188, v190\n\t" \
  "v_cvt_pk_bf16_f32 v223, v192, v194\n\t" \
  "ds_write_b128 v236, v[220:223] offset:0\n\t" \
  "v_cvt_pk_bf16_f32 v224, v181, v183\n\t" \
  "v_cvt_pk_bf16_f32 v225, v185, v187\n\t" \
  "v_cvt_pk_bf16_f32 v226, v189, v191\n\t" \
  "v_cvt_pk_bf16_f32 v227, v193, v195\n\t" \
  "ds_write_b128 v236, v[224:227] offset:128\n\t" \
  "v_pk_fma_f32 v[196:197], v[140:141], v[188:189], v[22:23] op_sel:[1,0,0]\n\t" \
  "v_pk_fma_f32 v[204:205], v[140:141], v[180:181], v[38:39] op_sel:[1,0,0] neg_lo:[1,0,0] neg_hi:[1,0,0]\n\t" \
  "v_pk_fma_f32 v[198:199], v[142:143], v[190:191], v[54:55] op_sel:[1,0,0]\n\t" \
  "v_pk_fma_f32 v[206:207], v[142:143], v[182:183], v[70:71] op_sel:[1,0,0] neg_lo:[1,0,0] neg_hi:[1,0,0]\n\t" \
  "v_pk_fma_f32 v[200:201], v[144:145], v[192:193], v[86:87] op_sel:[1,0,0]\n\t" \
  "v_pk_fma_f32 v[208:209], v[144:145], v[184:185], v[102:103] op_sel:[1,0,0] neg_lo:[1,0,0] neg_hi:[1,0,0]\n\t" \
  "v_pk_fma_f32 v[202:203], v[146:147], v[194:195], v[118:119] op_sel:[1,0,0]\n\t" \
  "v_pk_fma_f32 v[210:211], v[146:147], v[186:187], v[134:135] op_sel:[1,0,0] neg_lo:[1,0,0] neg_hi:[1,0,0]\n\t" \
  "v_xor_b32 v237, 112, v214\n\t" \
  "v_pk_fma_f32 v[180:181], v[140:141], v[180:181], v[196:197] op_sel_hi:[0,1,1]\n\t" \
  "v_pk_fma_f32 v[188:189], v[140:141], v[188:189], v[204:205] op_sel_hi:[0,1,1]\n\t" \
  "v_pk_fma_f32 v[182:183], v[142:143], v[182:183], v[198:199] op_sel_hi:[0,1,1]\n\t" \
  "v_pk_fma_f32 v[190:191], v[142:143], v[190:191], v[206:207] op_sel_hi:[0,1,1]\n\t" \
  "v_pk_fma_f32 v[184:185], v[144:145], v[184:185], v[200:201] op_sel_hi:[0,1,1]\n\t" \
  "v_pk_fma_f32 v[192:193], v[144:145], v[192:193], v[208:209] op_sel_hi:[0,1,1]\n\t" \
  "v_pk_fma_f32 v[186:187], v[146:147], v[186:187], v[202:203] op_sel_hi:[0,1,1]\n\t" \
  "v_pk_fma_f32 v[194:195], v[146:147], v[194:195], v[210:211] op_sel_hi:[0,1,1]\n\t" \
  "v_cvt_pk_bf16_f32 v228, v180, v182\n\t" \
  "v_cvt_pk_bf16_f32 v229, v184, v186\n\t" \
  "v_cvt_pk_bf16_f32 v230, v188, v190\n\t" \
  "v_cvt_pk_bf16_f32 v231, v192, v194\n\t" \
  "ds_write_b128 v237, v[228:231] offset:0\n\t" \
  "v_cvt_pk_bf16_f32 v232, v181, v183\n\t" \
  "v_cvt_pk_bf16_f32 v233, v185, v187\n\t" \
  "v_cvt_pk_bf16_f32 v234, v189, v191\n\t" \
  "v_cvt_pk_bf16_f32 v235, v193, v195\n\t" \
  "ds_write_b128 v237, v[232:235] offset:128\n\t" \
  "s_waitcnt lgkmcnt(0)\n\t" \
  "s_barrier\n\t" \
  "ds_read_b128 v[8:11], v215 offset:0\n\t" \
  "ds_read_b128 v[12:15], v215 offset:1024\n\t" \
  "ds_read_b128 v[16:19], v215 offset:2048\n\t" \
  "ds_read_b128 v[20:23], v215 offset:3072\n\t" \
  "ds_read_b128 v[24:27], v216 offset:4096\n\t" \
  "ds_read_b128 v[28:31], v216 offset:5120\n\t" \
  "ds_read_b128 v[32:35], v216 offset:6144\n\t" \
  "ds_read_b128 v[36:39], v216 offset:7168\n\t" \
  "s_mov_b32 s60, s82\n\t" \
  "ds_read_b128 v[40:43], v215 offset:8192\n\t" \
  "s_waitcnt lgkmcnt(8)\n\t" \
  "buffer_store_dwordx4 v[8:11], v4, s[44:47], s60 offen offset:0 sc1\n\t" \
  "ds_read_b128 v[44:47], v215 offset:9216\n\t" \
  "s_waitcnt lgkmcnt(8)\n\t" \
  "buffer_store_dwordx4 v[12:15], v4, s[44:47], s60 offen offset:1024 sc1\n\t" \
  "ds_read_b128 v[48:51], v215 offset:10240\n\t" \
  "s_waitcnt lgkmcnt(8)\n\t" \
  "buffer_store_dwordx4 v[16:19], v4, s[44:47], s60 offen offset:2048 sc1\n\t" \
  "ds_read_b128 v[52:55], v215 offset:11264\n\t" \
  "s_waitcnt lgkmcnt(8)\n\t" \
  "buffer_store_dwordx4 v[20:23], v4, s[44:47], s60 offen offset:3072 sc1\n\t" \
  "s_add_u32 s60, s60, 4096\n\t" \
  "ds_read_b128 v[56:59], v216 offset:12288\n\t" \
  "s_waitcnt lgkmcnt(8)\n\t" \
  "buffer_store_dwordx4 v[24:27], v4, s[44:47], s60 offen offset:0 sc1\n\t" \
  "ds_read_b128 v[60:63], v216 offset:13312\n\t" \
  "s_waitcnt lgkmcnt(8)\n\t" \
  "buffer_store_dwordx4 v[28:31], v4, s[44:47], s60 offen offset:1024 sc1\n\t" \
  "ds_read_b128 v[64:67], v216 offset:14336\n\t" \
  "s_waitcnt lgkmcnt(8)\n\t" \
  "buffer_store_dwordx4 v[32:35], v4, s[44:47], s60 offen offset:2048 sc1\n\t" \
  "ds_read_b128 v[68:71], v216 offset:15360\n\t" \
  "s_waitcnt lgkmcnt(8)\n\t" \
  "buffer_store_dwordx4 v[36:39], v4, s[44:47], s60 offen offset:3072 sc1\n\t" \
  "s_add_u32 s60, s60, 4096\n\t" \
  "s_waitcnt lgkmcnt(7)\n\t" \
  "buffer_store_dwordx4 v[40:43], v4, s[44:47], s60 offen offset:0 sc1\n\t" \
  "s_waitcnt lgkmcnt(6)\n\t" \
  "buffer_store_dwordx4 v[44:47], v4, s[44:47], s60 offen offset:1024 sc1\n\t" \
  "s_waitcnt lgkmcnt(5)\n\t" \
  "buffer_store_dwordx4 v[48:51], v4, s[44:47], s60 offen offset:2048 sc1\n\t" \
  "s_waitcnt lgkmcnt(4)\n\t" \
  "buffer_store_dwordx4 v[52:55], v4, s[44:47], s60 offen offset:3072 sc1\n\t" \
  "s_add_u32 s60, s60, 4096\n\t" \
  "s_waitcnt lgkmcnt(3)\n\t" \
  "buffer_store_dwordx4 v[56:59], v4, s[44:47], s60 offen offset:0 sc1\n\t" \
  "s_waitcnt lgkmcnt(2)\n\t" \
  "buffer_store_dwordx4 v[60:63], v4, s[44:47], s60 offen offset:1024 sc1\n\t" \
  "s_waitcnt lgkmcnt(1)\n\t" \
  "buffer_store_dwordx4 v[64:67], v4, s[44:47], s60 offen offset:2048 sc1\n\t" \
  "s_waitcnt lgkmcnt(0)\n\t" \
  "buffer_store_dwordx4 v[68:71], v4, s[44:47], s60 offen offset:3072 sc1\n\t" \
  "s_cmp_lg_u32 s78, 0\n\t" \
  "s_cbranch_scc1 ST_E_%=\n\t" \
  "s_add_u32 s61, s73, 81920\n\t" \
  "buffer_load_dwordx4 a[208:211], v4, s[44:47], s61 offen offset:2048 sc1\n\t" \
  "buffer_load_dwordx4 a[212:215], v4, s[44:47], s61 offen offset:3072 sc1\n\t" \
  "buffer_load_dwordx4 a[216:219], v5, s[44:47], s61 offen offset:0 sc1\n\t" \
  "s_add_u32 s61, s73, 122880\n\t" \
  "buffer_load_dwordx4 a[220:223], v4, s[44:47], s61 offen offset:0 sc1\n\t" \
  "buffer_load_dwordx4 a[224:227], v4, s[44:47], s61 offen offset:1024 sc1\n\t" \
  "buffer_load_dwordx4 a[228:231], v4, s[44:47], s61 offen offset:2048 sc1\n\t" \
  "buffer_load_dwordx4 a[232:235], v4, s[44:47], s61 offen offset:3072 sc1\n\t" \
  "buffer_load_dwordx4 a[236:239], v5, s[44:47], s61 offen offset:0 sc1\n\t" \
  "ST_E_%=:\n\t"
#define P1_FFN_CLOB "v1", "v3", "v4", "v5", "v6", "v7", "v8", "v9", "v10", "v11", "v12", "v13", "v14", "v15", "v16", "v17", "v18", "v19", "v20", "v21", "v22", "v23", "v24", "v25", "v26", "v27", "v28", "v29", "v30", "v31", "v32", "v33", "v34", "v35", "v36", "v37", "v38", "v39", "v40", "v41", "v42", "v43", "v44", "v45", "v46", "v47", "v48", "v49", "v50", "v51", "v52", "v53", "v54", "v55", "v56", "v57", "v58", "v59", "v60", "v61", "v62", "v63", "v64", "v65", "v66", "v67", "v68", "v69", "v70", "v71", "v72", "v73", "v74", "v75", "v76", "v77", "v78", "v79", "v80", "v81", "v82", "v83", "v84", "v85", "v86", "v87", "v88", "v89", "v90", "v91", "v92", "v93", "v94", "v95", "v96", "v97", "v98", "v99", "v100", "v101", "v102", "v103", "v104", "v105", "v106", "v107", "v108", "v109", "v110", "v111", "v112", "v113", "v114", "v115", "v116", "v117", "v118", "v119", "v120", "v121", "v122", "v123", "v124", "v125", "v126", "v127", "v128", "v129", "v130", "v131", "v132", "v133", "v134", "v135", "v136", "v137", "v138", "v139", "v140", "v141", "v142", "v143", "v144", "v145", "v146", "v147", "v148", "v149", "v150", "v151", "v152", "v153", "v154", "v155", "v156", "v157", "v158", "v159", "v160", "v161", "v162", "v163", "v164", "v165", "v166", "v167", "v168", "v169", "v170", "v171", "v172", "v173", "v174", "v175", "v176", "v177", "v178", "v179", "v180", "v181", "v182", "v183", "v184", "v185", "v186", "v187", "v188", "v189", "v190", "v191", "v192", "v193", "v194", "v195", "v196", "v197", "v198", "v199", "v200", "v201", "v202", "v203", "v204", "v205", "v206", "v207", "v208", "v209", "v210", "v211", "v212", "v213", "v214", "v215", "v216", "v217", "v218", "v219", "v220", "v221", "v222", "v223", "v224", "v225", "v226", "v227", "v228", "v229", "v230", "v231", "v232", "v233", "v234", "v235", "v236", "v237", "v238", "v239", "v240", "v241", "v242", "v243", "v244", "v245", "v246", "v247", "v248", "v249", "v250", "v251", "v252", "v253", "v254", "v255", "s4", "s5", "s6", "s7", "s8", "s9", "s10", "s11", "s12", "s13", "s14", "s15", "s16", "s17", "s18", "s19", "s20", "s21", "s22", "s23", "s24", "s25", "s26", "s27", "s28", "s29", "s30", "s31", "s35", "s36", "s37", "s38", "s39", "s42", "s43", "s44", "s45", "s46", "s47", "s48", "s49", "s50", "s51", "s52", "s53", "s54", "s55", "s56", "s57", "s58", "s59", "s60", "s61", "s62", "s63", "s64", "s65", "s66", "s67", "s68", "s69", "s70", "s71", "s72", "s73", "s75", "s76", "s77", "s78", "s79", "s80", "s81", "s82", "s83", "s84", "s85", "s86", "s87", "s88", "s89", "s90", "s91", "s92", "s93", "s94", "s95", "s96", "s97", "a0", "a1", "a2", "a3", "a4", "a5", "a6", "a7", "a8", "a9", "a10", "a11", "a12", "a13", "a14", "a15", "a16", "a17", "a18", "a19", "a20", "a21", "a22", "a23", "a24", "a25", "a26", "a27", "a28", "a29", "a30", "a31", "a32", "a33", "a34", "a35", "a36", "a37", "a38", "a39", "a40", "a41", "a42", "a43", "a44", "a45", "a46", "a47", "a48", "a49", "a50", "a51", "a52", "a53", "a54", "a55", "a56", "a57", "a58", "a59", "a60", "a61", "a62", "a63", "a64", "a65", "a66", "a67", "a68", "a69", "a70", "a71", "a72", "a73", "a74", "a75", "a76", "a77", "a78", "a79", "a80", "a81", "a82", "a83", "a84", "a85", "a86", "a87", "a88", "a89", "a90", "a91", "a92", "a93", "a94", "a95", "a96", "a97", "a98", "a99", "a100", "a101", "a102", "a103", "a104", "a105", "a106", "a107", "a108", "a109", "a110", "a111", "a112", "a113", "a114", "a115", "a116", "a117", "a118", "a119", "a120", "a121", "a122", "a123", "a124", "a125", "a126", "a127", "a128", "a129", "a130", "a131", "a132", "a133", "a134", "a135", "a136", "a137", "a138", "a139", "a140", "a141", "a142", "a143", "a144", "a145", "a146", "a147", "a148", "a149", "a150", "a151", "a152", "a153", "a154", "a155", "a156", "a157", "a158", "a159", "a160", "a161", "a162", "a163", "a164", "a165", "a166", "a167", "a168", "a169", "a170", "a171", "a172", "a173", "a174", "a175", "a176", "a177", "a178", "a179", "a180", "a181", "a182", "a183", "a184", "a185", "a186", "a187", "a188", "a189", "a190", "a191", "a192", "a193", "a194", "a195", "a196", "a197", "a198", "a199", "a200", "a201", "a202", "a203", "a204", "a205", "a206", "a207", "a208", "a209", "a210", "a211", "a212", "a213", "a214", "a215", "a216", "a217", "a218", "a219", "a220", "a221", "a222", "a223", "a224", "a225", "a226", "a227", "a228", "a229", "a230", "a231", "a232", "a233", "a234", "a235", "a236", "a237", "a238", "a239", "a240", "a241", "a242", "a243", "a244", "a245", "a246", "a247", "a248", "a249", "a250", "a251", "a252", "a253", "vcc", "scc", "memory"
#define P1_START_CLOB "v1", "v8", "v9", "v10", "v11", "v12", "v13", "v14", "v15", "s4", "s5", "s6", "s7", "s8", "s9", "s10", "s11", "s12", "s13", "s14", "s15", "s16", "s17", "s18", "s19", "s20", "s21", "s22", "s23", "s24", "s25", "s26", "s27", "s28", "s29", "s30", "s31", "s56", "s57", "s58", "s59", "s60", "s61", "s62", "s63", "a240", "a241", "a242", "a243", "a244", "a245", "a246", "a247", "a248", "a249", "a250", "a251", "a252", "a253", "vcc", "scc", "memory"
#define P1_SSM_CLOB "v8", "v9", "v10", "v11", "v12", "v13", "v14", "v15", "v16", "v17", "v18", "v19", "v20", "v21", "v22", "v23", "v24", "v25", "v26", "v27", "v28", "v29", "v30", "v31", "v32", "v33", "v34", "v35", "v36", "v37", "v38", "v39", "v40", "v41", "v42", "v43", "v44", "v45", "v46", "v47", "v48", "v49", "v50", "v51", "v52", "v53", "v54", "v55", "v56", "v57", "v58", "v59", "v60", "v61", "v62", "v63", "v64", "v65", "v66", "v67", "v68", "v69", "v70", "v71", "v72", "v73", "v74", "v75", "v76", "v77", "v78", "v79", "v80", "v81", "v82", "v83", "v84", "v85", "v86", "v87", "v88", "v89", "v90", "v91", "v92", "v93", "v94", "v95", "v96", "v97", "v98", "v99", "v100", "v101", "v102", "v103", "v104", "v105", "v106", "v107", "v108", "v109", "v110", "v111", "v112", "v113", "v114", "v115", "v116", "v117", "v118", "v119", "v120", "v121", "v122", "v123", "v124", "v125", "v126", "v127", "v128", "v129", "v130", "v131", "v132", "v133", "v134", "v135", "v156", "v157", "v158", "v159", "v160", "v161", "v162", "v163", "v164", "v165", "v166", "v167", "v168", "v169", "v170", "v171", "v172", "v173", "v174", "v175", "v176", "v177", "v178", "v179", "v180", "v181", "v182", "v183", "v184", "v185", "v186", "v187", "v188", "v189", "v190", "v191", "v192", "v193", "v194", "v195", "v196", "v197", "v198", "v199", "v200", "v201", "v202", "v203", "v204", "v205", "v206", "v207", "v208", "v209", "v210", "v211", "v212", "v213", "v214", "v215", "v216", "v217", "v218", "v219", "v220", "v221", "v222", "v223", "v224", "v225", "v226", "v227", "v228", "v229", "v230", "v231", "v232", "v233", "v234", "v235", "v236", "v237", "v238", "v239", "s56", "s57", "s58", "s59", "s60", "s61", "s62", "s63", "a0", "a1", "a2", "a3", "a4", "a5", "a6", "a7", "a8", "a9", "a10", "a11", "a12", "a13", "a14", "a15", "a16", "a17", "a18", "a19", "a20", "a21", "a22", "a23", "a24", "a25", "a26", "a27", "a28", "a29", "a30", "a31", "a32", "a33", "a34", "a35", "a36", "a37", "a38", "a39", "a40", "a41", "a42", "a43", "a44", "a45", "a46", "a47", "a48", "a49", "a50", "a51", "a52", "a53", "a54", "a55", "a56", "a57", "a58", "a59", "a60", "a61", "a62", "a63", "a64", "a65", "a66", "a67", "a68", "a69", "a70", "a71", "a72", "a73", "a74", "a75", "a76", "a77", "a78", "a79", "a80", "a81", "a82", "a83", "a84", "a85", "a86", "a87", "a88", "a89", "a90", "a91", "a92", "a93", "a94", "a95", "a96", "a97", "a98", "a99", "a100", "a101", "a102", "a103", "a104", "a105", "a106", "a107", "a108", "a109", "a110", "a111", "a112", "a113", "a114", "a115", "a116", "a117", "a118", "a119", "a120", "a121", "a122", "a123", "a124", "a125", "a126", "a127", "a128", "a129", "a130", "a131", "a132", "a133", "a134", "a135", "a136", "a137", "a138", "a139", "a140", "a141", "a142", "a143", "a144", "a145", "a146", "a147", "a148", "a149", "a150", "a151", "a152", "a153", "a154", "a155", "a156", "a157", "a158", "a159", "a160", "a161", "a162", "a163", "a164", "a165", "a166", "a167", "a168", "a169", "a170", "a171", "a172", "a173", "a174", "a175", "a176", "a177", "a178", "a179", "a180", "a181", "a182", "a183", "a184", "a185", "a186", "a187", "a188", "a189", "a190", "a191", "a192", "a193", "a194", "a195", "a196", "a197", "a198", "a199", "a200", "a201", "a202", "a203", "a204", "a205", "a206", "a207", "a208", "a209", "a210", "a211", "a212", "a213", "a214", "a215", "a216", "a217", "a218", "a219", "a220", "a221", "a222", "a223", "a224", "a225", "a226", "a227", "a228", "a229", "a230", "a231", "a232", "a233", "a234", "a235", "a236", "a237", "a238", "a239", "a240", "a241", "a242", "a243", "a244", "a245", "a246", "a247", "a248", "a249", "a250", "a251", "a252", "a253", "vcc", "scc", "memory"
#define P2_MAIN \
  "s_nop 4\n\t" \
  "v_readlane_b32 s76, v2, 9\n\t" \
  "v_readlane_b32 s77, v2, 10\n\t" \
  "s_mov_b32 s72, s74\n\t" \
  "s_not_b32 s75, s74\n\t" \
  "s_mov_b64 s[44:45], s[40:41]\n\t" \
  "s_mov_b32 s46, 16531712\n\t" \
  "s_mov_b32 s47, 0x20000\n\t" \
  "v_mbcnt_lo_u32_b32 v255, -1, 0\n\t" \
  "v_mbcnt_hi_u32_b32 v255, -1, v255\n\t" \
  "v_mov_b32 v252, s75\n\t" \
  "v_min_u32 v250, s76, v255\n\t" \
  "v_lshlrev_b32 v250, 7, v250\n\t" \
  "v_add_u32 v250, s77, v250\n\t" \
  "buffer_load_dword v252, v250, s[44:47], 0 offen sc1\n\t" \
  "v_readlane_b32 s64, v2, 0\n\t" \
  "v_readlane_b32 s65, v2, 1\n\t" \
  "v_readlane_b32 s66, v2, 2\n\t" \
  "v_readlane_b32 s67, v2, 3\n\t" \
  "v_readlane_b32 s68, v2, 4\n\t" \
  "v_readlane_b32 s69, v2, 5\n\t" \
  "v_readlane_b32 s70, v2, 6\n\t" \
  "v_readlane_b32 s71, v2, 7\n\t" \
  "v_readlane_b32 s73, v2, 8\n\t" \
  "v_readlane_b32 s78, v2, 11\n\t" \
  "v_readlane_b32 s79, v2, 12\n\t" \
  "v_readlane_b32 s80, v2, 13\n\t" \
  "v_readlane_b32 s81, v2, 14\n\t" \
  "v_readlane_b32 s82, v2, 15\n\t" \
  "v_readlane_b32 s83, v2, 16\n\t" \
  "v_readlane_b32 s84, v2, 17\n\t" \
  "v_readlane_b32 s85, v2, 29\n\t" \
  "v_readlane_b32 s86, v2, 18\n\t" \
  "v_readlane_b32 s87, v2, 19\n\t" \
  "v_readlane_b32 s88, v2, 20\n\t" \
  "v_readlane_b32 s89, v2, 21\n\t" \
  "s_mov_b64 s[48:49], s[40:41]\n\t" \
  "s_mov_b32 s50, 0\n\t" \
  "s_mov_b32 s51, 0x20000\n\t" \
  "v_lshlrev_b32 v248, 4, v255\n\t" \
  "v_add_u32 v249, s65, v248\n\t" \
  "v_add_u32 v7, s84, v248\n\t" \
  "v_min_u32 v251, s78, v255\n\t" \
  "v_lshlrev_b32 v251, 7, v251\n\t" \
  "v_add_u32 v251, s79, v251\n\t" \
  "v_and_b32 v255, 15, v255\n\t" \
  "v_lshlrev_b32 v255, 3, v255\n\t" \
  "v_lshrrev_b32 v254, 8, v248\n\t" \
  "v_lshlrev_b32 v254, 3, v254\n\t" \
  "v_and_b32 v253, 0xf0, v248\n\t" \
  "v_mul_u32_u24 v253, 80, v253\n\t" \
  "v_add_u32 v253, v253, v254\n\t" \
  "s_add_u32 s52, s64, 0\n\t" \
  "s_and_b32 s52, s52, 3\n\t" \
  "s_lshl_b32 s52, s52, 10\n\t" \
  "s_add_u32 s53, s64, 1\n\t" \
  "s_and_b32 s53, s53, 3\n\t" \
  "s_lshl_b32 s53, s53, 10\n\t" \
  "s_add_u32 s54, s64, 2\n\t" \
  "s_and_b32 s54, s54, 3\n\t" \
  "s_lshl_b32 s54, s54, 10\n\t" \
  "s_add_u32 s55, s64, 3\n\t" \
  "s_and_b32 s55, s55, 3\n\t" \
  "s_lshl_b32 s55, s55, 10\n\t" \
  "s_mov_b32 s62, s84\n\t" \
  "s_mov_b32 s63, 0\n\t" \
  "s_bitcmp0_b32 s66, 0\n\t" \
  "s_cbranch_scc0 P2_NP_%=\n\t" \
  "v_add_u32 v89, 4096, v248\n\t" \
  "s_add_u32 s61, s82, 40960\n\t" \
  "buffer_load_dwordx4 a[192:195], v248, s[44:47], s61 offen offset:3072 sc1\n\t" \
  "buffer_load_dwordx4 a[196:199], v89, s[44:47], s61 offen offset:0 sc1\n\t" \
  "s_add_u32 s61, s82, 81920\n\t" \
  "buffer_load_dwordx4 a[200:203], v248, s[44:47], s61 offen offset:0 sc1\n\t" \
  "buffer_load_dwordx4 a[204:207], v248, s[44:47], s61 offen offset:1024 sc1\n\t" \
  "buffer_load_dwordx4 a[208:211], v248, s[44:47], s61 offen offset:2048 sc1\n\t" \
  "buffer_load_dwordx4 a[212:215], v248, s[44:47], s61 offen offset:3072 sc1\n\t" \
  "buffer_load_dwordx4 a[216:219], v89, s[44:47], s61 offen offset:0 sc1\n\t" \
  "s_add_u32 s61, s82, 122880\n\t" \
  "buffer_load_dwordx4 a[220:223], v248, s[44:47], s61 offen offset:0 sc1\n\t" \
  "buffer_load_dwordx4 a[224:227], v248, s[44:47], s61 offen offset:1024 sc1\n\t" \
  "buffer_load_dwordx4 a[228:231], v248, s[44:47], s61 offen offset:2048 sc1\n\t" \
  "buffer_load_dwordx4 a[232:235], v248, s[44:47], s61 offen offset:3072 sc1\n\t" \
  "buffer_load_dwordx4 a[236:239], v89, s[44:47], s61 offen offset:0 sc1\n\t" \
  "P2_NP_%=:\n\t" \
  "buffer_load_dword v252, v250, s[44:47], 0 offen sc1\n\t" \
  "s_add_u32 m0, s65, 0\n\t" \
  "s_add_u32 s61, s67, 0\n\t" \
  "buffer_load_dwordx4 v248, s[44:47], s61 offen offset:0 sc1 lds\n\t" \
  "buffer_load_dwordx4 v248, s[44:47], s61 offen offset:1024 sc1 lds\n\t" \
  "buffer_load_dwordx4 v248, s[44:47], s61 offen offset:2048 sc1 lds\n\t" \
  "buffer_load_dwordx4 v248, s[44:47], s61 offen offset:3072 sc1 lds\n\t" \
  "s_add_u32 m0, s65, 4096\n\t" \
  "s_add_u32 s61, s67, 4096\n\t" \
  "buffer_load_dwordx4 v248, s[44:47], s61 offen offset:0 sc1 lds\n\t" \
  "buffer_load_dword v252, v250, s[44:47], 0 offen sc1\n\t" \
  "s_add_u32 m0, s65, 5120\n\t" \
  "s_add_u32 s61, s67, 40960\n\t" \
  "buffer_load_dwordx4 v248, s[44:47], s61 offen offset:0 sc1 lds\n\t" \
  "buffer_load_dwordx4 v248, s[44:47], s61 offen offset:1024 sc1 lds\n\t" \
  "buffer_load_dwordx4 v248, s[44:47], s61 offen offset:2048 sc1 lds\n\t" \
  "buffer_load_dwordx4 v248, s[44:47], s61 offen offset:3072 sc1 lds\n\t" \
  "s_add_u32 m0, s65, 9216\n\t" \
  "s_add_u32 s61, s67, 45056\n\t" \
  "buffer_load_dwordx4 v248, s[44:47], s61 offen offset:0 sc1 lds\n\t" \
  "buffer_load_dword v252, v250, s[44:47], 0 offen sc1\n\t" \
  "s_add_u32 m0, s65, 10240\n\t" \
  "s_add_u32 s61, s67, 81920\n\t" \
  "buffer_load_dwordx4 v248, s[44:47], s61 offen offset:0 sc1 lds\n\t" \
  "buffer_load_dwordx4 v248, s[44:47], s61 offen offset:1024 sc1 lds\n\t" \
  "buffer_load_dwordx4 v248, s[44:47], s61 offen offset:2048 sc1 lds\n\t" \
  "buffer_load_dwordx4 v248, s[44:47], s61 offen offset:3072 sc1 lds\n\t" \
  "buffer_load_dword v252, v250, s[44:47], 0 offen sc1\n\t" \
  "v_cmp_ne_u32 vcc, s72, v252\n\t" \
  "s_nop 4\n\t" \
  "s_cbranch_vccz P2_PFX_%=\n\t" \
  "P2_PF_%=:\n\t" \
  "buffer_load_dword v252, v250, s[44:47], 0 offen sc1\n\t" \
  "s_sleep 3\n\t" \
  "s_add_u32 s63, s63, 1\n\t" \
  "s_cmp_gt_u32 s63, 1048576\n\t" \
  "s_cbranch_scc1 P2_PFX_%=\n\t" \
  "v_cmp_ne_u32 vcc, s72, v252\n\t" \
  "s_nop 4\n\t" \
  "s_cbranch_vccnz P2_PF_%=\n\t" \
  "P2_PFX_%=:\n\t" \
  "s_mov_b32 s60, s69\n\t" \
  "s_add_u32 s56, s60, s52\n\t" \
  "buffer_load_dwordx4 v[120:123], v248, s[44:47], s56 offen sc1\n\t" \
  "s_add_u32 s56, s60, s53\n\t" \
  "buffer_load_dwordx4 v[124:127], v248, s[44:47], s56 offen sc1\n\t" \
  "s_add_u32 s56, s60, s54\n\t" \
  "buffer_load_dwordx4 v[128:131], v248, s[44:47], s56 offen sc1\n\t" \
  "s_add_u32 s56, s60, s55\n\t" \
  "buffer_load_dwordx4 v[132:135], v248, s[44:47], s56 offen sc1\n\t" \
  "s_add_u32 s60, s60, 4096\n\t" \
  "s_add_u32 s56, s60, s52\n\t" \
  "buffer_load_dwordx4 v[136:139], v248, s[44:47], s56 offen sc1\n\t" \
  "s_add_u32 s56, s60, s53\n\t" \
  "buffer_load_dwordx4 v[140:143], v248, s[44:47], s56 offen sc1\n\t" \
  "s_add_u32 s56, s60, s54\n\t" \
  "buffer_load_dwordx4 v[144:147], v248, s[44:47], s56 offen sc1\n\t" \
  "s_add_u32 s56, s60, s55\n\t" \
  "buffer_load_dwordx4 v[148:151], v248, s[44:47], s56 offen sc1\n\t" \
  "s_add_u32 s60, s60, 4096\n\t" \
  "s_add_u32 s56, s60, s52\n\t" \
  "buffer_load_dwordx4 v[152:155], v248, s[44:47], s56 offen sc1\n\t" \
  "s_add_u32 s56, s60, s53\n\t" \
  "buffer_load_dwordx4 v[156:159], v248, s[44:47], s56 offen sc1\n\t" \
  "s_add_u32 s56, s60, s54\n\t" \
  "buffer_load_dwordx4 v[160:163], v248, s[44:47], s56 offen sc1\n\t" \
  "s_add_u32 s56, s60, s55\n\t" \
  "buffer_load_dwordx4 v[164:167], v248, s[44:47], s56 offen sc1\n\t" \
  "s_add_u32 s60, s60, 4096\n\t" \
  "s_add_u32 s56, s60, s52\n\t" \
  "buffer_load_dwordx4 v[168:171], v248, s[44:47], s56 offen sc1\n\t" \
  "s_add_u32 s56, s60, s53\n\t" \
  "buffer_load_dwordx4 v[172:175], v248, s[44:47], s56 offen sc1\n\t" \
  "s_add_u32 s56, s60, s54\n\t" \
  "buffer_load_dwordx4 v[176:179], v248, s[44:47], s56 offen sc1\n\t" \
  "s_add_u32 s56, s60, s55\n\t" \
  "buffer_load_dwordx4 v[180:183], v248, s[44:47], s56 offen sc1\n\t" \
  "s_waitcnt vmcnt(16)\n\t" \
  "ds_read_b128 v[216:219], v7 offset:0\n\t" \
  "ds_read_b128 v[220:223], v7 offset:5120\n\t" \
  "ds_read_b128 v[224:227], v7 offset:1024\n\t" \
  "ds_read_b128 v[228:231], v7 offset:6144\n\t" \
  "s_bitcmp0_b32 s66, 0\n\t" \
  "s_cbranch_scc1 P2_NQ_%=\n\t" \
  "v_mov_b32 v112, s72\n\t" \
  "v_mov_b32 v113, s72\n\t" \
  "v_mov_b32 v114, s72\n\t" \
  "v_mov_b32 v115, s72\n\t" \
  "s_mov_b64 exec, 0xff\n\t" \
  "buffer_store_dwordx4 v[112:115], v248, s[44:47], s73 offen sc1\n\t" \
  "s_mov_b64 exec, -1\n\t" \
  "s_nop 0\n\t" \
  "P2_NQ_%=:\n\t" \
  "v_mov_b32 v252, s75\n\t" \
  "s_waitcnt vmcnt(15) lgkmcnt(3)\n\t" \
  "v_mfma_f32_16x16x32_bf16 v[8:11], v[216:219], v[120:123], 0\n\t" \
  "ds_read_b128 v[232:235], v7 offset:2048\n\t" \
  "s_waitcnt vmcnt(11) lgkmcnt(3)\n\t" \
  "v_mfma_f32_16x16x32_bf16 v[8:11], v[220:223], v[136:139], v[8:11]\n\t" \
  "ds_read_b128 v[236:239], v7 offset:7168\n\t" \
  "s_add_u32 s60, s60, 4096\n\t" \
  "s_add_u32 s56, s60, s52\n\t" \
  "buffer_load_dwordx4 v[184:187], v248, s[44:47], s56 offen sc1\n\t" \
  "v_mfma_f32_16x16x32_bf16 v[28:31], v[216:219], v[124:127], 0\n\t" \
  "s_waitcnt vmcnt(11)\n\t" \
  "v_mfma_f32_16x16x32_bf16 v[28:31], v[220:223], v[140:143], v[28:31]\n\t" \
  "v_mfma_f32_16x16x32_bf16 v[48:51], v[216:219], v[128:131], 0\n\t" \
  "s_add_u32 m0, s65, 14336\n\t" \
  "s_add_u32 s61, s67, 86016\n\t" \
  "buffer_load_dwordx4 v248, s[44:47], s61 offen offset:0 sc1 lds\n\t" \
  "s_waitcnt vmcnt(11)\n\t" \
  "v_mfma_f32_16x16x32_bf16 v[48:51], v[220:223], v[144:147], v[48:51]\n\t" \
  "v_mfma_f32_16x16x32_bf16 v[68:71], v[216:219], v[132:135], 0\n\t" \
  "s_waitcnt vmcnt(10)\n\t" \
  "v_mfma_f32_16x16x32_bf16 v[68:71], v[220:223], v[148:151], v[68:71]\n\t" \
  "s_add_u32 s56, s60, s53\n\t" \
  "buffer_load_dwordx4 v[188:191], v248, s[44:47], s56 offen sc1\n\t" \
  "s_waitcnt lgkmcnt(3)\n\t" \
  "v_mfma_f32_16x16x32_bf16 v[12:15], v[224:227], v[120:123], 0\n\t" \
  "ds_read_b128 v[240:243], v7 offset:3072\n\t" \
  "s_waitcnt lgkmcnt(3)\n\t" \
  "v_mfma_f32_16x16x32_bf16 v[12:15], v[228:231], v[136:139], v[12:15]\n\t" \
  "ds_read_b128 v[244:247], v7 offset:8192\n\t" \
  "v_mfma_f32_16x16x32_bf16 v[32:35], v[224:227], v[124:127], 0\n\t" \
  "s_add_u32 m0, s65, 15360\n\t" \
  "s_add_u32 s61, s67, 122880\n\t" \
  "buffer_load_dwordx4 v248, s[44:47], s61 offen offset:0 sc1 lds\n\t" \
  "v_mfma_f32_16x16x32_bf16 v[32:35], v[228:231], v[140:143], v[32:35]\n\t" \
  "v_mfma_f32_16x16x32_bf16 v[52:55], v[224:227], v[128:131], 0\n\t" \
  "v_mfma_f32_16x16x32_bf16 v[52:55], v[228:231], v[144:147], v[52:55]\n\t" \
  "v_mfma_f32_16x16x32_bf16 v[72:75], v[224:227], v[132:135], 0\n\t" \
  "s_add_u32 s56, s60, s54\n\t" \
  "buffer_load_dwordx4 v[192:195], v248, s[44:47], s56 offen sc1\n\t" \
  "v_mfma_f32_16x16x32_bf16 v[72:75], v[228:231], v[148:151], v[72:75]\n\t" \
  "s_waitcnt lgkmcnt(3)\n\t" \
  "v_mfma_f32_16x16x32_bf16 v[16:19], v[232:235], v[120:123], 0\n\t" \
  "ds_read_b128 v[216:219], v7 offset:4096\n\t" \
  "s_waitcnt lgkmcnt(3)\n\t" \
  "v_mfma_f32_16x16x32_bf16 v[16:19], v[236:239], v[136:139], v[16:19]\n\t" \
  "ds_read_b128 v[220:223], v7 offset:9216\n\t" \
  "s_add_u32 s56, s60, s55\n\t" \
  "buffer_load_dwordx4 v[196:199], v248, s[44:47], s56 offen sc1\n\t" \
  "v_mfma_f32_16x16x32_bf16 v[36:39], v[232:235], v[124:127], 0\n\t" \
  "v_mfma_f32_16x16x32_bf16 v[36:39], v[236:239], v[140:143], v[36:39]\n\t" \
  "v_mfma_f32_16x16x32_bf16 v[56:59], v[232:235], v[128:131], 0\n\t" \
  "global_load_dwordx2 v[88:89], v254, s[86:87] offset:0\n\t" \
  "v_mfma_f32_16x16x32_bf16 v[56:59], v[236:239], v[144:147], v[56:59]\n\t" \
  "v_mfma_f32_16x16x32_bf16 v[76:79], v[232:235], v[132:135], 0\n\t" \
  "v_mfma_f32_16x16x32_bf16 v[76:79], v[236:239], v[148:151], v[76:79]\n\t" \
  "s_add_u32 s60, s60, 4096\n\t" \
  "s_add_u32 s56, s60, s52\n\t" \
  "buffer_load_dwordx4 v[200:203], v248, s[44:47], s56 offen sc1\n\t" \
  "s_waitcnt lgkmcnt(3)\n\t" \
  "v_mfma_f32_16x16x32_bf16 v[20:23], v[240:243], v[120:123], 0\n\t" \
  "ds_read_b128 v[224:227], v7 offset:10240\n\t" \
  "s_waitcnt lgkmcnt(3)\n\t" \
  "v_mfma_f32_16x16x32_bf16 v[20:23], v[244:247], v[136:139], v[20:23]\n\t" \
  "ds_read_b128 v[228:231], v7 offset:15360\n\t" \
  "v_mfma_f32_16x16x32_bf16 v[40:43], v[240:243], v[124:127], 0\n\t" \
  "v_mfma_f32_16x16x32_bf16 v[40:43], v[244:247], v[140:143], v[40:43]\n\t" \
  "s_add_u32 s56, s60, s53\n\t" \
  "buffer_load_dwordx4 v[204:207], v248, s[44:47], s56 offen sc1\n\t" \
  "v_mfma_f32_16x16x32_bf16 v[60:63], v[240:243], v[128:131], 0\n\t" \
  "v_mfma_f32_16x16x32_bf16 v[60:63], v[244:247], v[144:147], v[60:63]\n\t" \
  "v_mfma_f32_16x16x32_bf16 v[80:83], v[240:243], v[132:135], 0\n\t" \
  "global_load_dwordx2 v[90:91], v254, s[86:87] offset:32\n\t" \
  "v_mfma_f32_16x16x32_bf16 v[80:83], v[244:247], v[148:151], v[80:83]\n\t" \
  "s_waitcnt lgkmcnt(3)\n\t" \
  "v_mfma_f32_16x16x32_bf16 v[24:27], v[216:219], v[120:123], 0\n\t" \
  "ds_read_b128 v[232:235], v7 offset:11264\n\t" \
  "s_waitcnt lgkmcnt(3)\n\t" \
  "v_mfma_f32_16x16x32_bf16 v[24:27], v[220:223], v[136:139], v[24:27]\n\t" \
  "ds_read_b128 v[236:239], v7 offset:16384\n\t" \
  "s_add_u32 s56, s60, s54\n\t" \
  "buffer_load_dwordx4 v[208:211], v248, s[44:47], s56 offen sc1\n\t" \
  "v_mfma_f32_16x16x32_bf16 v[44:47], v[216:219], v[124:127], 0\n\t" \
  "v_mfma_f32_16x16x32_bf16 v[44:47], v[220:223], v[140:143], v[44:47]\n\t" \
  "v_mfma_f32_16x16x32_bf16 v[64:67], v[216:219], v[128:131], 0\n\t" \
  "s_add_u32 s56, s60, s55\n\t" \
  "buffer_load_dwordx4 v[212:215], v248, s[44:47], s56 offen sc1\n\t" \
  "v_mfma_f32_16x16x32_bf16 v[64:67], v[220:223], v[144:147], v[64:67]\n\t" \
  "v_mfma_f32_16x16x32_bf16 v[84:87], v[216:219], v[132:135], 0\n\t" \
  "v_mfma_f32_16x16x32_bf16 v[84:87], v[220:223], v[148:151], v[84:87]\n\t" \
  "s_waitcnt vmcnt(19) lgkmcnt(3)\n\t" \
  "v_mfma_f32_16x16x32_bf16 v[8:11], v[224:227], v[152:155], v[8:11]\n\t" \
  "ds_read_b128 v[240:243], v7 offset:12288\n\t" \
  "s_waitcnt vmcnt(15) lgkmcnt(3)\n\t" \
  "v_mfma_f32_16x16x32_bf16 v[8:11], v[228:231], v[168:171], v[8:11]\n\t" \
  "ds_read_b128 v[244:247], v7 offset:17408\n\t" \
  "s_add_u32 s60, s60, 4096\n\t" \
  "s_add_u32 s56, s60, s52\n\t" \
  "buffer_load_dwordx4 v[120:123], v248, s[44:47], s56 offen sc1\n\t" \
  "v_mfma_f32_16x16x32_bf16 v[28:31], v[224:227], v[156:159], v[28:31]\n\t" \
  "s_waitcnt vmcnt(15)\n\t" \
  "v_mfma_f32_16x16x32_bf16 v[28:31], v[228:231], v[172:175], v[28:31]\n\t" \
  "v_mfma_f32_16x16x32_bf16 v[48:51], v[224:227], v[160:163], v[48:51]\n\t" \
  "s_add_u32 m0, s65, 15360\n\t" \
  "s_add_u32 s61, s67, 122880\n\t" \
  "buffer_load_dwordx4 v248, s[44:47], s61 offen offset:1024 sc1 lds\n\t" \
  "s_waitcnt vmcnt(15)\n\t" \
  "v_mfma_f32_16x16x32_bf16 v[48:51], v[228:231], v[176:179], v[48:51]\n\t" \
  "v_mfma_f32_16x16x32_bf16 v[68:71], v[224:227], v[164:167], v[68:71]\n\t" \
  "s_waitcnt vmcnt(14)\n\t" \
  "v_mfma_f32_16x16x32_bf16 v[68:71], v[228:231], v[180:183], v[68:71]\n\t" \
  "s_add_u32 s56, s60, s53\n\t" \
  "buffer_load_dwordx4 v[124:127], v248, s[44:47], s56 offen sc1\n\t" \
  "s_waitcnt lgkmcnt(3)\n\t" \
  "v_mfma_f32_16x16x32_bf16 v[12:15], v[232:235], v[152:155], v[12:15]\n\t" \
  "ds_read_b128 v[216:219], v7 offset:13312\n\t" \
  "s_waitcnt lgkmcnt(3)\n\t" \
  "v_mfma_f32_16x16x32_bf16 v[12:15], v[236:239], v[168:171], v[12:15]\n\t" \
  "ds_read_b128 v[220:223], v7 offset:18432\n\t" \
  "v_mfma_f32_16x16x32_bf16 v[32:35], v[232:235], v[156:159], v[32:35]\n\t" \
  "global_load_dwordx2 v[92:93], v254, s[86:87] offset:64\n\t" \
  "v_mfma_f32_16x16x32_bf16 v[32:35], v[236:239], v[172:175], v[32:35]\n\t" \
  "v_mfma_f32_16x16x32_bf16 v[52:55], v[232:235], v[160:163], v[52:55]\n\t" \
  "v_mfma_f32_16x16x32_bf16 v[52:55], v[236:239], v[176:179], v[52:55]\n\t" \
  "v_mfma_f32_16x16x32_bf16 v[72:75], v[232:235], v[164:167], v[72:75]\n\t" \
  "s_add_u32 s56, s60, s54\n\t" \
  "buffer_load_dwordx4 v[128:131], v248, s[44:47], s56 offen sc1\n\t" \
  "v_mfma_f32_16x16x32_bf16 v[72:75], v[236:239], v[180:183], v[72:75]\n\t" \
  "s_waitcnt lgkmcnt(3)\n\t" \
  "v_mfma_f32_16x16x32_bf16 v[16:19], v[240:243], v[152:155], v[16:19]\n\t" \
  "ds_read_b128 v[224:227], v7 offset:14336\n\t" \
  "s_waitcnt lgkmcnt(3)\n\t" \
  "v_mfma_f32_16x16x32_bf16 v[16:19], v[244:247], v[168:171], v[16:19]\n\t" \
  "ds_read_b128 v[228:231], v7 offset:19456\n\t" \
  "s_add_u32 s56, s60, s55\n\t" \
  "buffer_load_dwordx4 v[132:135], v248, s[44:47], s56 offen sc1\n\t" \
  "v_mfma_f32_16x16x32_bf16 v[36:39], v[240:243], v[156:159], v[36:39]\n\t" \
  "v_mfma_f32_16x16x32_bf16 v[36:39], v[244:247], v[172:175], v[36:39]\n\t" \
  "v_mfma_f32_16x16x32_bf16 v[56:59], v[240:243], v[160:163], v[56:59]\n\t" \
  "global_load_dwordx2 v[94:95], v254, s[86:87] offset:96\n\t" \
  "v_mfma_f32_16x16x32_bf16 v[56:59], v[244:247], v[176:179], v[56:59]\n\t" \
  "v_mfma_f32_16x16x32_bf16 v[76:79], v[240:243], v[164:167], v[76:79]\n\t" \
  "v_mfma_f32_16x16x32_bf16 v[76:79], v[244:247], v[180:183], v[76:79]\n\t" \
  "s_add_u32 s60, s60, 4096\n\t" \
  "s_add_u32 s56, s60, s52\n\t" \
  "buffer_load_dwordx4 v[136:139], v248, s[44:47], s56 offen sc1\n\t" \
  "s_waitcnt lgkmcnt(3)\n\t" \
  "v_mfma_f32_16x16x32_bf16 v[20:23], v[216:219], v[152:155], v[20:23]\n\t" \
  "s_waitcnt lgkmcnt(2)\n\t" \
  "v_mfma_f32_16x16x32_bf16 v[20:23], v[220:223], v[168:171], v[20:23]\n\t" \
  "v_mfma_f32_16x16x32_bf16 v[40:43], v[216:219], v[156:159], v[40:43]\n\t" \
  "v_mfma_f32_16x16x32_bf16 v[40:43], v[220:223], v[172:175], v[40:43]\n\t" \
  "s_add_u32 s56, s60, s53\n\t" \
  "buffer_load_dwordx4 v[140:143], v248, s[44:47], s56 offen sc1\n\t" \
  "v_mfma_f32_16x16x32_bf16 v[60:63], v[216:219], v[160:163], v[60:63]\n\t" \
  "v_mfma_f32_16x16x32_bf16 v[60:63], v[220:223], v[176:179], v[60:63]\n\t" \
  "v_mfma_f32_16x16x32_bf16 v[80:83], v[216:219], v[164:167], v[80:83]\n\t" \
  "global_load_dwordx2 v[96:97], v254, s[86:87] offset:128\n\t" \
  "v_mfma_f32_16x16x32_bf16 v[80:83], v[220:223], v[180:183], v[80:83]\n\t" \
  "s_waitcnt lgkmcnt(1)\n\t" \
  "v_mfma_f32_16x16x32_bf16 v[24:27], v[224:227], v[152:155], v[24:27]\n\t" \
  "s_waitcnt lgkmcnt(0)\n\t" \
  "v_mfma_f32_16x16x32_bf16 v[24:27], v[228:231], v[168:171], v[24:27]\n\t" \
  "s_add_u32 s56, s60, s54\n\t" \
  "buffer_load_dwordx4 v[144:147], v248, s[44:47], s56 offen sc1\n\t" \
  "v_mfma_f32_16x16x32_bf16 v[44:47], v[224:227], v[156:159], v[44:47]\n\t" \
  "v_mfma_f32_16x16x32_bf16 v[44:47], v[228:231], v[172:175], v[44:47]\n\t" \
  "v_mfma_f32_16x16x32_bf16 v[64:67], v[224:227], v[160:163], v[64:67]\n\t" \
  "s_add_u32 s56, s60, s55\n\t" \
  "buffer_load_dwordx4 v[148:151], v248, s[44:47], s56 offen sc1\n\t" \
  "v_mfma_f32_16x16x32_bf16 v[64:67], v[228:231], v[176:179], v[64:67]\n\t" \
  "v_mfma_f32_16x16x32_bf16 v[84:87], v[224:227], v[164:167], v[84:87]\n\t" \
  "v_mfma_f32_16x16x32_bf16 v[84:87], v[228:231], v[180:183], v[84:87]\n\t" \
  "s_waitcnt vmcnt(23)\n\t" \
  "v_mfma_f32_16x16x32_bf16 v[8:11], a[0:3], v[184:187], v[8:11]\n\t" \
  "s_waitcnt vmcnt(16)\n\t" \
  "v_mfma_f32_16x16x32_bf16 v[8:11], a[20:23], v[200:203], v[8:11]\n\t" \
  "s_add_u32 s60, s60, 4096\n\t" \
  "s_add_u32 s56, s60, s52\n\t" \
  "buffer_load_dwordx4 v[152:155], v248, s[44:47], s56 offen sc1\n\t" \
  "v_mfma_f32_16x16x32_bf16 v[28:31], a[0:3], v[188:191], v[28:31]\n\t" \
  "s_waitcnt vmcnt(16)\n\t" \
  "v_mfma_f32_16x16x32_bf16 v[28:31], a[20:23], v[204:207], v[28:31]\n\t" \
  "v_mfma_f32_16x16x32_bf16 v[48:51], a[0:3], v[192:195], v[48:51]\n\t" \
  "s_add_u32 m0, s65, 15360\n\t" \
  "s_add_u32 s61, s67, 122880\n\t" \
  "buffer_load_dwordx4 v248, s[44:47], s61 offen offset:2048 sc1 lds\n\t" \
  "s_waitcnt vmcnt(15)\n\t" \
  "v_mfma_f32_16x16x32_bf16 v[48:51], a[20:23], v[208:211], v[48:51]\n\t" \
  "v_mfma_f32_16x16x32_bf16 v[68:71], a[0:3], v[196:199], v[68:71]\n\t" \
  "s_waitcnt vmcnt(14)\n\t" \
  "v_mfma_f32_16x16x32_bf16 v[68:71], a[20:23], v[212:215], v[68:71]\n\t" \
  "s_add_u32 s56, s60, s53\n\t" \
  "buffer_load_dwordx4 v[156:159], v248, s[44:47], s56 offen sc1\n\t" \
  "v_mfma_f32_16x16x32_bf16 v[12:15], a[4:7], v[184:187], v[12:15]\n\t" \
  "v_mfma_f32_16x16x32_bf16 v[12:15], a[24:27], v[200:203], v[12:15]\n\t" \
  "v_mfma_f32_16x16x32_bf16 v[32:35], a[4:7], v[188:191], v[32:35]\n\t" \
  "s_add_u32 m0, s65, 15360\n\t" \
  "s_add_u32 s61, s67, 122880\n\t" \
  "buffer_load_dwordx4 v248, s[44:47], s61 offen offset:3072 sc1 lds\n\t" \
  "v_mfma_f32_16x16x32_bf16 v[32:35], a[24:27], v[204:207], v[32:35]\n\t" \
  "v_mfma_f32_16x16x32_bf16 v[52:55], a[4:7], v[192:195], v[52:55]\n\t" \
  "v_mfma_f32_16x16x32_bf16 v[52:55], a[24:27], v[208:211], v[52:55]\n\t" \
  "v_mfma_f32_16x16x32_bf16 v[72:75], a[4:7], v[196:199], v[72:75]\n\t" \
  "s_add_u32 s56, s60, s54\n\t" \
  "buffer_load_dwordx4 v[160:163], v248, s[44:47], s56 offen sc1\n\t" \
  "v_mfma_f32_16x16x32_bf16 v[72:75], a[24:27], v[212:215], v[72:75]\n\t" \
  "v_mfma_f32_16x16x32_bf16 v[16:19], a[8:11], v[184:187], v[16:19]\n\t" \
  "v_mfma_f32_16x16x32_bf16 v[16:19], a[28:31], v[200:203], v[16:19]\n\t" \
  "s_add_u32 s56, s60, s55\n\t" \
  "buffer_load_dwordx4 v[164:167], v248, s[44:47], s56 offen sc1\n\t" \
  "v_mfma_f32_16x16x32_bf16 v[36:39], a[8:11], v[188:191], v[36:39]\n\t" \
  "v_mfma_f32_16x16x32_bf16 v[36:39], a[28:31], v[204:207], v[36:39]\n\t" \
  "v_mfma_f32_16x16x32_bf16 v[56:59], a[8:11], v[192:195], v[56:59]\n\t" \
  "s_add_u32 m0, s65, 19456\n\t" \
  "s_add_u32 s61, s67, 126976\n\t" \
  "buffer_load_dwordx4 v248, s[44:47], s61 offen offset:0 sc1 lds\n\t" \
  "v_mfma_f32_16x16x32_bf16 v[56:59], a[28:31], v[208:211], v[56:59]\n\t" \
  "v_mfma_f32_16x16x32_bf16 v[76:79], a[8:11], v[196:199], v[76:79]\n\t" \
  "v_mfma_f32_16x16x32_bf16 v[76:79], a[28:31], v[212:215], v[76:79]\n\t" \
  "s_add_u32 s60, s60, 4096\n\t" \
  "s_add_u32 s56, s60, s52\n\t" \
  "buffer_load_dwordx4 v[168:171], v248, s[44:47], s56 offen sc1\n\t" \
  "v_mfma_f32_16x16x32_bf16 v[20:23], a[12:15], v[184:187], v[20:23]\n\t" \
  "v_mfma_f32_16x16x32_bf16 v[20:23], a[32:35], v[200:203], v[20:23]\n\t" \
  "v_mfma_f32_16x16x32_bf16 v[40:43], a[12:15], v[188:191], v[40:43]\n\t" \
  "v_mfma_f32_16x16x32_bf16 v[40:43], a[32:35], v[204:207], v[40:43]\n\t" \
  "s_add_u32 s56, s60, s53\n\t" \
  "buffer_load_dwordx4 v[172:175], v248, s[44:47], s56 offen sc1\n\t" \
  "v_mfma_f32_16x16x32_bf16 v[60:63], a[12:15], v[192:195], v[60:63]\n\t" \
  "v_mfma_f32_16x16x32_bf16 v[60:63], a[32:35], v[208:211], v[60:63]\n\t" \
  "v_mfma_f32_16x16x32_bf16 v[80:83], a[12:15], v[196:199], v[80:83]\n\t" \
  "s_add_u32 m0, s62, 0\n\t" \
  "s_add_u32 s61, s68, 0\n\t" \
  "buffer_load_dwordx4 v248, s[44:47], s61 offen offset:0 sc1 lds\n\t" \
  "v_mfma_f32_16x16x32_bf16 v[80:83], a[32:35], v[212:215], v[80:83]\n\t" \
  "v_mfma_f32_16x16x32_bf16 v[24:27], a[16:19], v[184:187], v[24:27]\n\t" \
  "v_mfma_f32_16x16x32_bf16 v[24:27], a[36:39], v[200:203], v[24:27]\n\t" \
  "s_add_u32 s56, s60, s54\n\t" \
  "buffer_load_dwordx4 v[176:179], v248, s[44:47], s56 offen sc1\n\t" \
  "v_mfma_f32_16x16x32_bf16 v[44:47], a[16:19], v[188:191], v[44:47]\n\t" \
  "v_mfma_f32_16x16x32_bf16 v[44:47], a[36:39], v[204:207], v[44:47]\n\t" \
  "v_mfma_f32_16x16x32_bf16 v[64:67], a[16:19], v[192:195], v[64:67]\n\t" \
  "s_add_u32 s56, s60, s55\n\t" \
  "buffer_load_dwordx4 v[180:183], v248, s[44:47], s56 offen sc1\n\t" \
  "v_mfma_f32_16x16x32_bf16 v[64:67], a[36:39], v[208:211], v[64:67]\n\t" \
  "v_mfma_f32_16x16x32_bf16 v[84:87], a[16:19], v[196:199], v[84:87]\n\t" \
  "v_mfma_f32_16x16x32_bf16 v[84:87], a[36:39], v[212:215], v[84:87]\n\t" \
  "s_waitcnt vmcnt(23)\n\t" \
  "v_mfma_f32_16x16x32_bf16 v[8:11], a[40:43], v[120:123], v[8:11]\n\t" \
  "v_accvgpr_read_b32 v99, a244\n\t" \
  "s_waitcnt vmcnt(16)\n\t" \
  "v_mfma_f32_16x16x32_bf16 v[8:11], a[60:63], v[136:139], v[8:11]\n\t" \
  "s_add_u32 s60, s60, 4096\n\t" \
  "s_add_u32 s56, s60, s52\n\t" \
  "buffer_load_dwordx4 v[184:187], v248, s[44:47], s56 offen sc1\n\t" \
  "s_waitcnt vmcnt(15)\n\t" \
  "v_lshlrev_b32 v100, 16, v88\n\t" \
  "v_mfma_f32_16x16x32_bf16 v[28:31], a[40:43], v[124:127], v[28:31]\n\t" \
  "v_and_b32 v101, 0xffff0000, v88\n\t" \
  "v_mfma_f32_16x16x32_bf16 v[28:31], a[60:63], v[140:143], v[28:31]\n\t" \
  "v_lshlrev_b32 v98, 16, v99\n\t" \
  "v_mfma_f32_16x16x32_bf16 v[48:51], a[40:43], v[128:131], v[48:51]\n\t" \
  "buffer_load_dword v252, v251, s[44:47], 0 offen sc1\n\t" \
  "v_and_b32 v99, 0xffff0000, v99\n\t" \
  "s_waitcnt vmcnt(15)\n\t" \
  "v_mfma_f32_16x16x32_bf16 v[48:51], a[60:63], v[144:147], v[48:51]\n\t" \
  "v_mul_f32 v98, v98, v100\n\t" \
  "v_mfma_f32_16x16x32_bf16 v[68:71], a[40:43], v[132:135], v[68:71]\n\t" \
  "v_mul_f32 v99, v99, v101\n\t" \
  "s_waitcnt vmcnt(14)\n\t" \
  "v_mfma_f32_16x16x32_bf16 v[68:71], a[60:63], v[148:151], v[68:71]\n\t" \
  "s_add_u32 s56, s60, s53\n\t" \
  "buffer_load_dwordx4 v[188:191], v248, s[44:47], s56 offen sc1\n\t" \
  "v_cvt_pk_bf16_f32 v88, v98, v99\n\t" \
  "v_mfma_f32_16x16x32_bf16 v[12:15], a[44:47], v[120:123], v[12:15]\n\t" \
  "v_accvgpr_read_b32 v99, a245\n\t" \
  "v_mfma_f32_16x16x32_bf16 v[12:15], a[64:67], v[136:139], v[12:15]\n\t" \
  "v_lshlrev_b32 v100, 16, v89\n\t" \
  "v_mfma_f32_16x16x32_bf16 v[32:35], a[44:47], v[124:127], v[32:35]\n\t" \
  "s_add_u32 m0, s62, 0\n\t" \
  "s_add_u32 s61, s68, 0\n\t" \
  "buffer_load_dwordx4 v248, s[44:47], s61 offen offset:1024 sc1 lds\n\t" \
  "v_and_b32 v101, 0xffff0000, v89\n\t" \
  "v_mfma_f32_16x16x32_bf16 v[32:35], a[64:67], v[140:143], v[32:35]\n\t" \
  "v_lshlrev_b32 v98, 16, v99\n\t" \
  "v_mfma_f32_16x16x32_bf16 v[52:55], a[44:47], v[128:131], v[52:55]\n\t" \
  "v_and_b32 v99, 0xffff0000, v99\n\t" \
  "v_mfma_f32_16x16x32_bf16 v[52:55], a[64:67], v[144:147], v[52:55]\n\t" \
  "v_mul_f32 v98, v98, v100\n\t" \
  "v_mfma_f32_16x16x32_bf16 v[72:75], a[44:47], v[132:135], v[72:75]\n\t" \
  "s_add_u32 s56, s60, s54\n\t" \
  "buffer_load_dwordx4 v[192:195], v248, s[44:47], s56 offen sc1\n\t" \
  "v_mul_f32 v99, v99, v101\n\t" \
  "v_mfma_f32_16x16x32_bf16 v[72:75], a[64:67], v[148:151], v[72:75]\n\t" \
  "v_cvt_pk_bf16_f32 v89, v98, v99\n\t" \
  "v_mfma_f32_16x16x32_bf16 v[16:19], a[48:51], v[120:123], v[16:19]\n\t" \
  "v_accvgpr_read_b32 v99, a246\n\t" \
  "v_mfma_f32_16x16x32_bf16 v[16:19], a[68:71], v[136:139], v[16:19]\n\t" \
  "s_add_u32 s56, s60, s55\n\t" \
  "buffer_load_dwordx4 v[196:199], v248, s[44:47], s56 offen sc1\n\t" \
  "v_lshlrev_b32 v100, 16, v90\n\t" \
  "v_mfma_f32_16x16x32_bf16 v[36:39], a[48:51], v[124:127], v[36:39]\n\t" \
  "v_and_b32 v101, 0xffff0000, v90\n\t" \
  "v_mfma_f32_16x16x32_bf16 v[36:39], a[68:71], v[140:143], v[36:39]\n\t" \
  "v_lshlrev_b32 v98, 16, v99\n\t" \
  "v_mfma_f32_16x16x32_bf16 v[56:59], a[48:51], v[128:131], v[56:59]\n\t" \
  "buffer_load_dwordx4 v248, s[44:47], s61 offen offset:2048 sc1 lds\n\t" \
  "v_and_b32 v99, 0xffff0000, v99\n\t" \
  "v_mfma_f32_16x16x32_bf16 v[56:59], a[68:71], v[144:147], v[56:59]\n\t" \
  "v_mul_f32 v98, v98, v100\n\t" \
  "v_mfma_f32_16x16x32_bf16 v[76:79], a[48:51], v[132:135], v[76:79]\n\t" \
  "v_mul_f32 v99, v99, v101\n\t" \
  "v_mfma_f32_16x16x32_bf16 v[76:79], a[68:71], v[148:151], v[76:79]\n\t" \
  "s_add_u32 s60, s60, 4096\n\t" \
  "s_cmp_lt_u32 s64, 2\n\t" \
  "s_cselect_b32 s50, 16531712, 0\n\t" \
  "s_add_u32 s56, s60, s52\n\t" \
  "buffer_load_dwordx4 v[200:203], v248, s[48:51], s56 offen sc1\n\t" \
  "v_cvt_pk_bf16_f32 v90, v98, v99\n\t" \
  "v_mfma_f32_16x16x32_bf16 v[20:23], a[52:55], v[120:123], v[20:23]\n\t" \
  "v_accvgpr_read_b32 v99, a247\n\t" \
  "v_mfma_f32_16x16x32_bf16 v[20:23], a[72:75], v[136:139], v[20:23]\n\t" \
  "v_lshlrev_b32 v100, 16, v91\n\t" \
  "v_mfma_f32_16x16x32_bf16 v[40:43], a[52:55], v[124:127], v[40:43]\n\t" \
  "v_and_b32 v101, 0xffff0000, v91\n\t" \
  "v_mfma_f32_16x16x32_bf16 v[40:43], a[72:75], v[140:143], v[40:43]\n\t" \
  "s_add_u32 s56, s60, s53\n\t" \
  "buffer_load_dwordx4 v[204:207], v248, s[48:51], s56 offen sc1\n\t" \
  "v_lshlrev_b32 v98, 16, v99\n\t" \
  "v_mfma_f32_16x16x32_bf16 v[60:63], a[52:55], v[128:131], v[60:63]\n\t" \
  "v_and_b32 v99, 0xffff0000, v99\n\t" \
  "v_mfma_f32_16x16x32_bf16 v[60:63], a[72:75], v[144:147], v[60:63]\n\t" \
  "v_mul_f32 v98, v98, v100\n\t" \
  "v_mfma_f32_16x16x32_bf16 v[80:83], a[52:55], v[132:135], v[80:83]\n\t" \
  "buffer_load_dword v252, v251, s[44:47], 0 offen sc1\n\t" \
  "v_mul_f32 v99, v99, v101\n\t" \
  "v_mfma_f32_16x16x32_bf16 v[80:83], a[72:75], v[148:151], v[80:83]\n\t" \
  "v_cvt_pk_bf16_f32 v91, v98, v99\n\t" \
  "v_mfma_f32_16x16x32_bf16 v[24:27], a[56:59], v[120:123], v[24:27]\n\t" \
  "v_accvgpr_read_b32 v99, a248\n\t" \
  "v_mfma_f32_16x16x32_bf16 v[24:27], a[76:79], v[136:139], v[24:27]\n\t" \
  "s_add_u32 s56, s60, s54\n\t" \
  "buffer_load_dwordx4 v[208:211], v248, s[48:51], s56 offen sc1\n\t" \
  "v_lshlrev_b32 v100, 16, v92\n\t" \
  "v_mfma_f32_16x16x32_bf16 v[44:47], a[56:59], v[124:127], v[44:47]\n\t" \
  "v_and_b32 v101, 0xffff0000, v92\n\t" \
  "v_mfma_f32_16x16x32_bf16 v[44:47], a[76:79], v[140:143], v[44:47]\n\t" \
  "v_lshlrev_b32 v98, 16, v99\n\t" \
  "v_mfma_f32_16x16x32_bf16 v[64:67], a[56:59], v[128:131], v[64:67]\n\t" \
  "s_add_u32 s56, s60, s55\n\t" \
  "buffer_load_dwordx4 v[212:215], v248, s[48:51], s56 offen sc1\n\t" \
  "v_and_b32 v99, 0xffff0000, v99\n\t" \
  "v_mfma_f32_16x16x32_bf16 v[64:67], a[76:79], v[144:147], v[64:67]\n\t" \
  "v_mul_f32 v98, v98, v100\n\t" \
  "v_mfma_f32_16x16x32_bf16 v[84:87], a[56:59], v[132:135], v[84:87]\n\t" \
  "v_mul_f32 v99, v99, v101\n\t" \
  "v_mfma_f32_16x16x32_bf16 v[84:87], a[76:79], v[148:151], v[84:87]\n\t" \
  "v_cvt_pk_bf16_f32 v92, v98, v99\n\t" \
  "s_cmp_lt_u32 s64, 2\n\t" \
  "s_cbranch_scc1 P2_S1X_%=\n\t" \
  "v_cmp_ne_u32 vcc, s72, v252\n\t" \
  "s_nop 4\n\t" \
  "s_cbranch_vccz P2_S1X_%=\n\t" \
  "P2_S1_%=:\n\t" \
  "buffer_load_dword v252, v251, s[44:47], 0 offen sc1\n\t" \
  "s_sleep 3\n\t" \
  "s_add_u32 s63, s63, 1\n\t" \
  "s_cmp_gt_u32 s63, 1048576\n\t" \
  "s_cbranch_scc1 P2_S1X_%=\n\t" \
  "v_cmp_ne_u32 vcc, s72, v252\n\t" \
  "s_nop 4\n\t" \
  "s_cbranch_vccnz P2_S1_%=\n\t" \
  "P2_S1X_%=:\n\t" \
  "s_waitcnt vmcnt(23)\n\t" \
  "v_mfma_f32_16x16x32_bf16 v[8:11], a[80:83], v[152:155], v[8:11]\n\t" \
  "v_accvgpr_write_b32 a0, 0\n\t" \
  "s_waitcnt vmcnt(16)\n\t" \
  "v_mfma_f32_16x16x32_bf16 v[8:11], a[100:103], v[168:171], v[8:11]\n\t" \
  "s_mov_b32 s60, s70\n\t" \
  "s_add_u32 s56, s60, s52\n\t" \
  "buffer_load_dwordx4 v[120:123], v248, s[44:47], s56 offen sc1\n\t" \
  "v_accvgpr_write_b32 a1, 0\n\t" \
  "v_mfma_f32_16x16x32_bf16 v[28:31], a[80:83], v[156:159], v[28:31]\n\t" \
  "v_accvgpr_write_b32 a2, 0\n\t" \
  "s_waitcnt vmcnt(16)\n\t" \
  "v_mfma_f32_16x16x32_bf16 v[28:31], a[100:103], v[172:175], v[28:31]\n\t" \
  "v_accvgpr_write_b32 a3, 0\n\t" \
  "v_mfma_f32_16x16x32_bf16 v[48:51], a[80:83], v[160:163], v[48:51]\n\t" \
  "s_add_u32 m0, s62, 0\n\t" \
  "s_add_u32 s61, s68, 0\n\t" \
  "buffer_load_dwordx4 v248, s[44:47], s61 offen offset:3072 sc1 lds\n\t" \
  "v_accvgpr_write_b32 a4, 0\n\t" \
  "s_waitcnt vmcnt(15)\n\t" \
  "v_mfma_f32_16x16x32_bf16 v[48:51], a[100:103], v[176:179], v[48:51]\n\t" \
  "v_accvgpr_write_b32 a5, 0\n\t" \
  "v_mfma_f32_16x16x32_bf16 v[68:71], a[80:83], v[164:167], v[68:71]\n\t" \
  "v_accvgpr_write_b32 a6, 0\n\t" \
  "s_waitcnt vmcnt(14)\n\t" \
  "v_mfma_f32_16x16x32_bf16 v[68:71], a[100:103], v[180:183], v[68:71]\n\t" \
  "s_add_u32 s56, s60, s53\n\t" \
  "buffer_load_dwordx4 v[124:127], v248, s[44:47], s56 offen sc1\n\t" \
  "v_accvgpr_write_b32 a7, 0\n\t" \
  "v_mfma_f32_16x16x32_bf16 v[12:15], a[84:87], v[152:155], v[12:15]\n\t" \
  "v_accvgpr_write_b32 a8, 0\n\t" \
  "v_mfma_f32_16x16x32_bf16 v[12:15], a[104:107], v[168:171], v[12:15]\n\t" \
  "v_accvgpr_write_b32 a9, 0\n\t" \
  "v_mfma_f32_16x16x32_bf16 v[32:35], a[84:87], v[156:159], v[32:35]\n\t" \
  "s_add_u32 m0, s62, 4096\n\t" \
  "s_add_u32 s61, s68, 4096\n\t" \
  "buffer_load_dwordx4 v248, s[44:47], s61 offen offset:0 sc1 lds\n\t" \
  "v_accvgpr_write_b32 a10, 0\n\t" \
  "v_mfma_f32_16x16x32_bf16 v[32:35], a[104:107], v[172:175], v[32:35]\n\t" \
  "v_accvgpr_write_b32 a11, 0\n\t" \
  "v_mfma_f32_16x16x32_bf16 v[52:55], a[84:87], v[160:163], v[52:55]\n\t" \
  "v_accvgpr_write_b32 a12, 0\n\t" \
  "v_mfma_f32_16x16x32_bf16 v[52:55], a[104:107], v[176:179], v[52:55]\n\t" \
  "v_accvgpr_write_b32 a13, 0\n\t" \
  "v_mfma_f32_16x16x32_bf16 v[72:75], a[84:87], v[164:167], v[72:75]\n\t" \
  "s_add_u32 s56, s60, s54\n\t" \
  "buffer_load_dwordx4 v[128:131], v248, s[44:47], s56 offen sc1\n\t" \
  "v_accvgpr_write_b32 a14, 0\n\t" \
  "v_mfma_f32_16x16x32_bf16 v[72:75], a[104:107], v[180:183], v[72:75]\n\t" \
  "v_accvgpr_write_b32 a15, 0\n\t" \
  "v_mfma_f32_16x16x32_bf16 v[16:19], a[88:91], v[152:155], v[16:19]\n\t" \
  "v_accvgpr_write_b32 a16, 0\n\t" \
  "v_mfma_f32_16x16x32_bf16 v[16:19], a[108:111], v[168:171], v[16:19]\n\t" \
  "s_add_u32 s56, s60, s55\n\t" \
  "buffer_load_dwordx4 v[132:135], v248, s[44:47], s56 offen sc1\n\t" \
  "v_accvgpr_write_b32 a17, 0\n\t" \
  "v_mfma_f32_16x16x32_bf16 v[36:39], a[88:91], v[156:159], v[36:39]\n\t" \
  "v_accvgpr_write_b32 a18, 0\n\t" \
  "v_mfma_f32_16x16x32_bf16 v[36:39], a[108:111], v[172:175], v[36:39]\n\t" \
  "v_accvgpr_write_b32 a19, 0\n\t" \
  "v_mfma_f32_16x16x32_bf16 v[56:59], a[88:91], v[160:163], v[56:59]\n\t" \
  "s_add_u32 m0, s62, 5120\n\t" \
  "s_add_u32 s61, s68, 40960\n\t" \
  "buffer_load_dwordx4 v248, s[44:47], s61 offen offset:0 sc1 lds\n\t" \
  "v_accvgpr_write_b32 a20, 0\n\t" \
  "v_mfma_f32_16x16x32_bf16 v[56:59], a[108:111], v[176:179], v[56:59]\n\t" \
  "v_accvgpr_write_b32 a21, 0\n\t" \
  "v_mfma_f32_16x16x32_bf16 v[76:79], a[88:91], v[164:167], v[76:79]\n\t" \
  "v_accvgpr_write_b32 a22, 0\n\t" \
  "v_mfma_f32_16x16x32_bf16 v[76:79], a[108:111], v[180:183], v[76:79]\n\t" \
  "s_add_u32 s60, s60, 4096\n\t" \
  "s_add_u32 s56, s60, s52\n\t" \
  "buffer_load_dwordx4 v[136:139], v248, s[44:47], s56 offen sc1\n\t" \
  "v_accvgpr_write_b32 a23, 0\n\t" \
  "v_mfma_f32_16x16x32_bf16 v[20:23], a[92:95], v[152:155], v[20:23]\n\t" \
  "v_accvgpr_write_b32 a24, 0\n\t" \
  "v_mfma_f32_16x16x32_bf16 v[20:23], a[112:115], v[168:171], v[20:23]\n\t" \
  "v_accvgpr_write_b32 a25, 0\n\t" \
  "v_mfma_f32_16x16x32_bf16 v[40:43], a[92:95], v[156:159], v[40:43]\n\t" \
  "v_accvgpr_write_b32 a26, 0\n\t" \
  "v_mfma_f32_16x16x32_bf16 v[40:43], a[112:115], v[172:175], v[40:43]\n\t" \
  "s_add_u32 s56, s60, s53\n\t" \
  "buffer_load_dwordx4 v[140:143], v248, s[44:47], s56 offen sc1\n\t" \
  "v_accvgpr_write_b32 a27, 0\n\t" \
  "v_mfma_f32_16x16x32_bf16 v[60:63], a[92:95], v[160:163], v[60:63]\n\t" \
  "v_accvgpr_write_b32 a28, 0\n\t" \
  "v_mfma_f32_16x16x32_bf16 v[60:63], a[112:115], v[176:179], v[60:63]\n\t" \
  "v_accvgpr_write_b32 a29, 0\n\t" \
  "v_mfma_f32_16x16x32_bf16 v[80:83], a[92:95], v[164:167], v[80:83]\n\t" \
  "buffer_load_dwordx4 v248, s[44:47], s61 offen offset:1024 sc1 lds\n\t" \
  "v_accvgpr_write_b32 a30, 0\n\t" \
  "v_mfma_f32_16x16x32_bf16 v[80:83], a[112:115], v[180:183], v[80:83]\n\t" \
  "v_accvgpr_write_b32 a31, 0\n\t" \
  "v_mfma_f32_16x16x32_bf16 v[24:27], a[96:99], v[152:155], v[24:27]\n\t" \
  "v_accvgpr_write_b32 a32, 0\n\t" \
  "v_mfma_f32_16x16x32_bf16 v[24:27], a[116:119], v[168:171], v[24:27]\n\t" \
  "s_add_u32 s56, s60, s54\n\t" \
  "buffer_load_dwordx4 v[144:147], v248, s[44:47], s56 offen sc1\n\t" \
  "v_accvgpr_write_b32 a33, 0\n\t" \
  "v_mfma_f32_16x16x32_bf16 v[44:47], a[96:99], v[156:159], v[44:47]\n\t" \
  "v_accvgpr_write_b32 a34, 0\n\t" \
  "v_mfma_f32_16x16x32_bf16 v[44:47], a[116:119], v[172:175], v[44:47]\n\t" \
  "v_accvgpr_write_b32 a35, 0\n\t" \
  "v_mfma_f32_16x16x32_bf16 v[64:67], a[96:99], v[160:163], v[64:67]\n\t" \
  "s_add_u32 s56, s60, s55\n\t" \
  "buffer_load_dwordx4 v[148:151], v248, s[44:47], s56 offen sc1\n\t" \
  "v_accvgpr_write_b32 a36, 0\n\t" \
  "v_mfma_f32_16x16x32_bf16 v[64:67], a[116:119], v[176:179], v[64:67]\n\t" \
  "v_accvgpr_write_b32 a37, 0\n\t" \
  "v_mfma_f32_16x16x32_bf16 v[84:87], a[96:99], v[164:167], v[84:87]\n\t" \
  "v_accvgpr_write_b32 a38, 0\n\t" \
  "v_mfma_f32_16x16x32_bf16 v[84:87], a[116:119], v[180:183], v[84:87]\n\t" \
  "v_accvgpr_write_b32 a39, 0\n\t" \
  "s_waitcnt vmcnt(23)\n\t" \
  "v_mfma_f32_16x16x32_bf16 v[8:11], a[120:123], v[184:187], v[8:11]\n\t" \
  "v_accvgpr_write_b32 a40, 0\n\t" \
  "s_waitcnt vmcnt(16)\n\t" \
  "v_mfma_f32_16x16x32_bf16 v[8:11], a[140:143], v[200:203], v[8:11]\n\t" \
  "s_add_u32 s60, s60, 4096\n\t" \
  "s_add_u32 s56, s60, s52\n\t" \
  "buffer_load_dwordx4 v[152:155], v248, s[44:47], s56 offen sc1\n\t" \
  "v_accvgpr_write_b32 a41, 0\n\t" \
  "v_mfma_f32_16x16x32_bf16 v[28:31], a[120:123], v[188:191], v[28:31]\n\t" \
  "v_accvgpr_write_b32 a42, 0\n\t" \
  "s_waitcnt vmcnt(16)\n\t" \
  "v_mfma_f32_16x16x32_bf16 v[28:31], a[140:143], v[204:207], v[28:31]\n\t" \
  "v_accvgpr_write_b32 a43, 0\n\t" \
  "v_mfma_f32_16x16x32_bf16 v[48:51], a[120:123], v[192:195], v[48:51]\n\t" \
  "buffer_load_dword v252, v251, s[44:47], 0 offen sc1\n\t" \
  "v_accvgpr_write_b32 a44, 0\n\t" \
  "s_waitcnt vmcnt(15)\n\t" \
  "v_mfma_f32_16x16x32_bf16 v[48:51], a[140:143], v[208:211], v[48:51]\n\t" \
  "v_accvgpr_write_b32 a45, 0\n\t" \
  "v_mfma_f32_16x16x32_bf16 v[68:71], a[120:123], v[196:199], v[68:71]\n\t" \
  "v_accvgpr_write_b32 a46, 0\n\t" \
  "s_waitcnt vmcnt(14)\n\t" \
  "v_mfma_f32_16x16x32_bf16 v[68:71], a[140:143], v[212:215], v[68:71]\n\t" \
  "s_add_u32 s56, s60, s53\n\t" \
  "buffer_load_dwordx4 v[156:159], v248, s[44:47], s56 offen sc1\n\t" \
  "v_accvgpr_write_b32 a47, 0\n\t" \
  "v_mfma_f32_16x16x32_bf16 v[12:15], a[124:127], v[184:187], v[12:15]\n\t" \
  "v_accvgpr_write_b32 a48, 0\n\t" \
  "v_mfma_f32_16x16x32_bf16 v[12:15], a[144:147], v[200:203], v[12:15]\n\t" \
  "v_accvgpr_write_b32 a49, 0\n\t" \
  "v_mfma_f32_16x16x32_bf16 v[32:35], a[124:127], v[188:191], v[32:35]\n\t" \
  "s_add_u32 m0, s62, 5120\n\t" \
  "s_add_u32 s61, s68, 40960\n\t" \
  "buffer_load_dwordx4 v248, s[44:47], s61 offen offset:2048 sc1 lds\n\t" \
  "v_accvgpr_write_b32 a50, 0\n\t" \
  "v_mfma_f32_16x16x32_bf16 v[32:35], a[144:147], v[204:207], v[32:35]\n\t" \
  "v_accvgpr_write_b32 a51, 0\n\t" \
  "v_mfma_f32_16x16x32_bf16 v[52:55], a[124:127], v[192:195], v[52:55]\n\t" \
  "v_accvgpr_write_b32 a52, 0\n\t" \
  "v_mfma_f32_16x16x32_bf16 v[52:55], a[144:147], v[208:211], v[52:55]\n\t" \
  "v_accvgpr_write_b32 a53, 0\n\t" \
  "v_mfma_f32_16x16x32_bf16 v[72:75], a[124:127], v[196:199], v[72:75]\n\t" \
  "s_add_u32 s56, s60, s54\n\t" \
  "buffer_load_dwordx4 v[160:163], v248, s[44:47], s56 offen sc1\n\t" \
  "v_accvgpr_write_b32 a54, 0\n\t" \
  "v_mfma_f32_16x16x32_bf16 v[72:75], a[144:147], v[212:215], v[72:75]\n\t" \
  "v_accvgpr_write_b32 a55, 0\n\t" \
  "v_mfma_f32_16x16x32_bf16 v[16:19], a[128:131], v[184:187], v[16:19]\n\t" \
  "v_accvgpr_write_b32 a56, 0\n\t" \
  "v_mfma_f32_16x16x32_bf16 v[16:19], a[148:151], v[200:203], v[16:19]\n\t" \
  "s_add_u32 s56, s60, s55\n\t" \
  "buffer_load_dwordx4 v[164:167], v248, s[44:47], s56 offen sc1\n\t" \
  "v_accvgpr_write_b32 a57, 0\n\t" \
  "v_mfma_f32_16x16x32_bf16 v[36:39], a[128:131], v[188:191], v[36:39]\n\t" \
  "v_accvgpr_write_b32 a58, 0\n\t" \
  "v_mfma_f32_16x16x32_bf16 v[36:39], a[148:151], v[204:207], v[36:39]\n\t" \
  "v_accvgpr_write_b32 a59, 0\n\t" \
  "v_mfma_f32_16x16x32_bf16 v[56:59], a[128:131], v[192:195], v[56:59]\n\t" \
  "buffer_load_dwordx4 v248, s[44:47], s61 offen offset:3072 sc1 lds\n\t" \
  "v_accvgpr_write_b32 a60, 0\n\t" \
  "v_mfma_f32_16x16x32_bf16 v[56:59], a[148:151], v[208:211], v[56:59]\n\t" \
  "v_accvgpr_write_b32 a61, 0\n\t" \
  "v_mfma_f32_16x16x32_bf16 v[76:79], a[128:131], v[196:199], v[76:79]\n\t" \
  "v_accvgpr_write_b32 a62, 0\n\t" \
  "v_mfma_f32_16x16x32_bf16 v[76:79], a[148:151], v[212:215], v[76:79]\n\t" \
  "s_add_u32 s60, s60, 4096\n\t" \
  "s_add_u32 s56, s60, s52\n\t" \
  "buffer_load_dwordx4 v[168:171], v248, s[44:47], s56 offen sc1\n\t" \
  "v_accvgpr_write_b32 a63, 0\n\t" \
  "v_mfma_f32_16x16x32_bf16 v[20:23], a[132:135], v[184:187], v[20:23]\n\t" \
  "v_accvgpr_write_b32 a64, 0\n\t" \
  "v_mfma_f32_16x16x32_bf16 v[20:23], a[152:155], v[200:203], v[20:23]\n\t" \
  "v_accvgpr_write_b32 a65, 0\n\t" \
  "v_mfma_f32_16x16x32_bf16 v[40:43], a[132:135], v[188:191], v[40:43]\n\t" \
  "v_accvgpr_write_b32 a66, 0\n\t" \
  "v_mfma_f32_16x16x32_bf16 v[40:43], a[152:155], v[204:207], v[40:43]\n\t" \
  "s_add_u32 s56, s60, s53\n\t" \
  "buffer_load_dwordx4 v[172:175], v248, s[44:47], s56 offen sc1\n\t" \
  "v_accvgpr_write_b32 a67, 0\n\t" \
  "v_mfma_f32_16x16x32_bf16 v[60:63], a[132:135], v[192:195], v[60:63]\n\t" \
  "v_accvgpr_write_b32 a68, 0\n\t" \
  "v_mfma_f32_16x16x32_bf16 v[60:63], a[152:155], v[208:211], v[60:63]\n\t" \
  "v_accvgpr_write_b32 a69, 0\n\t" \
  "v_mfma_f32_16x16x32_bf16 v[80:83], a[132:135], v[196:199], v[80:83]\n\t" \
  "buffer_load_dword v252, v251, s[44:47], 0 offen sc1\n\t" \
  "v_accvgpr_write_b32 a70, 0\n\t" \
  "v_mfma_f32_16x16x32_bf16 v[80:83], a[152:155], v[212:215], v[80:83]\n\t" \
  "v_accvgpr_write_b32 a71, 0\n\t" \
  "v_mfma_f32_16x16x32_bf16 v[24:27], a[136:139], v[184:187], v[24:27]\n\t" \
  "v_accvgpr_write_b32 a72, 0\n\t" \
  "v_mfma_f32_16x16x32_bf16 v[24:27], a[156:159], v[200:203], v[24:27]\n\t" \
  "s_add_u32 s56, s60, s54\n\t" \
  "buffer_load_dwordx4 v[176:179], v248, s[44:47], s56 offen sc1\n\t" \
  "v_accvgpr_write_b32 a73, 0\n\t" \
  "v_mfma_f32_16x16x32_bf16 v[44:47], a[136:139], v[188:191], v[44:47]\n\t" \
  "v_accvgpr_write_b32 a74, 0\n\t" \
  "v_mfma_f32_16x16x32_bf16 v[44:47], a[156:159], v[204:207], v[44:47]\n\t" \
  "v_accvgpr_write_b32 a75, 0\n\t" \
  "v_mfma_f32_16x16x32_bf16 v[64:67], a[136:139], v[192:195], v[64:67]\n\t" \
  "s_add_u32 s56, s60, s55\n\t" \
  "buffer_load_dwordx4 v[180:183], v248, s[44:47], s56 offen sc1\n\t" \
  "v_accvgpr_write_b32 a76, 0\n\t" \
  "v_mfma_f32_16x16x32_bf16 v[64:67], a[156:159], v[208:211], v[64:67]\n\t" \
  "v_accvgpr_write_b32 a77, 0\n\t" \
  "v_mfma_f32_16x16x32_bf16 v[84:87], a[136:139], v[196:199], v[84:87]\n\t" \
  "v_accvgpr_write_b32 a78, 0\n\t" \
  "v_mfma_f32_16x16x32_bf16 v[84:87], a[156:159], v[212:215], v[84:87]\n\t" \
  "v_accvgpr_write_b32 a79, 0\n\t" \
  "s_cmp_lt_u32 s64, 2\n\t" \
  "s_cbranch_scc0 P2_E_%=\n\t" \
  "v_cmp_ne_u32 vcc, s72, v252\n\t" \
  "s_nop 4\n\t" \
  "s_cbranch_vccz P2_S2X_%=\n\t" \
  "P2_S2_%=:\n\t" \
  "buffer_load_dword v252, v251, s[44:47], 0 offen sc1\n\t" \
  "s_sleep 3\n\t" \
  "s_add_u32 s63, s63, 1\n\t" \
  "s_cmp_gt_u32 s63, 1048576\n\t" \
  "s_cbranch_scc1 P2_S2X_%=\n\t" \
  "v_cmp_ne_u32 vcc, s72, v252\n\t" \
  "s_nop 4\n\t" \
  "s_cbranch_vccnz P2_S2_%=\n\t" \
  "P2_S2X_%=:\n\t" \
  "s_waitcnt vmcnt(23)\n\t" \
  "v_mfma_f32_16x16x32_bf16 v[8:11], a[160:163], v[120:123], v[8:11]\n\t" \
  "v_accvgpr_read_b32 v99, a249\n\t" \
  "s_waitcnt vmcnt(16)\n\t" \
  "v_mfma_f32_16x16x32_bf16 v[8:11], a[180:183], v[136:139], v[8:11]\n\t" \
  "s_mov_b32 s60, s71\n\t" \
  "s_add_u32 s56, s60, s52\n\t" \
  "buffer_load_dwordx4 v[184:187], v248, s[44:47], s56 offen sc1\n\t" \
  "v_lshlrev_b32 v100, 16, v93\n\t" \
  "v_mfma_f32_16x16x32_bf16 v[28:31], a[160:163], v[124:127], v[28:31]\n\t" \
  "v_and_b32 v101, 0xffff0000, v93\n\t" \
  "s_waitcnt vmcnt(16)\n\t" \
  "v_mfma_f32_16x16x32_bf16 v[28:31], a[180:183], v[140:143], v[28:31]\n\t" \
  "v_lshlrev_b32 v98, 16, v99\n\t" \
  "v_mfma_f32_16x16x32_bf16 v[48:51], a[160:163], v[128:131], v[48:51]\n\t" \
  "s_add_u32 m0, s62, 9216\n\t" \
  "s_add_u32 s61, s68, 45056\n\t" \
  "buffer_load_dwordx4 v248, s[44:47], s61 offen offset:0 sc1 lds\n\t" \
  "v_and_b32 v99, 0xffff0000, v99\n\t" \
  "s_waitcnt vmcnt(15)\n\t" \
  "v_mfma_f32_16x16x32_bf16 v[48:51], a[180:183], v[144:147], v[48:51]\n\t" \
  "v_mul_f32 v98, v98, v100\n\t" \
  "v_mfma_f32_16x16x32_bf16 v[68:71], a[160:163], v[132:135], v[68:71]\n\t" \
  "v_mul_f32 v99, v99, v101\n\t" \
  "s_waitcnt vmcnt(14)\n\t" \
  "v_mfma_f32_16x16x32_bf16 v[68:71], a[180:183], v[148:151], v[68:71]\n\t" \
  "s_add_u32 s56, s60, s53\n\t" \
  "buffer_load_dwordx4 v[188:191], v248, s[44:47], s56 offen sc1\n\t" \
  "v_cvt_pk_bf16_f32 v93, v98, v99\n\t" \
  "v_mfma_f32_16x16x32_bf16 v[12:15], a[164:167], v[120:123], v[12:15]\n\t" \
  "v_accvgpr_read_b32 v99, a250\n\t" \
  "v_mfma_f32_16x16x32_bf16 v[12:15], a[184:187], v[136:139], v[12:15]\n\t" \
  "v_lshlrev_b32 v100, 16, v94\n\t" \
  "v_mfma_f32_16x16x32_bf16 v[32:35], a[164:167], v[124:127], v[32:35]\n\t" \
  "s_lshr_b32 s57, s52, 3\n\t" \
  "s_add_u32 s57, s57, s80\n\t" \
  "buffer_load_dwordx2 v[104:105], v255, s[44:47], s57 offen sc1\n\t" \
  "v_and_b32 v101, 0xffff0000, v94\n\t" \
  "v_mfma_f32_16x16x32_bf16 v[32:35], a[184:187], v[140:143], v[32:35]\n\t" \
  "v_lshlrev_b32 v98, 16, v99\n\t" \
  "v_mfma_f32_16x16x32_bf16 v[52:55], a[164:167], v[128:131], v[52:55]\n\t" \
  "v_and_b32 v99, 0xffff0000, v99\n\t" \
  "v_mfma_f32_16x16x32_bf16 v[52:55], a[184:187], v[144:147], v[52:55]\n\t" \
  "s_add_u32 s56, s60, s54\n\t" \
  "buffer_load_dwordx4 v[192:195], v248, s[44:47], s56 offen sc1\n\t" \
  "v_mul_f32 v98, v98, v100\n\t" \
  "v_mfma_f32_16x16x32_bf16 v[72:75], a[164:167], v[132:135], v[72:75]\n\t" \
  "v_mul_f32 v99, v99, v101\n\t" \
  "v_mfma_f32_16x16x32_bf16 v[72:75], a[184:187], v[148:151], v[72:75]\n\t" \
  "v_cvt_pk_bf16_f32 v94, v98, v99\n\t" \
  "v_mfma_f32_16x16x32_bf16 v[16:19], a[168:171], v[120:123], v[16:19]\n\t" \
  "s_add_u32 s56, s60, s55\n\t" \
  "buffer_load_dwordx4 v[196:199], v248, s[44:47], s56 offen sc1\n\t" \
  "v_accvgpr_read_b32 v99, a251\n\t" \
  "v_mfma_f32_16x16x32_bf16 v[16:19], a[188:191], v[136:139], v[16:19]\n\t" \
  "v_lshlrev_b32 v100, 16, v95\n\t" \
  "v_mfma_f32_16x16x32_bf16 v[36:39], a[168:171], v[124:127], v[36:39]\n\t" \
  "v_and_b32 v101, 0xffff0000, v95\n\t" \
  "v_mfma_f32_16x16x32_bf16 v[36:39], a[188:191], v[140:143], v[36:39]\n\t" \
  "s_lshr_b32 s57, s53, 3\n\t" \
  "s_add_u32 s57, s57, s80\n\t" \
  "buffer_load_dwordx2 v[106:107], v255, s[44:47], s57 offen sc1\n\t" \
  "v_lshlrev_b32 v98, 16, v99\n\t" \
  "v_mfma_f32_16x16x32_bf16 v[56:59], a[168:171], v[128:131], v[56:59]\n\t" \
  "v_and_b32 v99, 0xffff0000, v99\n\t" \
  "v_mfma_f32_16x16x32_bf16 v[56:59], a[188:191], v[144:147], v[56:59]\n\t" \
  "v_mul_f32 v98, v98, v100\n\t" \
  "v_mfma_f32_16x16x32_bf16 v[76:79], a[168:171], v[132:135], v[76:79]\n\t" \
  "s_add_u32 s60, s60, 4096\n\t" \
  "s_add_u32 s56, s60, s52\n\t" \
  "buffer_load_dwordx4 v[200:203], v248, s[44:47], s56 offen sc1\n\t" \
  "v_mul_f32 v99, v99, v101\n\t" \
  "v_mfma_f32_16x16x32_bf16 v[76:79], a[188:191], v[148:151], v[76:79]\n\t" \
  "v_cvt_pk_bf16_f32 v95, v98, v99\n\t" \
  "v_mfma_f32_16x16x32_bf16 v[20:23], a[172:175], v[120:123], v[20:23]\n\t" \
  "v_accvgpr_read_b32 v99, a252\n\t" \
  "v_mfma_f32_16x16x32_bf16 v[20:23], a[192:195], v[136:139], v[20:23]\n\t" \
  "s_lshr_b32 s57, s54, 3\n\t" \
  "s_add_u32 s57, s57, s80\n\t" \
  "buffer_load_dwordx2 v[108:109], v255, s[44:47], s57 offen sc1\n\t" \
  "v_lshlrev_b32 v100, 16, v96\n\t" \
  "v_mfma_f32_16x16x32_bf16 v[40:43], a[172:175], v[124:127], v[40:43]\n\t" \
  "v_and_b32 v101, 0xffff0000, v96\n\t" \
  "v_mfma_f32_16x16x32_bf16 v[40:43], a[192:195], v[140:143], v[40:43]\n\t" \
  "v_lshlrev_b32 v98, 16, v99\n\t" \
  "v_mfma_f32_16x16x32_bf16 v[60:63], a[172:175], v[128:131], v[60:63]\n\t" \
  "s_add_u32 s56, s60, s53\n\t" \
  "buffer_load_dwordx4 v[204:207], v248, s[44:47], s56 offen sc1\n\t" \
  "v_and_b32 v99, 0xffff0000, v99\n\t" \
  "v_mfma_f32_16x16x32_bf16 v[60:63], a[192:195], v[144:147], v[60:63]\n\t" \
  "v_mul_f32 v98, v98, v100\n\t" \
  "v_mfma_f32_16x16x32_bf16 v[80:83], a[172:175], v[132:135], v[80:83]\n\t" \
  "v_mul_f32 v99, v99, v101\n\t" \
  "v_mfma_f32_16x16x32_bf16 v[80:83], a[192:195], v[148:151], v[80:83]\n\t" \
  "s_add_u32 s56, s60, s54\n\t" \
  "buffer_load_dwordx4 v[208:211], v248, s[44:47], s56 offen sc1\n\t" \
  "v_cvt_pk_bf16_f32 v96, v98, v99\n\t" \
  "v_mfma_f32_16x16x32_bf16 v[24:27], a[176:179], v[120:123], v[24:27]\n\t" \
  "v_accvgpr_read_b32 v99, a253\n\t" \
  "v_mfma_f32_16x16x32_bf16 v[24:27], a[196:199], v[136:139], v[24:27]\n\t" \
  "v_lshlrev_b32 v100, 16, v97\n\t" \
  "v_mfma_f32_16x16x32_bf16 v[44:47], a[176:179], v[124:127], v[44:47]\n\t" \
  "s_lshr_b32 s57, s55, 3\n\t" \
  "s_add_u32 s57, s57, s80\n\t" \
  "buffer_load_dwordx2 v[110:111], v255, s[44:47], s57 offen sc1\n\t" \
  "v_and_b32 v101, 0xffff0000, v97\n\t" \
  "v_mfma_f32_16x16x32_bf16 v[44:47], a[196:199], v[140:143], v[44:47]\n\t" \
  "v_lshlrev_b32 v98, 16, v99\n\t" \
  "v_mfma_f32_16x16x32_bf16 v[64:67], a[176:179], v[128:131], v[64:67]\n\t" \
  "v_and_b32 v99, 0xffff0000, v99\n\t" \
  "v_mfma_f32_16x16x32_bf16 v[64:67], a[196:199], v[144:147], v[64:67]\n\t" \
  "s_add_u32 s56, s60, s55\n\t" \
  "buffer_load_dwordx4 v[212:215], v248, s[44:47], s56 offen sc1\n\t" \
  "v_mul_f32 v98, v98, v100\n\t" \
  "v_mfma_f32_16x16x32_bf16 v[84:87], a[176:179], v[132:135], v[84:87]\n\t" \
  "v_mul_f32 v99, v99, v101\n\t" \
  "v_mfma_f32_16x16x32_bf16 v[84:87], a[196:199], v[148:151], v[84:87]\n\t" \
  "v_cvt_pk_bf16_f32 v97, v98, v99\n\t" \
  "s_waitcnt vmcnt(24)\n\t" \
  "v_mfma_f32_16x16x32_bf16 v[8:11], a[200:203], v[152:155], v[8:11]\n\t" \
  "s_sub_u32 s58, s64, 1\n\ts_and_b32 s58, s58, 3\n\ts_cmp_eq_u32 s58, 3\n\ts_cselect_b32 s58, 7, s58\n\t" \
  "s_waitcnt vmcnt(17)\n\t" \
  "v_mfma_f32_16x16x32_bf16 v[8:11], a[220:223], v[168:171], v[8:11]\n\t" \
  "s_add_u32 s60, s60, 4096\n\t" \
  "s_add_u32 s56, s60, s52\n\t" \
  "buffer_load_dwordx4 v[120:123], v248, s[44:47], s56 offen sc1\n\t" \
  "v_mfma_f32_16x16x32_bf16 v[28:31], a[200:203], v[156:159], v[28:31]\n\t" \
  "s_mul_i32 s58, s58, 20480\n\t" \
  "s_waitcnt vmcnt(17)\n\t" \
  "v_mfma_f32_16x16x32_bf16 v[28:31], a[220:223], v[172:175], v[28:31]\n\t" \
  "v_mfma_f32_16x16x32_bf16 v[48:51], a[200:203], v[160:163], v[48:51]\n\t" \
  "s_add_u32 m0, s62, 10240\n\t" \
  "s_add_u32 s61, s68, 81920\n\t" \
  "buffer_load_dwordx4 v248, s[44:47], s61 offen offset:0 sc1 lds\n\t" \
  "s_waitcnt vmcnt(16)\n\t" \
  "v_mfma_f32_16x16x32_bf16 v[48:51], a[220:223], v[176:179], v[48:51]\n\t" \
  "s_add_u32 s58, s58, s85\n\t" \
  "v_mfma_f32_16x16x32_bf16 v[68:71], a[200:203], v[164:167], v[68:71]\n\t" \
  "s_waitcnt vmcnt(15)\n\t" \
  "v_mfma_f32_16x16x32_bf16 v[68:71], a[220:223], v[180:183], v[68:71]\n\t" \
  "s_add_u32 s56, s60, s53\n\t" \
  "buffer_load_dwordx4 v[124:127], v248, s[44:47], s56 offen sc1\n\t" \
  "s_add_u32 s58, s58, 0\n\t" \
  "v_mfma_f32_16x16x32_bf16 v[12:15], a[204:207], v[152:155], v[12:15]\n\t" \
  "v_mfma_f32_16x16x32_bf16 v[12:15], a[224:227], v[168:171], v[12:15]\n\t" \
  "v_mfma_f32_16x16x32_bf16 v[32:35], a[204:207], v[156:159], v[32:35]\n\t" \
  "buffer_load_dwordx4 v248, s[44:47], s61 offen offset:1024 sc1 lds\n\t" \
  "v_add_u32 v250, s58, v248\n\t" \
  "v_mfma_f32_16x16x32_bf16 v[32:35], a[224:227], v[172:175], v[32:35]\n\t" \
  "v_mfma_f32_16x16x32_bf16 v[52:55], a[204:207], v[160:163], v[52:55]\n\t" \
  "s_sub_u32 s58, s64, 2\n\ts_and_b32 s58, s58, 3\n\ts_cmp_eq_u32 s58, 3\n\ts_cselect_b32 s58, 7, s58\n\t" \
  "v_mfma_f32_16x16x32_bf16 v[52:55], a[224:227], v[176:179], v[52:55]\n\t" \
  "s_add_u32 s56, s60, s54\n\t" \
  "buffer_load_dwordx4 v[128:131], v248, s[44:47], s56 offen sc1\n\t" \
  "v_mfma_f32_16x16x32_bf16 v[72:75], a[204:207], v[164:167], v[72:75]\n\t" \
  "v_mfma_f32_16x16x32_bf16 v[72:75], a[224:227], v[180:183], v[72:75]\n\t" \
  "s_mul_i32 s58, s58, 20480\n\t" \
  "v_mfma_f32_16x16x32_bf16 v[16:19], a[208:211], v[152:155], v[16:19]\n\t" \
  "s_add_u32 s56, s60, s55\n\t" \
  "buffer_load_dwordx4 v[132:135], v248, s[44:47], s56 offen sc1\n\t" \
  "v_mfma_f32_16x16x32_bf16 v[16:19], a[228:231], v[168:171], v[16:19]\n\t" \
  "s_add_u32 s58, s58, s85\n\t" \
  "v_mfma_f32_16x16x32_bf16 v[36:39], a[208:211], v[156:159], v[36:39]\n\t" \
  "v_mfma_f32_16x16x32_bf16 v[36:39], a[228:231], v[172:175], v[36:39]\n\t" \
  "buffer_load_dwordx4 v248, s[44:47], s61 offen offset:2048 sc1 lds\n\t" \
  "v_mfma_f32_16x16x32_bf16 v[56:59], a[208:211], v[160:163], v[56:59]\n\t" \
  "s_add_u32 s58, s58, 5120\n\t" \
  "v_mfma_f32_16x16x32_bf16 v[56:59], a[228:231], v[176:179], v[56:59]\n\t" \
  "v_mfma_f32_16x16x32_bf16 v[76:79], a[208:211], v[164:167], v[76:79]\n\t" \
  "s_add_u32 s60, s60, 4096\n\t" \
  "s_add_u32 s56, s60, s52\n\t" \
  "buffer_load_dwordx4 v[136:139], v248, s[44:47], s56 offen sc1\n\t" \
  "v_add_u32 v251, s58, v248\n\t" \
  "v_mfma_f32_16x16x32_bf16 v[76:79], a[228:231], v[180:183], v[76:79]\n\t" \
  "v_mfma_f32_16x16x32_bf16 v[20:23], a[212:215], v[152:155], v[20:23]\n\t" \
  "ds_read_b128 v[232:235], v249 offset:0\n\t" \
  "v_mfma_f32_16x16x32_bf16 v[20:23], a[232:235], v[168:171], v[20:23]\n\t" \
  "ds_read_b128 v[236:239], v249 offset:5120\n\t" \
  "buffer_load_dwordx4 v248, s[44:47], s61 offen offset:3072 sc1 lds\n\t" \
  "s_sub_u32 s58, s64, 3\n\ts_and_b32 s58, s58, 3\n\ts_cmp_eq_u32 s58, 3\n\ts_cselect_b32 s58, 7, s58\n\t" \
  "v_mfma_f32_16x16x32_bf16 v[40:43], a[212:215], v[156:159], v[40:43]\n\t" \
  "v_mfma_f32_16x16x32_bf16 v[40:43], a[232:235], v[172:175], v[40:43]\n\t" \
  "s_mul_i32 s58, s58, 20480\n\t" \
  "v_mfma_f32_16x16x32_bf16 v[60:63], a[212:215], v[160:163], v[60:63]\n\t" \
  "s_add_u32 s56, s60, s53\n\t" \
  "buffer_load_dwordx4 v[140:143], v248, s[44:47], s56 offen sc1\n\t" \
  "v_mfma_f32_16x16x32_bf16 v[60:63], a[232:235], v[176:179], v[60:63]\n\t" \
  "v_mfma_f32_16x16x32_bf16 v[80:83], a[212:215], v[164:167], v[80:83]\n\t" \
  "s_add_u32 s58, s58, s85\n\t" \
  "v_mfma_f32_16x16x32_bf16 v[80:83], a[232:235], v[180:183], v[80:83]\n\t" \
  "s_add_u32 s56, s60, s54\n\t" \
  "buffer_load_dwordx4 v[144:147], v248, s[44:47], s56 offen sc1\n\t" \
  "v_mfma_f32_16x16x32_bf16 v[24:27], a[216:219], v[152:155], v[24:27]\n\t" \
  "ds_read_b128 v[240:243], v249 offset:1024\n\t" \
  "s_add_u32 s58, s58, 10240\n\t" \
  "v_mfma_f32_16x16x32_bf16 v[24:27], a[236:239], v[168:171], v[24:27]\n\t" \
  "ds_read_b128 v[244:247], v249 offset:6144\n\t" \
  "v_mfma_f32_16x16x32_bf16 v[44:47], a[216:219], v[156:159], v[44:47]\n\t" \
  "s_add_u32 m0, s62, 14336\n\t" \
  "s_add_u32 s61, s68, 86016\n\t" \
  "buffer_load_dwordx4 v248, s[44:47], s61 offen offset:0 sc1 lds\n\t" \
  "v_mfma_f32_16x16x32_bf16 v[44:47], a[236:239], v[172:175], v[44:47]\n\t" \
  "v_add_u32 v254, s58, v248\n\t" \
  "v_mfma_f32_16x16x32_bf16 v[64:67], a[216:219], v[160:163], v[64:67]\n\t" \
  "v_mfma_f32_16x16x32_bf16 v[64:67], a[236:239], v[176:179], v[64:67]\n\t" \
  "s_add_u32 s56, s60, s55\n\t" \
  "buffer_load_dwordx4 v[148:151], v248, s[44:47], s56 offen sc1\n\t" \
  "v_lshlrev_b32 v253, 1, v253\n\t" \
  "v_mfma_f32_16x16x32_bf16 v[84:87], a[216:219], v[164:167], v[84:87]\n\t" \
  "v_mfma_f32_16x16x32_bf16 v[84:87], a[236:239], v[180:183], v[84:87]\n\t" \
  "s_branch P2_F_%=\n\t" \
  "P2_E_%=:\n\t" \
  "s_waitcnt vmcnt(23)\n\t" \
  "v_mfma_f32_16x16x32_bf16 a[0:3], a[160:163], v[120:123], a[0:3]\n\t" \
  "v_accvgpr_read_b32 v99, a249\n\t" \
  "s_waitcnt vmcnt(16)\n\t" \
  "v_mfma_f32_16x16x32_bf16 a[0:3], a[180:183], v[136:139], a[0:3]\n\t" \
  "s_mov_b32 s60, s71\n\t" \
  "s_add_u32 s56, s60, s52\n\t" \
  "buffer_load_dwordx4 v[184:187], v248, s[44:47], s56 offen sc1\n\t" \
  "v_lshlrev_b32 v100, 16, v93\n\t" \
  "v_mfma_f32_16x16x32_bf16 a[20:23], a[160:163], v[124:127], a[20:23]\n\t" \
  "v_and_b32 v101, 0xffff0000, v93\n\t" \
  "s_waitcnt vmcnt(16)\n\t" \
  "v_mfma_f32_16x16x32_bf16 a[20:23], a[180:183], v[140:143], a[20:23]\n\t" \
  "v_lshlrev_b32 v98, 16, v99\n\t" \
  "v_mfma_f32_16x16x32_bf16 a[40:43], a[160:163], v[128:131], a[40:43]\n\t" \
  "s_add_u32 m0, s62, 9216\n\t" \
  "s_add_u32 s61, s68, 45056\n\t" \
  "buffer_load_dwordx4 v248, s[44:47], s61 offen offset:0 sc1 lds\n\t" \
  "v_and_b32 v99, 0xffff0000, v99\n\t" \
  "s_waitcnt vmcnt(15)\n\t" \
  "v_mfma_f32_16x16x32_bf16 a[40:43], a[180:183], v[144:147], a[40:43]\n\t" \
  "v_mul_f32 v98, v98, v100\n\t" \
  "v_mfma_f32_16x16x32_bf16 a[60:63], a[160:163], v[132:135], a[60:63]\n\t" \
  "v_mul_f32 v99, v99, v101\n\t" \
  "s_waitcnt vmcnt(14)\n\t" \
  "v_mfma_f32_16x16x32_bf16 a[60:63], a[180:183], v[148:151], a[60:63]\n\t" \
  "s_add_u32 s56, s60, s53\n\t" \
  "buffer_load_dwordx4 v[188:191], v248, s[44:47], s56 offen sc1\n\t" \
  "v_cvt_pk_bf16_f32 v93, v98, v99\n\t" \
  "v_mfma_f32_16x16x32_bf16 a[4:7], a[164:167], v[120:123], a[4:7]\n\t" \
  "v_accvgpr_read_b32 v99, a250\n\t" \
  "v_mfma_f32_16x16x32_bf16 a[4:7], a[184:187], v[136:139], a[4:7]\n\t" \
  "v_lshlrev_b32 v100, 16, v94\n\t" \
  "v_mfma_f32_16x16x32_bf16 a[24:27], a[164:167], v[124:127], a[24:27]\n\t" \
  "s_lshr_b32 s57, s52, 3\n\t" \
  "s_add_u32 s57, s57, s80\n\t" \
  "buffer_load_dwordx2 v[104:105], v255, s[44:47], s57 offen sc1\n\t" \
  "v_and_b32 v101, 0xffff0000, v94\n\t" \
  "v_mfma_f32_16x16x32_bf16 a[24:27], a[184:187], v[140:143], a[24:27]\n\t" \
  "v_lshlrev_b32 v98, 16, v99\n\t" \
  "v_mfma_f32_16x16x32_bf16 a[44:47], a[164:167], v[128:131], a[44:47]\n\t" \
  "v_and_b32 v99, 0xffff0000, v99\n\t" \
  "v_mfma_f32_16x16x32_bf16 a[44:47], a[184:187], v[144:147], a[44:47]\n\t" \
  "s_add_u32 s56, s60, s54\n\t" \
  "buffer_load_dwordx4 v[192:195], v248, s[44:47], s56 offen sc1\n\t" \
  "v_mul_f32 v98, v98, v100\n\t" \
  "v_mfma_f32_16x16x32_bf16 a[64:67], a[164:167], v[132:135], a[64:67]\n\t" \
  "v_mul_f32 v99, v99, v101\n\t" \
  "v_mfma_f32_16x16x32_bf16 a[64:67], a[184:187], v[148:151], a[64:67]\n\t" \
  "v_cvt_pk_bf16_f32 v94, v98, v99\n\t" \
  "v_mfma_f32_16x16x32_bf16 a[8:11], a[168:171], v[120:123], a[8:11]\n\t" \
  "s_add_u32 s56, s60, s55\n\t" \
  "buffer_load_dwordx4 v[196:199], v248, s[44:47], s56 offen sc1\n\t" \
  "v_accvgpr_read_b32 v99, a251\n\t" \
  "v_mfma_f32_16x16x32_bf16 a[8:11], a[188:191], v[136:139], a[8:11]\n\t" \
  "v_lshlrev_b32 v100, 16, v95\n\t" \
  "v_mfma_f32_16x16x32_bf16 a[28:31], a[168:171], v[124:127], a[28:31]\n\t" \
  "v_and_b32 v101, 0xffff0000, v95\n\t" \
  "v_mfma_f32_16x16x32_bf16 a[28:31], a[188:191], v[140:143], a[28:31]\n\t" \
  "s_lshr_b32 s57, s53, 3\n\t" \
  "s_add_u32 s57, s57, s80\n\t" \
  "buffer_load_dwordx2 v[106:107], v255, s[44:47], s57 offen sc1\n\t" \
  "v_lshlrev_b32 v98, 16, v99\n\t" \
  "v_mfma_f32_16x16x32_bf16 a[48:51], a[168:171], v[128:131], a[48:51]\n\t" \
  "v_and_b32 v99, 0xffff0000, v99\n\t" \
  "v_mfma_f32_16x16x32_bf16 a[48:51], a[188:191], v[144:147], a[48:51]\n\t" \
  "v_mul_f32 v98, v98, v100\n\t" \
  "v_mfma_f32_16x16x32_bf16 a[68:71], a[168:171], v[132:135], a[68:71]\n\t" \
  "s_add_u32 s60, s60, 4096\n\t" \
  "s_add_u32 s56, s60, s52\n\t" \
  "buffer_load_dwordx4 v[200:203], v248, s[44:47], s56 offen sc1\n\t" \
  "v_mul_f32 v99, v99, v101\n\t" \
  "v_mfma_f32_16x16x32_bf16 a[68:71], a[188:191], v[148:151], a[68:71]\n\t" \
  "v_cvt_pk_bf16_f32 v95, v98, v99\n\t" \
  "v_mfma_f32_16x16x32_bf16 a[12:15], a[172:175], v[120:123], a[12:15]\n\t" \
  "v_accvgpr_read_b32 v99, a252\n\t" \
  "v_mfma_f32_16x16x32_bf16 a[12:15], a[192:195], v[136:139], a[12:15]\n\t" \
  "s_lshr_b32 s57, s54, 3\n\t" \
  "s_add_u32 s57, s57, s80\n\t" \
  "buffer_load_dwordx2 v[108:109], v255, s[44:47], s57 offen sc1\n\t" \
  "v_lshlrev_b32 v100, 16, v96\n\t" \
  "v_mfma_f32_16x16x32_bf16 a[32:35], a[172:175], v[124:127], a[32:35]\n\t" \
  "v_and_b32 v101, 0xffff0000, v96\n\t" \
  "v_mfma_f32_16x16x32_bf16 a[32:35], a[192:195], v[140:143], a[32:35]\n\t" \
  "v_lshlrev_b32 v98, 16, v99\n\t" \
  "v_mfma_f32_16x16x32_bf16 a[52:55], a[172:175], v[128:131], a[52:55]\n\t" \
  "s_add_u32 s56, s60, s53\n\t" \
  "buffer_load_dwordx4 v[204:207], v248, s[44:47], s56 offen sc1\n\t" \
  "v_and_b32 v99, 0xffff0000, v99\n\t" \
  "v_mfma_f32_16x16x32_bf16 a[52:55], a[192:195], v[144:147], a[52:55]\n\t" \
  "v_mul_f32 v98, v98, v100\n\t" \
  "v_mfma_f32_16x16x32_bf16 a[72:75], a[172:175], v[132:135], a[72:75]\n\t" \
  "v_mul_f32 v99, v99, v101\n\t" \
  "v_mfma_f32_16x16x32_bf16 a[72:75], a[192:195], v[148:151], a[72:75]\n\t" \
  "s_add_u32 s56, s60, s54\n\t" \
  "buffer_load_dwordx4 v[208:211], v248, s[44:47], s56 offen sc1\n\t" \
  "v_cvt_pk_bf16_f32 v96, v98, v99\n\t" \
  "v_mfma_f32_16x16x32_bf16 a[16:19], a[176:179], v[120:123], a[16:19]\n\t" \
  "v_accvgpr_read_b32 v99, a253\n\t" \
  "v_mfma_f32_16x16x32_bf16 a[16:19], a[196:199], v[136:139], a[16:19]\n\t" \
  "v_lshlrev_b32 v100, 16, v97\n\t" \
  "v_mfma_f32_16x16x32_bf16 a[36:39], a[176:179], v[124:127], a[36:39]\n\t" \
  "s_lshr_b32 s57, s55, 3\n\t" \
  "s_add_u32 s57, s57, s80\n\t" \
  "buffer_load_dwordx2 v[110:111], v255, s[44:47], s57 offen sc1\n\t" \
  "v_and_b32 v101, 0xffff0000, v97\n\t" \
  "v_mfma_f32_16x16x32_bf16 a[36:39], a[196:199], v[140:143], a[36:39]\n\t" \
  "v_lshlrev_b32 v98, 16, v99\n\t" \
  "v_mfma_f32_16x16x32_bf16 a[56:59], a[176:179], v[128:131], a[56:59]\n\t" \
  "v_and_b32 v99, 0xffff0000, v99\n\t" \
  "v_mfma_f32_16x16x32_bf16 a[56:59], a[196:199], v[144:147], a[56:59]\n\t" \
  "s_add_u32 s56, s60, s55\n\t" \
  "buffer_load_dwordx4 v[212:215], v248, s[44:47], s56 offen sc1\n\t" \
  "v_mul_f32 v98, v98, v100\n\t" \
  "v_mfma_f32_16x16x32_bf16 a[76:79], a[176:179], v[132:135], a[76:79]\n\t" \
  "v_mul_f32 v99, v99, v101\n\t" \
  "v_mfma_f32_16x16x32_bf16 a[76:79], a[196:199], v[148:151], a[76:79]\n\t" \
  "v_cvt_pk_bf16_f32 v97, v98, v99\n\t" \
  "s_waitcnt vmcnt(24)\n\t" \
  "v_mfma_f32_16x16x32_bf16 a[0:3], a[200:203], v[152:155], a[0:3]\n\t" \
  "s_sub_u32 s58, s64, 1\n\ts_and_b32 s58, s58, 3\n\ts_cmp_eq_u32 s58, 3\n\ts_cselect_b32 s58, 7, s58\n\t" \
  "s_waitcnt vmcnt(17)\n\t" \
  "v_mfma_f32_16x16x32_bf16 a[0:3], a[220:223], v[168:171], a[0:3]\n\t" \
  "s_add_u32 s60, s60, 4096\n\t" \
  "s_add_u32 s56, s60, s52\n\t" \
  "buffer_load_dwordx4 v[120:123], v248, s[44:47], s56 offen sc1\n\t" \
  "v_mfma_f32_16x16x32_bf16 a[20:23], a[200:203], v[156:159], a[20:23]\n\t" \
  "s_mul_i32 s58, s58, 20480\n\t" \
  "s_waitcnt vmcnt(17)\n\t" \
  "v_mfma_f32_16x16x32_bf16 a[20:23], a[220:223], v[172:175], a[20:23]\n\t" \
  "v_mfma_f32_16x16x32_bf16 a[40:43], a[200:203], v[160:163], a[40:43]\n\t" \
  "s_add_u32 m0, s62, 10240\n\t" \
  "s_add_u32 s61, s68, 81920\n\t" \
  "buffer_load_dwordx4 v248, s[44:47], s61 offen offset:0 sc1 lds\n\t" \
  "s_waitcnt vmcnt(16)\n\t" \
  "v_mfma_f32_16x16x32_bf16 a[40:43], a[220:223], v[176:179], a[40:43]\n\t" \
  "s_add_u32 s58, s58, s85\n\t" \
  "v_mfma_f32_16x16x32_bf16 a[60:63], a[200:203], v[164:167], a[60:63]\n\t" \
  "s_waitcnt vmcnt(15)\n\t" \
  "v_mfma_f32_16x16x32_bf16 a[60:63], a[220:223], v[180:183], a[60:63]\n\t" \
  "s_add_u32 s56, s60, s53\n\t" \
  "buffer_load_dwordx4 v[124:127], v248, s[44:47], s56 offen sc1\n\t" \
  "s_add_u32 s58, s58, 0\n\t" \
  "v_mfma_f32_16x16x32_bf16 a[4:7], a[204:207], v[152:155], a[4:7]\n\t" \
  "v_mfma_f32_16x16x32_bf16 a[4:7], a[224:227], v[168:171], a[4:7]\n\t" \
  "v_mfma_f32_16x16x32_bf16 a[24:27], a[204:207], v[156:159], a[24:27]\n\t" \
  "buffer_load_dwordx4 v248, s[44:47], s61 offen offset:1024 sc1 lds\n\t" \
  "v_add_u32 v250, s58, v248\n\t" \
  "v_mfma_f32_16x16x32_bf16 a[24:27], a[224:227], v[172:175], a[24:27]\n\t" \
  "v_mfma_f32_16x16x32_bf16 a[44:47], a[204:207], v[160:163], a[44:47]\n\t" \
  "s_sub_u32 s58, s64, 2\n\ts_and_b32 s58, s58, 3\n\ts_cmp_eq_u32 s58, 3\n\ts_cselect_b32 s58, 7, s58\n\t" \
  "v_mfma_f32_16x16x32_bf16 a[44:47], a[224:227], v[176:179], a[44:47]\n\t" \
  "s_add_u32 s56, s60, s54\n\t" \
  "buffer_load_dwordx4 v[128:131], v248, s[44:47], s56 offen sc1\n\t" \
  "v_mfma_f32_16x16x32_bf16 a[64:67], a[204:207], v[164:167], a[64:67]\n\t" \
  "v_mfma_f32_16x16x32_bf16 a[64:67], a[224:227], v[180:183], a[64:67]\n\t" \
  "s_mul_i32 s58, s58, 20480\n\t" \
  "v_mfma_f32_16x16x32_bf16 a[8:11], a[208:211], v[152:155], a[8:11]\n\t" \
  "s_add_u32 s56, s60, s55\n\t" \
  "buffer_load_dwordx4 v[132:135], v248, s[44:47], s56 offen sc1\n\t" \
  "v_mfma_f32_16x16x32_bf16 a[8:11], a[228:231], v[168:171], a[8:11]\n\t" \
  "s_add_u32 s58, s58, s85\n\t" \
  "v_mfma_f32_16x16x32_bf16 a[28:31], a[208:211], v[156:159], a[28:31]\n\t" \
  "v_mfma_f32_16x16x32_bf16 a[28:31], a[228:231], v[172:175], a[28:31]\n\t" \
  "buffer_load_dwordx4 v248, s[44:47], s61 offen offset:2048 sc1 lds\n\t" \
  "v_mfma_f32_16x16x32_bf16 a[48:51], a[208:211], v[160:163], a[48:51]\n\t" \
  "s_add_u32 s58, s58, 5120\n\t" \
  "v_mfma_f32_16x16x32_bf16 a[48:51], a[228:231], v[176:179], a[48:51]\n\t" \
  "v_mfma_f32_16x16x32_bf16 a[68:71], a[208:211], v[164:167], a[68:71]\n\t" \
  "s_add_u32 s60, s60, 4096\n\t" \
  "s_add_u32 s56, s60, s52\n\t" \
  "buffer_load_dwordx4 v[136:139], v248, s[44:47], s56 offen sc1\n\t" \
  "v_add_u32 v251, s58, v248\n\t" \
  "v_mfma_f32_16x16x32_bf16 a[68:71], a[228:231], v[180:183], a[68:71]\n\t" \
  "v_mfma_f32_16x16x32_bf16 a[12:15], a[212:215], v[152:155], a[12:15]\n\t" \
  "ds_read_b128 v[232:235], v249 offset:0\n\t" \
  "v_mfma_f32_16x16x32_bf16 a[12:15], a[232:235], v[168:171], a[12:15]\n\t" \
  "ds_read_b128 v[236:239], v249 offset:5120\n\t" \
  "buffer_load_dwordx4 v248, s[44:47], s61 offen offset:3072 sc1 lds\n\t" \
  "s_sub_u32 s58, s64, 3\n\ts_and_b32 s58, s58, 3\n\ts_cmp_eq_u32 s58, 3\n\ts_cselect_b32 s58, 7, s58\n\t" \
  "v_mfma_f32_16x16x32_bf16 a[32:35], a[212:215], v[156:159], a[32:35]\n\t" \
  "v_mfma_f32_16x16x32_bf16 a[32:35], a[232:235], v[172:175], a[32:35]\n\t" \
  "s_mul_i32 s58, s58, 20480\n\t" \
  "v_mfma_f32_16x16x32_bf16 a[52:55], a[212:215], v[160:163], a[52:55]\n\t" \
  "s_add_u32 s56, s60, s53\n\t" \
  "buffer_load_dwordx4 v[140:143], v248, s[44:47], s56 offen sc1\n\t" \
  "v_mfma_f32_16x16x32_bf16 a[52:55], a[232:235], v[176:179], a[52:55]\n\t" \
  "v_mfma_f32_16x16x32_bf16 a[72:75], a[212:215], v[164:167], a[72:75]\n\t" \
  "s_add_u32 s58, s58, s85\n\t" \
  "v_mfma_f32_16x16x32_bf16 a[72:75], a[232:235], v[180:183], a[72:75]\n\t" \
  "s_add_u32 s56, s60, s54\n\t" \
  "buffer_load_dwordx4 v[144:147], v248, s[44:47], s56 offen sc1\n\t" \
  "v_mfma_f32_16x16x32_bf16 a[16:19], a[216:219], v[152:155], a[16:19]\n\t" \
  "ds_read_b128 v[240:243], v249 offset:1024\n\t" \
  "s_add_u32 s58, s58, 10240\n\t" \
  "v_mfma_f32_16x16x32_bf16 a[16:19], a[236:239], v[168:171], a[16:19]\n\t" \
  "ds_read_b128 v[244:247], v249 offset:6144\n\t" \
  "v_mfma_f32_16x16x32_bf16 a[36:39], a[216:219], v[156:159], a[36:39]\n\t" \
  "s_add_u32 m0, s62, 14336\n\t" \
  "s_add_u32 s61, s68, 86016\n\t" \
  "buffer_load_dwordx4 v248, s[44:47], s61 offen offset:0 sc1 lds\n\t" \
  "v_mfma_f32_16x16x32_bf16 a[36:39], a[236:239], v[172:175], a[36:39]\n\t" \
  "v_add_u32 v254, s58, v248\n\t" \
  "v_mfma_f32_16x16x32_bf16 a[56:59], a[216:219], v[160:163], a[56:59]\n\t" \
  "v_mfma_f32_16x16x32_bf16 a[56:59], a[236:239], v[176:179], a[56:59]\n\t" \
  "s_add_u32 s56, s60, s55\n\t" \
  "buffer_load_dwordx4 v[148:151], v248, s[44:47], s56 offen sc1\n\t" \
  "v_lshlrev_b32 v253, 1, v253\n\t" \
  "v_mfma_f32_16x16x32_bf16 a[76:79], a[216:219], v[164:167], a[76:79]\n\t" \
  "v_mfma_f32_16x16x32_bf16 a[76:79], a[236:239], v[180:183], a[76:79]\n\t" \
  "P2_F_%=:\n\t" \
  "s_waitcnt vmcnt(14)\n\t" \
  "v_min3_u32 v98, v105, v107, v109\n\t" \
  "v_max3_u32 v99, v105, v107, v109\n\t" \
  "v_min_u32 v98, v98, v111\n\t" \
  "v_max_u32 v99, v99, v111\n\t" \
  "v_cmp_ne_u32 vcc, s72, v98\n\t" \
  "v_cmp_ne_u32 s[58:59], s72, v99\n\t" \
  "s_nop 1\n\t" \
  "s_or_b64 vcc, vcc, s[58:59]\n\t" \
  "s_cbranch_vccz P2_GX_%=\n\t" \
  "P2_G_%=:\n\t" \
  "s_lshr_b32 s57, s52, 3\n\t" \
  "s_add_u32 s57, s57, s80\n\t" \
  "buffer_load_dwordx2 v[104:105], v255, s[44:47], s57 offen sc1\n\t" \
  "s_lshr_b32 s57, s53, 3\n\t" \
  "s_add_u32 s57, s57, s80\n\t" \
  "buffer_load_dwordx2 v[106:107], v255, s[44:47], s57 offen sc1\n\t" \
  "s_lshr_b32 s57, s54, 3\n\t" \
  "s_add_u32 s57, s57, s80\n\t" \
  "buffer_load_dwordx2 v[108:109], v255, s[44:47], s57 offen sc1\n\t" \
  "s_lshr_b32 s57, s55, 3\n\t" \
  "s_add_u32 s57, s57, s80\n\t" \
  "buffer_load_dwordx2 v[110:111], v255, s[44:47], s57 offen sc1\n\t" \
  "s_add_u32 s63, s63, 1\n\t" \
  "s_cmp_gt_u32 s63, 1048576\n\t" \
  "s_waitcnt vmcnt(0)\n\t" \
  "s_cbranch_scc1 P2_GX_%=\n\t" \
  "v_min3_u32 v98, v105, v107, v109\n\t" \
  "v_max3_u32 v99, v105, v107, v109\n\t" \
  "v_min_u32 v98, v98, v111\n\t" \
  "v_max_u32 v99, v99, v111\n\t" \
  "v_cmp_ne_u32 vcc, s72, v98\n\t" \
  "v_cmp_ne_u32 s[58:59], s72, v99\n\t" \
  "s_nop 1\n\t" \
  "s_or_b64 vcc, vcc, s[58:59]\n\t" \
  "s_cbranch_vccnz P2_G_%=\n\t" \
  "P2_GX_%=:\n\t" \
  "v_sub_f32 v105, 1.0, v104\n\t" \
  "v_sub_f32 v107, 1.0, v106\n\t" \
  "v_sub_f32 v109, 1.0, v108\n\t" \
  "v_sub_f32 v111, 1.0, v110\n\t" \
  "v_cvt_pk_bf16_f32 v105, v105, v105\n\t" \
  "v_cvt_pk_bf16_f32 v107, v107, v107\n\t" \
  "v_cvt_pk_bf16_f32 v109, v109, v109\n\t" \
  "v_cvt_pk_bf16_f32 v111, v111, v111\n\t" \
  "v_lshlrev_b32 v105, 16, v105\n\t" \
  "v_lshlrev_b32 v107, 16, v107\n\t" \
  "v_lshlrev_b32 v109, 16, v109\n\t" \
  "v_lshlrev_b32 v111, 16, v111\n\t" \
  "s_waitcnt lgkmcnt(3)\n\t" \
  "v_mfma_f32_16x16x32_bf16 a[0:3], v[232:235], v[184:187], a[0:3]\n\t" \
  "ds_read_b128 v[216:219], v249 offset:2048\n\t" \
  "v_mul_f32 v8, v105, v8\n\t" \
  "s_waitcnt lgkmcnt(3)\n\t" \
  "v_mfma_f32_16x16x32_bf16 a[0:3], v[236:239], v[200:203], a[0:3]\n\t" \
  "ds_read_b128 v[220:223], v249 offset:7168\n\t" \
  "s_add_u32 s60, s60, 4096\n\t" \
  "s_add_u32 s56, s60, s52\n\t" \
  "buffer_load_dwordx4 v[152:155], v248, s[44:47], s56 offen sc1\n\t" \
  "v_mul_f32 v9, v105, v9\n\t" \
  "v_mfma_f32_16x16x32_bf16 a[20:23], v[232:235], v[188:191], a[20:23]\n\t" \
  "v_mul_f32 v10, v105, v10\n\t" \
  "v_mfma_f32_16x16x32_bf16 a[20:23], v[236:239], v[204:207], a[20:23]\n\t" \
  "v_mul_f32 v11, v105, v11\n\t" \
  "v_mfma_f32_16x16x32_bf16 a[40:43], v[232:235], v[192:195], a[40:43]\n\t" \
  "s_add_u32 m0, s62, 15360\n\t" \
  "s_add_u32 s61, s68, 122880\n\t" \
  "buffer_load_dwordx4 v248, s[44:47], s61 offen offset:0 sc1 lds\n\t" \
  "v_mul_f32 v12, v105, v12\n\t" \
  "v_mfma_f32_16x16x32_bf16 a[40:43], v[236:239], v[208:211], a[40:43]\n\t" \
  "v_mul_f32 v13, v105, v13\n\t" \
  "v_mfma_f32_16x16x32_bf16 a[60:63], v[232:235], v[196:199], a[60:63]\n\t" \
  "v_mul_f32 v14, v105, v14\n\t" \
  "s_waitcnt vmcnt(15)\n\t" \
  "v_mfma_f32_16x16x32_bf16 a[60:63], v[236:239], v[212:215], a[60:63]\n\t" \
  "s_add_u32 s56, s60, s53\n\t" \
  "buffer_load_dwordx4 v[156:159], v248, s[44:47], s56 offen sc1\n\t" \
  "v_mul_f32 v15, v105, v15\n\t" \
  "s_waitcnt lgkmcnt(3)\n\t" \
  "v_mfma_f32_16x16x32_bf16 a[4:7], v[240:243], v[184:187], a[4:7]\n\t" \
  "ds_read_b128 v[224:227], v249 offset:3072\n\t" \
  "v_mul_f32 v16, v105, v16\n\t" \
  "s_waitcnt lgkmcnt(3)\n\t" \
  "v_mfma_f32_16x16x32_bf16 a[4:7], v[244:247], v[200:203], a[4:7]\n\t" \
  "ds_read_b128 v[228:231], v249 offset:8192\n\t" \
  "v_mul_f32 v17, v105, v17\n\t" \
  "v_mfma_f32_16x16x32_bf16 a[24:27], v[240:243], v[188:191], a[24:27]\n\t" \
  "buffer_load_dwordx4 v248, s[44:47], s61 offen offset:1024 sc1 lds\n\t" \
  "v_mul_f32 v18, v105, v18\n\t" \
  "v_mfma_f32_16x16x32_bf16 a[24:27], v[244:247], v[204:207], a[24:27]\n\t" \
  "v_mul_f32 v19, v105, v19\n\t" \
  "v_mfma_f32_16x16x32_bf16 a[44:47], v[240:243], v[192:195], a[44:47]\n\t" \
  "v_mul_f32 v20, v105, v20\n\t" \
  "v_mfma_f32_16x16x32_bf16 a[44:47], v[244:247], v[208:211], a[44:47]\n\t" \
  "s_add_u32 s56, s60, s54\n\t" \
  "buffer_load_dwordx4 v[160:163], v248, s[44:47], s56 offen sc1\n\t" \
  "v_mul_f32 v21, v105, v21\n\t" \
  "v_mfma_f32_16x16x32_bf16 a[64:67], v[240:243], v[196:199], a[64:67]\n\t" \
  "v_mul_f32 v22, v105, v22\n\t" \
  "v_mfma_f32_16x16x32_bf16 a[64:67], v[244:247], v[212:215], a[64:67]\n\t" \
  "v_mul_f32 v23, v105, v23\n\t" \
  "s_waitcnt lgkmcnt(3)\n\t" \
  "v_mfma_f32_16x16x32_bf16 a[8:11], v[216:219], v[184:187], a[8:11]\n\t" \
  "ds_read_b128 v[232:235], v249 offset:4096\n\t" \
  "s_add_u32 s56, s60, s55\n\t" \
  "buffer_load_dwordx4 v[164:167], v248, s[44:47], s56 offen sc1\n\t" \
  "v_mul_f32 v24, v105, v24\n\t" \
  "s_waitcnt lgkmcnt(3)\n\t" \
  "v_mfma_f32_16x16x32_bf16 a[8:11], v[220:223], v[200:203], a[8:11]\n\t" \
  "ds_read_b128 v[236:239], v249 offset:9216\n\t" \
  "v_mul_f32 v25, v105, v25\n\t" \
  "v_mfma_f32_16x16x32_bf16 a[28:31], v[216:219], v[188:191], a[28:31]\n\t" \
  "v_mul_f32 v26, v105, v26\n\t" \
  "v_mfma_f32_16x16x32_bf16 a[28:31], v[220:223], v[204:207], a[28:31]\n\t" \
  "buffer_load_dwordx4 v248, s[44:47], s61 offen offset:2048 sc1 lds\n\t" \
  "v_mul_f32 v27, v105, v27\n\t" \
  "v_mfma_f32_16x16x32_bf16 a[48:51], v[216:219], v[192:195], a[48:51]\n\t" \
  "v_mul_f32 v28, v107, v28\n\t" \
  "v_mfma_f32_16x16x32_bf16 a[48:51], v[220:223], v[208:211], a[48:51]\n\t" \
  "v_mul_f32 v29, v107, v29\n\t" \
  "v_mfma_f32_16x16x32_bf16 a[68:71], v[216:219], v[196:199], a[68:71]\n\t" \
  "s_add_u32 s60, s60, 4096\n\t" \
  "s_add_u32 s56, s60, s52\n\t" \
  "buffer_load_dwordx4 v[168:171], v248, s[44:47], s56 offen sc1\n\t" \
  "v_mul_f32 v30, v107, v30\n\t" \
  "v_mfma_f32_16x16x32_bf16 a[68:71], v[220:223], v[212:215], a[68:71]\n\t" \
  "v_mul_f32 v31, v107, v31\n\t" \
  "s_waitcnt lgkmcnt(3)\n\t" \
  "v_mfma_f32_16x16x32_bf16 a[12:15], v[224:227], v[184:187], a[12:15]\n\t" \
  "ds_read_b128 v[240:243], v249 offset:10240\n\t" \
  "v_mul_f32 v32, v107, v32\n\t" \
  "s_waitcnt lgkmcnt(3)\n\t" \
  "v_mfma_f32_16x16x32_bf16 a[12:15], v[228:231], v[200:203], a[12:15]\n\t" \
  "ds_read_b128 v[244:247], v249 offset:15360\n\t" \
  "buffer_load_dwordx4 v248, s[44:47], s61 offen offset:3072 sc1 lds\n\t" \
  "v_mul_f32 v33, v107, v33\n\t" \
  "v_mfma_f32_16x16x32_bf16 a[32:35], v[224:227], v[188:191], a[32:35]\n\t" \
  "v_mul_f32 v34, v107, v34\n\t" \
  "v_mfma_f32_16x16x32_bf16 a[32:35], v[228:231], v[204:207], a[32:35]\n\t" \
  "v_mul_f32 v35, v107, v35\n\t" \
  "v_mfma_f32_16x16x32_bf16 a[52:55], v[224:227], v[192:195], a[52:55]\n\t" \
  "s_add_u32 s56, s60, s53\n\t" \
  "buffer_load_dwordx4 v[172:175], v248, s[44:47], s56 offen sc1\n\t" \
  "v_mul_f32 v36, v107, v36\n\t" \
  "v_mfma_f32_16x16x32_bf16 a[52:55], v[228:231], v[208:211], a[52:55]\n\t" \
  "v_mul_f32 v37, v107, v37\n\t" \
  "v_mfma_f32_16x16x32_bf16 a[72:75], v[224:227], v[196:199], a[72:75]\n\t" \
  "v_mul_f32 v38, v107, v38\n\t" \
  "v_mfma_f32_16x16x32_bf16 a[72:75], v[228:231], v[212:215], a[72:75]\n\t" \
  "s_add_u32 s56, s60, s54\n\t" \
  "buffer_load_dwordx4 v[176:179], v248, s[44:47], s56 offen sc1\n\t" \
  "v_mul_f32 v39, v107, v39\n\t" \
  "s_waitcnt lgkmcnt(3)\n\t" \
  "v_mfma_f32_16x16x32_bf16 a[16:19], v[232:235], v[184:187], a[16:19]\n\t" \
  "ds_read_b128 v[216:219], v249 offset:11264\n\t" \
  "v_mul_f32 v40, v107, v40\n\t" \
  "s_waitcnt lgkmcnt(3)\n\t" \
  "v_mfma_f32_16x16x32_bf16 a[16:19], v[236:239], v[200:203], a[16:19]\n\t" \
  "ds_read_b128 v[220:223], v249 offset:16384\n\t" \
  "v_mul_f32 v41, v107, v41\n\t" \
  "v_mfma_f32_16x16x32_bf16 a[36:39], v[232:235], v[188:191], a[36:39]\n\t" \
  "s_add_u32 m0, s62, 19456\n\t" \
  "s_add_u32 s61, s68, 126976\n\t" \
  "buffer_load_dwordx4 v248, s[44:47], s61 offen offset:0 sc1 lds\n\t" \
  "v_mul_f32 v42, v107, v42\n\t" \
  "v_mfma_f32_16x16x32_bf16 a[36:39], v[236:239], v[204:207], a[36:39]\n\t" \
  "v_mul_f32 v43, v107, v43\n\t" \
  "v_mfma_f32_16x16x32_bf16 a[56:59], v[232:235], v[192:195], a[56:59]\n\t" \
  "v_mul_f32 v44, v107, v44\n\t" \
  "v_mfma_f32_16x16x32_bf16 a[56:59], v[236:239], v[208:211], a[56:59]\n\t" \
  "s_add_u32 s56, s60, s55\n\t" \
  "buffer_load_dwordx4 v[180:183], v248, s[44:47], s56 offen sc1\n\t" \
  "v_mul_f32 v45, v107, v45\n\t" \
  "v_mfma_f32_16x16x32_bf16 a[76:79], v[232:235], v[196:199], a[76:79]\n\t" \
  "v_mul_f32 v46, v107, v46\n\t" \
  "v_mfma_f32_16x16x32_bf16 a[76:79], v[236:239], v[212:215], a[76:79]\n\t" \
  "v_mul_f32 v47, v107, v47\n\t" \
  "s_waitcnt vmcnt(25) lgkmcnt(3)\n\t" \
  "v_mfma_f32_16x16x32_bf16 a[0:3], v[240:243], v[120:123], a[0:3]\n\t" \
  "ds_read_b128 v[224:227], v249 offset:12288\n\t" \
  "v_mul_f32 v48, v109, v48\n\t" \
  "s_waitcnt vmcnt(18) lgkmcnt(3)\n\t" \
  "v_mfma_f32_16x16x32_bf16 a[0:3], v[244:247], v[136:139], a[0:3]\n\t" \
  "ds_read_b128 v[228:231], v249 offset:17408\n\t" \
  "s_add_u32 s60, s60, 4096\n\t" \
  "s_add_u32 s56, s60, s52\n\t" \
  "buffer_load_dwordx4 v[184:187], v248, s[44:47], s56 offen sc1\n\t" \
  "v_mul_f32 v49, v109, v49\n\t" \
  "v_mfma_f32_16x16x32_bf16 a[20:23], v[240:243], v[124:127], a[20:23]\n\t" \
  "v_mul_f32 v50, v109, v50\n\t" \
  "s_waitcnt vmcnt(17)\n\t" \
  "v_mfma_f32_16x16x32_bf16 a[20:23], v[244:247], v[140:143], a[20:23]\n\t" \
  "v_mul_f32 v51, v109, v51\n\t" \
  "v_mfma_f32_16x16x32_bf16 a[40:43], v[240:243], v[128:131], a[40:43]\n\t" \
  "v_mul_f32 v52, v109, v52\n\t" \
  "s_waitcnt vmcnt(16)\n\t" \
  "v_mfma_f32_16x16x32_bf16 a[40:43], v[244:247], v[144:147], a[40:43]\n\t" \
  "s_add_u32 s56, s60, s53\n\t" \
  "buffer_load_dwordx4 v[188:191], v248, s[44:47], s56 offen sc1\n\t" \
  "v_mul_f32 v53, v109, v53\n\t" \
  "v_mfma_f32_16x16x32_bf16 a[60:63], v[240:243], v[132:135], a[60:63]\n\t" \
  "v_mul_f32 v54, v109, v54\n\t" \
  "s_waitcnt vmcnt(15)\n\t" \
  "v_mfma_f32_16x16x32_bf16 a[60:63], v[244:247], v[148:151], a[60:63]\n\t" \
  "v_mul_f32 v55, v109, v55\n\t" \
  "s_waitcnt lgkmcnt(3)\n\t" \
  "v_mfma_f32_16x16x32_bf16 a[4:7], v[216:219], v[120:123], a[4:7]\n\t" \
  "ds_read_b128 v[232:235], v249 offset:13312\n\t" \
  "v_mul_f32 v56, v109, v56\n\t" \
  "s_waitcnt lgkmcnt(3)\n\t" \
  "v_mfma_f32_16x16x32_bf16 a[4:7], v[220:223], v[136:139], a[4:7]\n\t" \
  "ds_read_b128 v[236:239], v249 offset:18432\n\t" \
  "v_mul_f32 v57, v109, v57\n\t" \
  "v_mfma_f32_16x16x32_bf16 a[24:27], v[216:219], v[124:127], a[24:27]\n\t" \
  "s_add_u32 s56, s60, s54\n\t" \
  "buffer_load_dwordx4 v[192:195], v248, s[44:47], s56 offen sc1\n\t" \
  "v_mul_f32 v58, v109, v58\n\t" \
  "v_mfma_f32_16x16x32_bf16 a[24:27], v[220:223], v[140:143], a[24:27]\n\t" \
  "v_mul_f32 v59, v109, v59\n\t" \
  "v_mfma_f32_16x16x32_bf16 a[44:47], v[216:219], v[128:131], a[44:47]\n\t" \
  "v_mul_f32 v60, v109, v60\n\t" \
  "v_mfma_f32_16x16x32_bf16 a[44:47], v[220:223], v[144:147], a[44:47]\n\t" \
  "v_mul_f32 v61, v109, v61\n\t" \
  "v_mfma_f32_16x16x32_bf16 a[64:67], v[216:219], v[132:135], a[64:67]\n\t" \
  "v_mul_f32 v62, v109, v62\n\t" \
  "v_mfma_f32_16x16x32_bf16 a[64:67], v[220:223], v[148:151], a[64:67]\n\t" \
  "s_add_u32 s56, s60, s55\n\t" \
  "buffer_load_dwordx4 v[196:199], v248, s[44:47], s56 offen sc1\n\t" \
  "v_mul_f32 v63, v109, v63\n\t" \
  "s_waitcnt lgkmcnt(3)\n\t" \
  "v_mfma_f32_16x16x32_bf16 a[8:11], v[224:227], v[120:123], a[8:11]\n\t" \
  "ds_read_b128 v[240:243], v249 offset:14336\n\t" \
  "v_mul_f32 v64, v109, v64\n\t" \
  "s_waitcnt lgkmcnt(3)\n\t" \
  "v_mfma_f32_16x16x32_bf16 a[8:11], v[228:231], v[136:139], a[8:11]\n\t" \
  "ds_read_b128 v[244:247], v249 offset:19456\n\t" \
  "v_mul_f32 v65, v109, v65\n\t" \
  "v_mfma_f32_16x16x32_bf16 a[28:31], v[224:227], v[124:127], a[28:31]\n\t" \
  "v_mul_f32 v66, v109, v66\n\t" \
  "v_mfma_f32_16x16x32_bf16 a[28:31], v[228:231], v[140:143], a[28:31]\n\t" \
  "v_mul_f32 v67, v109, v67\n\t" \
  "v_mfma_f32_16x16x32_bf16 a[48:51], v[224:227], v[128:131], a[48:51]\n\t" \
  "s_add_u32 s60, s60, 4096\n\t" \
  "s_add_u32 s56, s60, s52\n\t" \
  "buffer_load_dwordx4 v[200:203], v248, s[44:47], s56 offen sc1\n\t" \
  "v_mul_f32 v68, v111, v68\n\t" \
  "v_mfma_f32_16x16x32_bf16 a[48:51], v[228:231], v[144:147], a[48:51]\n\t" \
  "v_mul_f32 v69, v111, v69\n\t" \
  "v_mfma_f32_16x16x32_bf16 a[68:71], v[224:227], v[132:135], a[68:71]\n\t" \
  "v_mul_f32 v70, v111, v70\n\t" \
  "v_mfma_f32_16x16x32_bf16 a[68:71], v[228:231], v[148:151], a[68:71]\n\t" \
  "v_mul_f32 v71, v111, v71\n\t" \
  "s_waitcnt lgkmcnt(3)\n\t" \
  "v_mfma_f32_16x16x32_bf16 a[12:15], v[232:235], v[120:123], a[12:15]\n\t" \
  "s_waitcnt vmcnt(6)\n\t" \
  "ds_read_b128 v[216:219], v7 offset:0\n\t" \
  "v_mul_f32 v72, v111, v72\n\t" \
  "s_waitcnt lgkmcnt(3)\n\t" \
  "v_mfma_f32_16x16x32_bf16 a[12:15], v[236:239], v[136:139], a[12:15]\n\t" \
  "ds_read_b128 v[220:223], v7 offset:5120\n\t" \
  "s_add_u32 s56, s60, s53\n\t" \
  "buffer_load_dwordx4 v[204:207], v248, s[44:47], s56 offen sc1\n\t" \
  "v_mul_f32 v73, v111, v73\n\t" \
  "v_mfma_f32_16x16x32_bf16 a[32:35], v[232:235], v[124:127], a[32:35]\n\t" \
  "v_mul_f32 v74, v111, v74\n\t" \
  "v_mfma_f32_16x16x32_bf16 a[32:35], v[236:239], v[140:143], a[32:35]\n\t" \
  "v_mul_f32 v75, v111, v75\n\t" \
  "v_mfma_f32_16x16x32_bf16 a[52:55], v[232:235], v[128:131], a[52:55]\n\t" \
  "v_mul_f32 v76, v111, v76\n\t" \
  "v_mfma_f32_16x16x32_bf16 a[52:55], v[236:239], v[144:147], a[52:55]\n\t" \
  "v_mul_f32 v77, v111, v77\n\t" \
  "v_mfma_f32_16x16x32_bf16 a[72:75], v[232:235], v[132:135], a[72:75]\n\t" \
  "s_add_u32 s56, s60, s54\n\t" \
  "buffer_load_dwordx4 v[208:211], v248, s[44:47], s56 offen sc1\n\t" \
  "v_mul_f32 v78, v111, v78\n\t" \
  "v_mfma_f32_16x16x32_bf16 a[72:75], v[236:239], v[148:151], a[72:75]\n\t" \
  "v_mul_f32 v79, v111, v79\n\t" \
  "s_waitcnt lgkmcnt(3)\n\t" \
  "v_mfma_f32_16x16x32_bf16 a[16:19], v[240:243], v[120:123], a[16:19]\n\t" \
  "ds_read_b128 v[224:227], v7 offset:1024\n\t" \
  "v_mul_f32 v80, v111, v80\n\t" \
  "s_waitcnt lgkmcnt(3)\n\t" \
  "v_mfma_f32_16x16x32_bf16 a[16:19], v[244:247], v[136:139], a[16:19]\n\t" \
  "ds_read_b128 v[228:231], v7 offset:6144\n\t" \
  "v_mul_f32 v81, v111, v81\n\t" \
  "v_mfma_f32_16x16x32_bf16 a[36:39], v[240:243], v[124:127], a[36:39]\n\t" \
  "v_mul_f32 v82, v111, v82\n\t" \
  "v_mfma_f32_16x16x32_bf16 a[36:39], v[244:247], v[140:143], a[36:39]\n\t" \
  "s_add_u32 s56, s60, s55\n\t" \
  "buffer_load_dwordx4 v[212:215], v248, s[44:47], s56 offen sc1\n\t" \
  "v_mul_f32 v83, v111, v83\n\t" \
  "v_mfma_f32_16x16x32_bf16 a[56:59], v[240:243], v[128:131], a[56:59]\n\t" \
  "v_mul_f32 v84, v111, v84\n\t" \
  "v_mfma_f32_16x16x32_bf16 a[56:59], v[244:247], v[144:147], a[56:59]\n\t" \
  "v_mul_f32 v85, v111, v85\n\t" \
  "v_mfma_f32_16x16x32_bf16 a[76:79], v[240:243], v[132:135], a[76:79]\n\t" \
  "v_mul_f32 v86, v111, v86\n\t" \
  "v_mfma_f32_16x16x32_bf16 a[76:79], v[244:247], v[148:151], a[76:79]\n\t" \
  "v_mul_f32 v87, v111, v87\n\t" \
  "s_waitcnt lgkmcnt(3)\n\t" \
  "v_mfma_f32_16x16x32_bf16 a[0:3], v[216:219], v[152:155], a[0:3]\n\t" \
  "ds_read_b128 v[232:235], v7 offset:2048\n\t" \
  "v_lshlrev_b32 v98, 16, v88\n\t" \
  "s_waitcnt lgkmcnt(3)\n\t" \
  "v_mfma_f32_16x16x32_bf16 a[0:3], v[220:223], v[168:171], a[0:3]\n\t" \
  "ds_read_b128 v[236:239], v7 offset:7168\n\t" \
  "v_and_b32 v99, 0xffff0000, v88\n\t" \
  "v_mfma_f32_16x16x32_bf16 a[20:23], v[216:219], v[156:159], a[20:23]\n\t" \
  "v_fmac_f32 v8, v104, v98\n\t" \
  "v_mfma_f32_16x16x32_bf16 a[20:23], v[220:223], v[172:175], a[20:23]\n\t" \
  "v_fmac_f32 v9, v104, v99\n\t" \
  "v_mfma_f32_16x16x32_bf16 a[40:43], v[216:219], v[160:163], a[40:43]\n\t" \
  "v_lshlrev_b32 v100, 16, v89\n\t" \
  "v_mfma_f32_16x16x32_bf16 a[40:43], v[220:223], v[176:179], a[40:43]\n\t" \
  "v_and_b32 v101, 0xffff0000, v89\n\t" \
  "v_mfma_f32_16x16x32_bf16 a[60:63], v[216:219], v[164:167], a[60:63]\n\t" \
  "v_fmac_f32 v10, v104, v100\n\t" \
  "s_waitcnt vmcnt(8)\n\t" \
  "v_mfma_f32_16x16x32_bf16 a[60:63], v[220:223], v[180:183], a[60:63]\n\t" \
  "v_fmac_f32 v11, v104, v101\n\t" \
  "s_waitcnt lgkmcnt(3)\n\t" \
  "v_mfma_f32_16x16x32_bf16 a[4:7], v[224:227], v[152:155], a[4:7]\n\t" \
  "ds_read_b128 v[240:243], v7 offset:3072\n\t" \
  "v_lshlrev_b32 v98, 16, v90\n\t" \
  "s_waitcnt lgkmcnt(3)\n\t" \
  "v_mfma_f32_16x16x32_bf16 a[4:7], v[228:231], v[168:171], a[4:7]\n\t" \
  "ds_read_b128 v[244:247], v7 offset:8192\n\t" \
  "v_and_b32 v99, 0xffff0000, v90\n\t" \
  "v_mfma_f32_16x16x32_bf16 a[24:27], v[224:227], v[156:159], a[24:27]\n\t" \
  "v_fmac_f32 v12, v104, v98\n\t" \
  "v_mfma_f32_16x16x32_bf16 a[24:27], v[228:231], v[172:175], a[24:27]\n\t" \
  "v_fmac_f32 v13, v104, v99\n\t" \
  "v_mfma_f32_16x16x32_bf16 a[44:47], v[224:227], v[160:163], a[44:47]\n\t" \
  "v_lshlrev_b32 v100, 16, v91\n\t" \
  "v_mfma_f32_16x16x32_bf16 a[44:47], v[228:231], v[176:179], a[44:47]\n\t" \
  "v_and_b32 v101, 0xffff0000, v91\n\t" \
  "v_mfma_f32_16x16x32_bf16 a[64:67], v[224:227], v[164:167], a[64:67]\n\t" \
  "v_fmac_f32 v14, v104, v100\n\t" \
  "v_mfma_f32_16x16x32_bf16 a[64:67], v[228:231], v[180:183], a[64:67]\n\t" \
  "v_fmac_f32 v15, v104, v101\n\t" \
  "s_waitcnt lgkmcnt(3)\n\t" \
  "v_mfma_f32_16x16x32_bf16 a[8:11], v[232:235], v[152:155], a[8:11]\n\t" \
  "ds_read_b128 v[216:219], v7 offset:4096\n\t" \
  "v_lshlrev_b32 v98, 16, v92\n\t" \
  "s_waitcnt lgkmcnt(3)\n\t" \
  "v_mfma_f32_16x16x32_bf16 a[8:11], v[236:239], v[168:171], a[8:11]\n\t" \
  "ds_read_b128 v[220:223], v7 offset:9216\n\t" \
  "v_and_b32 v99, 0xffff0000, v92\n\t" \
  "v_mfma_f32_16x16x32_bf16 a[28:31], v[232:235], v[156:159], a[28:31]\n\t" \
  "v_fmac_f32 v16, v104, v98\n\t" \
  "v_mfma_f32_16x16x32_bf16 a[28:31], v[236:239], v[172:175], a[28:31]\n\t" \
  "v_fmac_f32 v17, v104, v99\n\t" \
  "v_mfma_f32_16x16x32_bf16 a[48:51], v[232:235], v[160:163], a[48:51]\n\t" \
  "v_lshlrev_b32 v100, 16, v93\n\t" \
  "v_mfma_f32_16x16x32_bf16 a[48:51], v[236:239], v[176:179], a[48:51]\n\t" \
  "v_and_b32 v101, 0xffff0000, v93\n\t" \
  "v_mfma_f32_16x16x32_bf16 a[68:71], v[232:235], v[164:167], a[68:71]\n\t" \
  "v_fmac_f32 v18, v104, v100\n\t" \
  "v_mfma_f32_16x16x32_bf16 a[68:71], v[236:239], v[180:183], a[68:71]\n\t" \
  "v_fmac_f32 v19, v104, v101\n\t" \
  "s_waitcnt lgkmcnt(3)\n\t" \
  "v_mfma_f32_16x16x32_bf16 a[12:15], v[240:243], v[152:155], a[12:15]\n\t" \
  "ds_read_b128 v[224:227], v7 offset:10240\n\t" \
  "v_lshlrev_b32 v98, 16, v94\n\t" \
  "s_waitcnt lgkmcnt(3)\n\t" \
  "v_mfma_f32_16x16x32_bf16 a[12:15], v[244:247], v[168:171], a[12:15]\n\t" \
  "ds_read_b128 v[228:231], v7 offset:15360\n\t" \
  "v_and_b32 v99, 0xffff0000, v94\n\t" \
  "v_mfma_f32_16x16x32_bf16 a[32:35], v[240:243], v[156:159], a[32:35]\n\t" \
  "v_fmac_f32 v20, v104, v98\n\t" \
  "v_mfma_f32_16x16x32_bf16 a[32:35], v[244:247], v[172:175], a[32:35]\n\t" \
  "v_fmac_f32 v21, v104, v99\n\t" \
  "v_mfma_f32_16x16x32_bf16 a[52:55], v[240:243], v[160:163], a[52:55]\n\t" \
  "v_lshlrev_b32 v100, 16, v95\n\t" \
  "v_mfma_f32_16x16x32_bf16 a[52:55], v[244:247], v[176:179], a[52:55]\n\t" \
  "v_and_b32 v101, 0xffff0000, v95\n\t" \
  "v_mfma_f32_16x16x32_bf16 a[72:75], v[240:243], v[164:167], a[72:75]\n\t" \
  "v_fmac_f32 v22, v104, v100\n\t" \
  "v_mfma_f32_16x16x32_bf16 a[72:75], v[244:247], v[180:183], a[72:75]\n\t" \
  "v_fmac_f32 v23, v104, v101\n\t" \
  "s_waitcnt lgkmcnt(3)\n\t" \
  "v_mfma_f32_16x16x32_bf16 a[16:19], v[216:219], v[152:155], a[16:19]\n\t" \
  "ds_read_b128 v[232:235], v7 offset:11264\n\t" \
  "v_lshlrev_b32 v98, 16, v96\n\t" \
  "s_waitcnt lgkmcnt(3)\n\t" \
  "v_mfma_f32_16x16x32_bf16 a[16:19], v[220:223], v[168:171], a[16:19]\n\t" \
  "ds_read_b128 v[236:239], v7 offset:16384\n\t" \
  "v_and_b32 v99, 0xffff0000, v96\n\t" \
  "v_mfma_f32_16x16x32_bf16 a[36:39], v[216:219], v[156:159], a[36:39]\n\t" \
  "v_fmac_f32 v24, v104, v98\n\t" \
  "v_mfma_f32_16x16x32_bf16 a[36:39], v[220:223], v[172:175], a[36:39]\n\t" \
  "v_fmac_f32 v25, v104, v99\n\t" \
  "v_mfma_f32_16x16x32_bf16 a[56:59], v[216:219], v[160:163], a[56:59]\n\t" \
  "v_lshlrev_b32 v100, 16, v97\n\t" \
  "v_mfma_f32_16x16x32_bf16 a[56:59], v[220:223], v[176:179], a[56:59]\n\t" \
  "v_and_b32 v101, 0xffff0000, v97\n\t" \
  "v_mfma_f32_16x16x32_bf16 a[76:79], v[216:219], v[164:167], a[76:79]\n\t" \
  "v_fmac_f32 v26, v104, v100\n\t" \
  "v_mfma_f32_16x16x32_bf16 a[76:79], v[220:223], v[180:183], a[76:79]\n\t" \
  "v_fmac_f32 v27, v104, v101\n\t" \
  "s_waitcnt vmcnt(7) lgkmcnt(3)\n\t" \
  "v_mfma_f32_16x16x32_bf16 a[0:3], v[224:227], v[184:187], a[0:3]\n\t" \
  "ds_read_b128 v[240:243], v7 offset:12288\n\t" \
  "s_waitcnt vmcnt(3) lgkmcnt(3)\n\t" \
  "v_mfma_f32_16x16x32_bf16 a[0:3], v[228:231], v[200:203], a[0:3]\n\t" \
  "ds_read_b128 v[244:247], v7 offset:17408\n\t" \
  "v_mfma_f32_16x16x32_bf16 a[20:23], v[224:227], v[188:191], a[20:23]\n\t" \
  "s_waitcnt vmcnt(2)\n\t" \
  "v_mfma_f32_16x16x32_bf16 a[20:23], v[228:231], v[204:207], a[20:23]\n\t" \
  "v_mfma_f32_16x16x32_bf16 a[40:43], v[224:227], v[192:195], a[40:43]\n\t" \
  "s_waitcnt vmcnt(1)\n\t" \
  "v_mfma_f32_16x16x32_bf16 a[40:43], v[228:231], v[208:211], a[40:43]\n\t" \
  "v_mfma_f32_16x16x32_bf16 a[60:63], v[224:227], v[196:199], a[60:63]\n\t" \
  "s_waitcnt vmcnt(0)\n\t" \
  "v_mfma_f32_16x16x32_bf16 a[60:63], v[228:231], v[212:215], a[60:63]\n\t" \
  "s_waitcnt lgkmcnt(3)\n\t" \
  "v_mfma_f32_16x16x32_bf16 a[4:7], v[232:235], v[184:187], a[4:7]\n\t" \
  "ds_read_b128 v[216:219], v7 offset:13312\n\t" \
  "s_waitcnt lgkmcnt(3)\n\t" \
  "v_mfma_f32_16x16x32_bf16 a[4:7], v[236:239], v[200:203], a[4:7]\n\t" \
  "ds_read_b128 v[220:223], v7 offset:18432\n\t" \
  "v_accvgpr_read_b32 v112, a0\n\t" \
  "v_accvgpr_read_b32 v113, a1\n\t" \
  "v_accvgpr_read_b32 v114, a2\n\t" \
  "v_accvgpr_read_b32 v115, a3\n\t" \
  "v_fmac_f32 v8, v104, v112\n\t" \
  "v_mfma_f32_16x16x32_bf16 a[24:27], v[232:235], v[188:191], a[24:27]\n\t" \
  "v_fmac_f32 v9, v104, v113\n\t" \
  "v_fmac_f32 v10, v104, v114\n\t" \
  "v_fmac_f32 v11, v104, v115\n\t" \
  "v_accvgpr_read_b32 v116, a20\n\t" \
  "v_accvgpr_read_b32 v117, a21\n\t" \
  "v_mfma_f32_16x16x32_bf16 a[24:27], v[236:239], v[204:207], a[24:27]\n\t" \
  "v_accvgpr_read_b32 v118, a22\n\t" \
  "v_accvgpr_read_b32 v119, a23\n\t" \
  "v_fmac_f32 v28, v106, v116\n\t" \
  "v_fmac_f32 v29, v106, v117\n\t" \
  "v_fmac_f32 v30, v106, v118\n\t" \
  "v_mfma_f32_16x16x32_bf16 a[44:47], v[232:235], v[192:195], a[44:47]\n\t" \
  "v_fmac_f32 v31, v106, v119\n\t" \
  "ds_write_b128 v249, v[28:31] offset:0\n\t" \
  "v_accvgpr_read_b32 v112, a40\n\t" \
  "v_accvgpr_read_b32 v113, a41\n\t" \
  "v_accvgpr_read_b32 v114, a42\n\t" \
  "v_mfma_f32_16x16x32_bf16 a[44:47], v[236:239], v[208:211], a[44:47]\n\t" \
  "v_accvgpr_read_b32 v115, a43\n\t" \
  "v_fmac_f32 v48, v108, v112\n\t" \
  "v_fmac_f32 v49, v108, v113\n\t" \
  "v_fmac_f32 v50, v108, v114\n\t" \
  "v_fmac_f32 v51, v108, v115\n\t" \
  "v_mfma_f32_16x16x32_bf16 a[64:67], v[232:235], v[196:199], a[64:67]\n\t" \
  "ds_write_b128 v249, v[48:51] offset:5120\n\t" \
  "v_accvgpr_read_b32 v116, a60\n\t" \
  "v_accvgpr_read_b32 v117, a61\n\t" \
  "v_accvgpr_read_b32 v118, a62\n\t" \
  "v_accvgpr_read_b32 v119, a63\n\t" \
  "v_mfma_f32_16x16x32_bf16 a[64:67], v[236:239], v[212:215], a[64:67]\n\t" \
  "v_fmac_f32 v68, v110, v116\n\t" \
  "v_fmac_f32 v69, v110, v117\n\t" \
  "v_fmac_f32 v70, v110, v118\n\t" \
  "v_fmac_f32 v71, v110, v119\n\t" \
  "ds_write_b128 v249, v[68:71] offset:10240\n\t" \
  "s_waitcnt lgkmcnt(6)\n\t" \
  "v_mfma_f32_16x16x32_bf16 a[8:11], v[240:243], v[184:187], a[8:11]\n\t" \
  "ds_read_b128 v[224:227], v7 offset:14336\n\t" \
  "s_waitcnt lgkmcnt(6)\n\t" \
  "v_mfma_f32_16x16x32_bf16 a[8:11], v[244:247], v[200:203], a[8:11]\n\t" \
  "ds_read_b128 v[228:231], v7 offset:19456\n\t" \
  "v_accvgpr_read_b32 v112, a4\n\t" \
  "v_accvgpr_read_b32 v113, a5\n\t" \
  "v_accvgpr_read_b32 v114, a6\n\t" \
  "v_accvgpr_read_b32 v115, a7\n\t" \
  "v_fmac_f32 v12, v104, v112\n\t" \
  "v_mfma_f32_16x16x32_bf16 a[28:31], v[240:243], v[188:191], a[28:31]\n\t" \
  "v_fmac_f32 v13, v104, v113\n\t" \
  "v_fmac_f32 v14, v104, v114\n\t" \
  "v_fmac_f32 v15, v104, v115\n\t" \
  "v_accvgpr_read_b32 v116, a24\n\t" \
  "v_accvgpr_read_b32 v117, a25\n\t" \
  "v_mfma_f32_16x16x32_bf16 a[28:31], v[244:247], v[204:207], a[28:31]\n\t" \
  "v_accvgpr_read_b32 v118, a26\n\t" \
  "v_accvgpr_read_b32 v119, a27\n\t" \
  "v_fmac_f32 v32, v106, v116\n\t" \
  "v_fmac_f32 v33, v106, v117\n\t" \
  "v_fmac_f32 v34, v106, v118\n\t" \
  "v_mfma_f32_16x16x32_bf16 a[48:51], v[240:243], v[192:195], a[48:51]\n\t" \
  "v_fmac_f32 v35, v106, v119\n\t" \
  "ds_write_b128 v249, v[32:35] offset:1024\n\t" \
  "v_accvgpr_read_b32 v112, a44\n\t" \
  "v_accvgpr_read_b32 v113, a45\n\t" \
  "v_accvgpr_read_b32 v114, a46\n\t" \
  "v_mfma_f32_16x16x32_bf16 a[48:51], v[244:247], v[208:211], a[48:51]\n\t" \
  "v_accvgpr_read_b32 v115, a47\n\t" \
  "v_fmac_f32 v52, v108, v112\n\t" \
  "v_fmac_f32 v53, v108, v113\n\t" \
  "v_fmac_f32 v54, v108, v114\n\t" \
  "v_fmac_f32 v55, v108, v115\n\t" \
  "v_mfma_f32_16x16x32_bf16 a[68:71], v[240:243], v[196:199], a[68:71]\n\t" \
  "ds_write_b128 v249, v[52:55] offset:6144\n\t" \
  "v_accvgpr_read_b32 v116, a64\n\t" \
  "v_accvgpr_read_b32 v117, a65\n\t" \
  "v_accvgpr_read_b32 v118, a66\n\t" \
  "v_accvgpr_read_b32 v119, a67\n\t" \
  "v_mfma_f32_16x16x32_bf16 a[68:71], v[244:247], v[212:215], a[68:71]\n\t" \
  "v_fmac_f32 v72, v110, v116\n\t" \
  "v_fmac_f32 v73, v110, v117\n\t" \
  "v_fmac_f32 v74, v110, v118\n\t" \
  "v_fmac_f32 v75, v110, v119\n\t" \
  "ds_write_b128 v249, v[72:75] offset:11264\n\t" \
  "s_waitcnt lgkmcnt(9)\n\t" \
  "v_mfma_f32_16x16x32_bf16 a[12:15], v[216:219], v[184:187], a[12:15]\n\t" \
  "s_waitcnt lgkmcnt(8)\n\t" \
  "v_mfma_f32_16x16x32_bf16 a[12:15], v[220:223], v[200:203], a[12:15]\n\t" \
  "v_accvgpr_read_b32 v112, a8\n\t" \
  "v_accvgpr_read_b32 v113, a9\n\t" \
  "v_accvgpr_read_b32 v114, a10\n\t" \
  "v_accvgpr_read_b32 v115, a11\n\t" \
  "v_fmac_f32 v16, v104, v112\n\t" \
  "v_mfma_f32_16x16x32_bf16 a[32:35], v[216:219], v[188:191], a[32:35]\n\t" \
  "v_fmac_f32 v17, v104, v113\n\t" \
  "v_fmac_f32 v18, v104, v114\n\t" \
  "v_fmac_f32 v19, v104, v115\n\t" \
  "v_accvgpr_read_b32 v116, a28\n\t" \
  "v_accvgpr_read_b32 v117, a29\n\t" \
  "v_mfma_f32_16x16x32_bf16 a[32:35], v[220:223], v[204:207], a[32:35]\n\t" \
  "v_accvgpr_read_b32 v118, a30\n\t" \
  "v_accvgpr_read_b32 v119, a31\n\t" \
  "v_fmac_f32 v36, v106, v116\n\t" \
  "v_fmac_f32 v37, v106, v117\n\t" \
  "v_fmac_f32 v38, v106, v118\n\t" \
  "v_mfma_f32_16x16x32_bf16 a[52:55], v[216:219], v[192:195], a[52:55]\n\t" \
  "v_fmac_f32 v39, v106, v119\n\t" \
  "ds_write_b128 v249, v[36:39] offset:2048\n\t" \
  "v_accvgpr_read_b32 v112, a48\n\t" \
  "v_accvgpr_read_b32 v113, a49\n\t" \
  "v_accvgpr_read_b32 v114, a50\n\t" \
  "v_mfma_f32_16x16x32_bf16 a[52:55], v[220:223], v[208:211], a[52:55]\n\t" \
  "v_accvgpr_read_b32 v115, a51\n\t" \
  "v_fmac_f32 v56, v108, v112\n\t" \
  "v_fmac_f32 v57, v108, v113\n\t" \
  "v_fmac_f32 v58, v108, v114\n\t" \
  "v_fmac_f32 v59, v108, v115\n\t" \
  "v_mfma_f32_16x16x32_bf16 a[72:75], v[216:219], v[196:199], a[72:75]\n\t" \
  "ds_write_b128 v249, v[56:59] offset:7168\n\t" \
  "v_accvgpr_read_b32 v116, a68\n\t" \
  "v_accvgpr_read_b32 v117, a69\n\t" \
  "v_accvgpr_read_b32 v118, a70\n\t" \
  "v_accvgpr_read_b32 v119, a71\n\t" \
  "v_mfma_f32_16x16x32_bf16 a[72:75], v[220:223], v[212:215], a[72:75]\n\t" \
  "v_fmac_f32 v76, v110, v116\n\t" \
  "v_fmac_f32 v77, v110, v117\n\t" \
  "v_fmac_f32 v78, v110, v118\n\t" \
  "v_fmac_f32 v79, v110, v119\n\t" \
  "ds_write_b128 v249, v[76:79] offset:12288\n\t" \
  "s_waitcnt lgkmcnt(7)\n\t" \
  "v_mfma_f32_16x16x32_bf16 a[16:19], v[224:227], v[184:187], a[16:19]\n\t" \
  "s_waitcnt lgkmcnt(6)\n\t" \
  "v_mfma_f32_16x16x32_bf16 a[16:19], v[228:231], v[200:203], a[16:19]\n\t" \
  "v_accvgpr_read_b32 v112, a12\n\t" \
  "v_accvgpr_read_b32 v113, a13\n\t" \
  "v_accvgpr_read_b32 v114, a14\n\t" \
  "v_accvgpr_read_b32 v115, a15\n\t" \
  "v_fmac_f32 v20, v104, v112\n\t" \
  "v_mfma_f32_16x16x32_bf16 a[36:39], v[224:227], v[188:191], a[36:39]\n\t" \
  "v_fmac_f32 v21, v104, v113\n\t" \
  "v_fmac_f32 v22, v104, v114\n\t" \
  "v_fmac_f32 v23, v104, v115\n\t" \
  "v_accvgpr_read_b32 v116, a32\n\t" \
  "v_accvgpr_read_b32 v117, a33\n\t" \
  "v_mfma_f32_16x16x32_bf16 a[36:39], v[228:231], v[204:207], a[36:39]\n\t" \
  "v_accvgpr_read_b32 v118, a34\n\t" \
  "v_accvgpr_read_b32 v119, a35\n\t" \
  "v_fmac_f32 v40, v106, v116\n\t" \
  "v_fmac_f32 v41, v106, v117\n\t" \
  "v_fmac_f32 v42, v106, v118\n\t" \
  "v_mfma_f32_16x16x32_bf16 a[56:59], v[224:227], v[192:195], a[56:59]\n\t" \
  "v_fmac_f32 v43, v106, v119\n\t" \
  "ds_write_b128 v249, v[40:43] offset:3072\n\t" \
  "v_accvgpr_read_b32 v112, a52\n\t" \
  "v_accvgpr_read_b32 v113, a53\n\t" \
  "v_accvgpr_read_b32 v114, a54\n\t" \
  "v_mfma_f32_16x16x32_bf16 a[56:59], v[228:231], v[208:211], a[56:59]\n\t" \
  "v_accvgpr_read_b32 v115, a55\n\t" \
  "v_fmac_f32 v60, v108, v112\n\t" \
  "v_fmac_f32 v61, v108, v113\n\t" \
  "v_fmac_f32 v62, v108, v114\n\t" \
  "v_fmac_f32 v63, v108, v115\n\t" \
  "v_mfma_f32_16x16x32_bf16 a[76:79], v[224:227], v[196:199], a[76:79]\n\t" \
  "ds_write_b128 v249, v[60:63] offset:8192\n\t" \
  "v_accvgpr_read_b32 v116, a72\n\t" \
  "v_accvgpr_read_b32 v117, a73\n\t" \
  "v_accvgpr_read_b32 v118, a74\n\t" \
  "v_accvgpr_read_b32 v119, a75\n\t" \
  "v_mfma_f32_16x16x32_bf16 a[76:79], v[228:231], v[212:215], a[76:79]\n\t" \
  "v_fmac_f32 v80, v110, v116\n\t" \
  "v_fmac_f32 v81, v110, v117\n\t" \
  "v_fmac_f32 v82, v110, v118\n\t" \
  "v_fmac_f32 v83, v110, v119\n\t" \
  "ds_write_b128 v249, v[80:83] offset:13312\n\t" \
  "s_nop 4\n\t" \
  "v_accvgpr_read_b32 v112, a16\n\t" \
  "v_accvgpr_read_b32 v113, a17\n\t" \
  "v_accvgpr_read_b32 v114, a18\n\t" \
  "v_accvgpr_read_b32 v115, a19\n\t" \
  "v_fmac_f32 v24, v104, v112\n\t" \
  "v_fmac_f32 v25, v104, v113\n\t" \
  "v_fmac_f32 v26, v104, v114\n\t" \
  "v_fmac_f32 v27, v104, v115\n\t" \
  "v_accvgpr_read_b32 v116, a36\n\t" \
  "v_accvgpr_read_b32 v117, a37\n\t" \
  "v_accvgpr_read_b32 v118, a38\n\t" \
  "v_accvgpr_read_b32 v119, a39\n\t" \
  "v_fmac_f32 v44, v106, v116\n\t" \
  "v_fmac_f32 v45, v106, v117\n\t" \
  "v_fmac_f32 v46, v106, v118\n\t" \
  "v_fmac_f32 v47, v106, v119\n\t" \
  "ds_write_b128 v249, v[44:47] offset:4096\n\t" \
  "v_accvgpr_read_b32 v112, a56\n\t" \
  "v_accvgpr_read_b32 v113, a57\n\t" \
  "v_accvgpr_read_b32 v114, a58\n\t" \
  "v_accvgpr_read_b32 v115, a59\n\t" \
  "v_fmac_f32 v64, v108, v112\n\t" \
  "v_fmac_f32 v65, v108, v113\n\t" \
  "v_fmac_f32 v66, v108, v114\n\t" \
  "v_fmac_f32 v67, v108, v115\n\t" \
  "ds_write_b128 v249, v[64:67] offset:9216\n\t" \
  "v_accvgpr_read_b32 v116, a76\n\t" \
  "v_accvgpr_read_b32 v117, a77\n\t" \
  "v_accvgpr_read_b32 v118, a78\n\t" \
  "v_accvgpr_read_b32 v119, a79\n\t" \
  "v_fmac_f32 v84, v110, v116\n\t" \
  "v_fmac_f32 v85, v110, v117\n\t" \
  "v_fmac_f32 v86, v110, v118\n\t" \
  "v_fmac_f32 v87, v110, v119\n\t" \
  "ds_write_b128 v249, v[84:87] offset:14336\n\t" \
  "s_waitcnt lgkmcnt(0)\n\t" \
  "s_barrier\n\t" \
  "ds_read_b128 v[152:155], v250 offset:0\n\t" \
  "ds_read_b128 v[156:159], v251 offset:0\n\t" \
  "ds_read_b128 v[160:163], v254 offset:0\n\t" \
  "ds_read_b128 v[164:167], v250 offset:1024\n\t" \
  "ds_read_b128 v[168:171], v251 offset:1024\n\t" \
  "ds_read_b128 v[172:175], v254 offset:1024\n\t" \
  "ds_read_b128 v[176:179], v250 offset:2048\n\t" \
  "ds_read_b128 v[180:183], v251 offset:2048\n\t" \
  "ds_read_b128 v[184:187], v254 offset:2048\n\t" \
  "ds_read_b128 v[188:191], v250 offset:3072\n\t" \
  "ds_read_b128 v[192:195], v251 offset:3072\n\t" \
  "ds_read_b128 v[196:199], v254 offset:3072\n\t" \
  "ds_read_b128 v[200:203], v250 offset:4096\n\t" \
  "ds_read_b128 v[204:207], v251 offset:4096\n\t" \
  "ds_read_b128 v[208:211], v254 offset:4096\n\t" \
  "s_waitcnt lgkmcnt(14)\n\t" \
  "v_pk_add_f32 v[8:9], v[8:9], v[152:153]\n\t" \
  "v_pk_add_f32 v[10:11], v[10:11], v[154:155]\n\t" \
  "s_waitcnt lgkmcnt(12)\n\t" \
  "v_pk_add_f32 v[156:157], v[156:157], v[160:161]\n\t" \
  "v_pk_add_f32 v[158:159], v[158:159], v[162:163]\n\t" \
  "v_pk_add_f32 v[8:9], v[8:9], v[156:157]\n\t" \
  "v_pk_add_f32 v[10:11], v[10:11], v[158:159]\n\t" \
  "global_store_dwordx4 v253, v[8:11], s[88:89] offset:0 sc0 sc1\n\t" \
  "s_waitcnt lgkmcnt(11)\n\t" \
  "v_pk_add_f32 v[12:13], v[12:13], v[164:165]\n\t" \
  "v_pk_add_f32 v[14:15], v[14:15], v[166:167]\n\t" \
  "s_waitcnt lgkmcnt(9)\n\t" \
  "v_pk_add_f32 v[168:169], v[168:169], v[172:173]\n\t" \
  "v_pk_add_f32 v[170:171], v[170:171], v[174:175]\n\t" \
  "v_pk_add_f32 v[12:13], v[12:13], v[168:169]\n\t" \
  "v_pk_add_f32 v[14:15], v[14:15], v[170:171]\n\t" \
  "global_store_dwordx4 v253, v[12:15], s[88:89] offset:64 sc0 sc1\n\t" \
  "s_waitcnt lgkmcnt(8)\n\t" \
  "v_pk_add_f32 v[16:17], v[16:17], v[176:177]\n\t" \
  "v_pk_add_f32 v[18:19], v[18:19], v[178:179]\n\t" \
  "s_waitcnt lgkmcnt(6)\n\t" \
  "v_pk_add_f32 v[180:181], v[180:181], v[184:185]\n\t" \
  "v_pk_add_f32 v[182:183], v[182:183], v[186:187]\n\t" \
  "v_pk_add_f32 v[16:17], v[16:17], v[180:181]\n\t" \
  "v_pk_add_f32 v[18:19], v[18:19], v[182:183]\n\t" \
  "global_store_dwordx4 v253, v[16:19], s[88:89] offset:128 sc0 sc1\n\t" \
  "s_waitcnt lgkmcnt(5)\n\t" \
  "v_pk_add_f32 v[20:21], v[20:21], v[188:189]\n\t" \
  "v_pk_add_f32 v[22:23], v[22:23], v[190:191]\n\t" \
  "s_waitcnt lgkmcnt(3)\n\t" \
  "v_pk_add_f32 v[192:193], v[192:193], v[196:197]\n\t" \
  "v_pk_add_f32 v[194:195], v[194:195], v[198:199]\n\t" \
  "v_pk_add_f32 v[20:21], v[20:21], v[192:193]\n\t" \
  "v_pk_add_f32 v[22:23], v[22:23], v[194:195]\n\t" \
  "global_store_dwordx4 v253, v[20:23], s[88:89] offset:192 sc0 sc1\n\t" \
  "s_waitcnt lgkmcnt(2)\n\t" \
  "v_pk_add_f32 v[24:25], v[24:25], v[200:201]\n\t" \
  "v_pk_add_f32 v[26:27], v[26:27], v[202:203]\n\t" \
  "s_waitcnt lgkmcnt(0)\n\t" \
  "v_pk_add_f32 v[204:205], v[204:205], v[208:209]\n\t" \
  "v_pk_add_f32 v[206:207], v[206:207], v[210:211]\n\t" \
  "v_pk_add_f32 v[24:25], v[24:25], v[204:205]\n\t" \
  "v_pk_add_f32 v[26:27], v[26:27], v[206:207]\n\t" \
  "global_store_dwordx4 v253, v[24:27], s[88:89] offset:256 sc0 sc1\n\t" \
  "s_nop 1\n\t"
#define P2_MAIN_CLOB "v7", "v8", "v9", "v10", "v11", "v12", "v13", "v14", "v15", "v16", "v17", "v18", "v19", "v20", "v21", "v22", "v23", "v24", "v25", "v26", "v27", "v28", "v29", "v30", "v31", "v32", "v33", "v34", "v35", "v36", "v37", "v38", "v39", "v40", "v41", "v42", "v43", "v44", "v45", "v46", "v47", "v48", "v49", "v50", "v51", "v52", "v53", "v54", "v55", "v56", "v57", "v58", "v59", "v60", "v61", "v62", "v63", "v64", "v65", "v66", "v67", "v68", "v69", "v70", "v71", "v72", "v73", "v74", "v75", "v76", "v77", "v78", "v79", "v80", "v81", "v82", "v83", "v84", "v85", "v86", "v87", "v88", "v89", "v90", "v91", "v92", "v93", "v94", "v95", "v96", "v97", "v98", "v99", "v100", "v101", "v102", "v103", "v104", "v105", "v106", "v107", "v108", "v109", "v110", "v111", "v112", "v113", "v114", "v115", "v116", "v117", "v118", "v119", "v120", "v121", "v122", "v123", "v124", "v125", "v126", "v127", "v128", "v129", "v130", "v131", "v132", "v133", "v134", "v135", "v136", "v137", "v138", "v139", "v140", "v141", "v142", "v143", "v144", "v145", "v146", "v147", "v148", "v149", "v150", "v151", "v152", "v153", "v154", "v155", "v156", "v157", "v158", "v159", "v160", "v161", "v162", "v163", "v164", "v165", "v166", "v167", "v168", "v169", "v170", "v171", "v172", "v173", "v174", "v175", "v176", "v177", "v178", "v179", "v180", "v181", "v182", "v183", "v184", "v185", "v186", "v187", "v188", "v189", "v190", "v191", "v192", "v193", "v194", "v195", "v196", "v197", "v198", "v199", "v200", "v201", "v202", "v203", "v204", "v205", "v206", "v207", "v208", "v209", "v210", "v211", "v212", "v213", "v214", "v215", "v216", "v217", "v218", "v219", "v220", "v221", "v222", "v223", "v224", "v225", "v226", "v227", "v228", "v229", "v230", "v231", "v232", "v233", "v234", "v235", "v236", "v237", "v238", "v239", "v240", "v241", "v242", "v243", "v244", "v245", "v246", "v247", "v248", "v249", "v250", "v251", "v252", "v253", "v254", "v255", "s44", "s45", "s46", "s47", "s48", "s49", "s50", "s51", "s52", "s53", "s54", "s55", "s56", "s57", "s58", "s59", "s60", "s61", "s62", "s63", "s64", "s65", "s66", "s67", "s68", "s69", "s70", "s71", "s72", "s73", "s75", "s76", "s77", "s78", "s79", "s80", "s81", "s82", "s83", "s84", "s85", "s86", "s87", "s88", "s89", "a0", "a1", "a2", "a3", "a4", "a5", "a6", "a7", "a8", "a9", "a10", "a11", "a12", "a13", "a14", "a15", "a16", "a17", "a18", "a19", "a20", "a21", "a22", "a23", "a24", "a25", "a26", "a27", "a28", "a29", "a30", "a31", "a32", "a33", "a34", "a35", "a36", "a37", "a38", "a39", "a40", "a41", "a42", "a43", "a44", "a45", "a46", "a47", "a48", "a49", "a50", "a51", "a52", "a53", "a54", "a55", "a56", "a57", "a58", "a59", "a60", "a61", "a62", "a63", "a64", "a65", "a66", "a67", "a68", "a69", "a70", "a71", "a72", "a73", "a74", "a75", "a76", "a77", "a78", "a79", "a80", "a81", "a82", "a83", "a84", "a85", "a86", "a87", "a88", "a89", "a90", "a91", "a92", "a93", "a94", "a95", "a96", "a97", "a98", "a99", "a100", "a101", "a102", "a103", "a104", "a105", "a106", "a107", "a108", "a109", "a110", "a111", "a112", "a113", "a114", "a115", "a116", "a117", "a118", "a119", "a120", "a121", "a122", "a123", "a124", "a125", "a126", "a127", "a128", "a129", "a130", "a131", "a132", "a133", "a134", "a135", "a136", "a137", "a138", "a139", "a140", "a141", "a142", "a143", "a144", "a145", "a146", "a147", "a148", "a149", "a150", "a151", "a152", "a153", "a154", "a155", "a156", "a157", "a158", "a159", "a160", "a161", "a162", "a163", "a164", "a165", "a166", "a167", "a168", "a169", "a170", "a171", "a172", "a173", "a174", "a175", "a176", "a177", "a178", "a179", "a180", "a181", "a182", "a183", "a184", "a185", "a186", "a187", "a188", "a189", "a190", "a191", "a192", "a193", "a194", "a195", "a196", "a197", "a198", "a199", "a200", "a201", "a202", "a203", "a204", "a205", "a206", "a207", "a208", "a209", "a210", "a211", "a212", "a213", "a214", "a215", "a216", "a217", "a218", "a219", "a220", "a221", "a222", "a223", "a224", "a225", "a226", "a227", "a228", "a229", "a230", "a231", "a232", "a233", "a234", "a235", "a236", "a237", "a238", "a239", "a240", "a241", "a242", "a243", "a244", "a245", "a246", "a247", "a248", "a249", "a250", "a251", "a252", "a253", "vcc", "scc", "memory"
#define WS_TOTAL 16531712

constexpr int NKS = 94;
constexpr unsigned H_BYTES = 32u * NKS * 4096u;
constexpr unsigned WS_WT = H_BYTES;
constexpr unsigned WS_FLAG = WS_WT + NKS * 40960;
constexpr unsigned WS_RF = WS_FLAG + 32 * 32 * 128;
constexpr unsigned WS_CARRY = WS_RF + 1024 * 128;
constexpr unsigned WS_G = WS_CARRY + 8 * 10 * 64 * 16;
constexpr unsigned WS_EPOCH = WS_G + 2048 * 8;
constexpr unsigned WS_DIAG = WS_EPOCH + 256;
constexpr unsigned WS_BYTES = WS_DIAG;
static_assert(WS_EPOCH == WS_TOTAL - 256, "epoch word");
static_assert(WS_BYTES == WS_TOTAL, "workspace map");
constexpr int LDS_BYTES = 163840;
#define MAP2 0
constexpr int CODE_BYTES = 53760;
#define RETOUCH_FROM 0
#define RETOUCH_MASK 7
#define KROT 4
constexpr unsigned SPIN_CAP = 1u << 18;

DI unsigned pk(float a, float b) { f2 v = {a, b}; return __builtin_bit_cast(unsigned, __builtin_convertvector(v, bf2)); }
DI float lo(unsigned p) { return __uint_as_float(p << 16); }
DI float hi(unsigned p) { return __uint_as_float(p & 0xffff0000u); }
DI float rbf(float x) { return lo(pk(x, x)); }
#define FMA2(a, b, c) __builtin_elementwise_fma((a), (b), (c))
DI unsigned RFL(unsigned x) { x = __builtin_amdgcn_readfirstlane(x); asm("" : "+s"(x)); return x; }
#define SP(x) ((f2){(x), (x)})
DI float sigm(float x) { return __builtin_amdgcn_rcpf(1.f + __builtin_amdgcn_exp2f(-1.44269504089f * x)); }

struct Args { const u16 *x, *lre, *lim, *bre, *bim, *cre, *cim, *ldt, *dsk, *w1, *w3, *w2, *gw; float* out; char* ws; };

__global__ __launch_bounds__(256, 1) void mix_kernel(Args A) {
  extern __shared__ __attribute__((aligned(16))) char smem[];
  asm volatile("mixk_entry:");
  const int b = blockIdx.x;
  const unsigned lb = (unsigned)(size_t)(LAS char*)smem;
  const int w0 = __builtin_amdgcn_readfirstlane(threadIdx.x >> 6);
  const auto ka = __builtin_amdgcn_kernarg_segment_ptr();
  unsigned tag, pv;
  u64 pws;
  #define START_OPS [ka]"s"(ka), [b]"s"(b), [w]"s"(w0), [lb]"s"(lb), [cb]"i"(CODE_BYTES)
  if (((b >> 3) & 7) >= 3 - (b & 1)) {
    asm volatile(P1_FFN : "=&{v2}"(pv), "=&{s74}"(tag), "=&{s[40:41]}"(pws) : START_OPS : P1_FFN_CLOB);
  } else {
    unsigned rlr, rli, rdt, v3; v4u v4; v16u va; v8u s36, s44; v4u s52; v16u s64, s80; v2u s96;
    #define CARRY(m) m "{v2}"(pv), m "{v3}"(v3), m "{v[4:7]}"(v4), m "{v[240:255]}"(va), m "{s[36:43]}"(s36), m "{s[44:51]}"(s44), m "{s[52:55]}"(s52), m "{s[64:79]}"(s64), m "{s[80:95]}"(s80), m "{s[96:97]}"(s96)
    asm volatile(P1_SSM_START : CARRY("=&"), "=&{v16}"(rlr), "=&{v17}"(rli), "=&{v18}"(rdt) : START_OPS : P1_START_CLOB);
    const unsigned tl = (v4[0] >> 4) & 15;
    float Lr1, Li1, cr1, ci1;
    { const float lr = lo(rlr), li = lo(rli), dt = rbf(expf(lo(rdt)));
      const float e = expf(dt * lr); float sn, cn; sincosf(dt * li, &sn, &cn);
      Lr1 = e * cn; Li1 = e * sn;
      const float nr = Lr1 - 1.f, dn = 2.f / (lr * lr + li * li);
      cr1 = (nr * lr + Li1 * li) * dn; ci1 = (Li1 * lr - nr * li) * dn; }
    v16u pp; v4u p1 = {__float_as_uint(Lr1), __float_as_uint(Li1), 0u, 0u};
    #pragma unroll
    for (int p = 0; p < 4; ++p) { const int s = (4 * tl + p) * 4;
      pp[2 * p] = __builtin_amdgcn_ds_bpermute(s, __float_as_int(Lr1)); pp[2 * p + 1] = __builtin_amdgcn_ds_bpermute(s, __float_as_int(Li1));
      pp[8 + 2 * p] = __builtin_amdgcn_ds_bpermute(s, __float_as_int(cr1)); pp[9 + 2 * p] = __builtin_amdgcn_ds_bpermute(s, __float_as_int(ci1)); }
    asm volatile(P1_SSM : CARRY("+"), "+{v[136:139]}"(p1), "+{v[140:155]}"(pp) :: P1_SSM_CLOB);
    tag = s64[10]; pws = ((u64)s36[5] << 32) | s36[4];
  }
  asm volatile(P2_MAIN :: "{v2}"(pv), "{s[40:41]}"(pws), "{s74}"(tag) : P2_MAIN_CLOB);
}

extern "C" void kernel_launch(void* const* d_in, const int* in_sizes, int n_in, void* d_out, int out_size, void* d_ws, size_t ws_size, hipStream_t stream) {
  Args A;
  A.x = (const u16*)d_in[0]; A.lre = (const u16*)d_in[1]; A.lim = (const u16*)d_in[2]; A.bre = (const u16*)d_in[3]; A.bim = (const u16*)d_in[4];
  A.cre = (const u16*)d_in[5]; A.cim = (const u16*)d_in[6]; A.ldt = (const u16*)d_in[7]; A.dsk = (const u16*)d_in[8];
  A.w1 = (const u16*)d_in[10]; A.w3 = (const u16*)d_in[11]; A.w2 = (const u16*)d_in[12]; A.gw = (const u16*)d_in[13];
  A.out = (float*)d_out; A.ws = (char*)d_ws;
  (void)hipFuncSetAttribute((const void*)mix_kernel, hipFuncAttributeMaxDynamicSharedMemorySize, LDS_BYTES);
  mix_kernel<<<256, 256, LDS_BYTES, stream>>>(A);
}
```
